```python
import torch

def _softmax(x, axis=-1):
    return torch.softmax(x, dim=axis)
def _gelu(x):
    return torch.nn.functional.gelu(x, approximate="tanh")
def _silu(x):
    return torch.nn.functional.silu(x)
def _sigmoid(x):
    return torch.sigmoid(x)
def _layernorm(x, axis=-1, eps=1e-5):
    m = x.mean(dim=axis, keepdim=True)
    v = x.var(dim=axis, keepdim=True, unbiased=False)
    return (x - m) / torch.sqrt(v + eps)
def _scatter_add(tgt, idx, src, axis=0):
    return tgt.clone().index_add_(axis, idx, src)
def _segment_sum(src, seg, n):
    out = torch.zeros((n,) + src.shape[1:], dtype=src.dtype)
    return out.index_add_(0, seg, src)
def _pad(x, pw):
    flat = tuple(v for pair in reversed(pw) for v in pair)
    return torch.nn.functional.pad(x, flat)

kernel_name = 'concat_heads__gqa_block_cached__gqa_block_cached__near_ridge'

def setup_inputs():

    _w = (lambda a: (lambda t: t.to(torch.bfloat16) if t.is_floating_point() else t)(torch.as_tensor(a)))
    def _A_setup():
        B, Sq, D = 8, 208, 640
        Hq, Hkv, Sk, Dh = 32, 32, 1024, 128
        assert Hq % Hkv == 0, (Hq, Hkv)
        return {
            "x": _w((lambda *s: torch.randn(*s, dtype=torch.bfloat16) * 0.8)(B, Sq, D)),
            "k_cache": _w((lambda *s: torch.randn(*s, dtype=torch.bfloat16) * 0.8)(B, Hkv, Sk, Dh)),
            "v_cache": _w((lambda *s: torch.randn(*s, dtype=torch.bfloat16) * 0.05)(B, Hkv, Sk, Dh)),
            "wq": _w((lambda *s: torch.randn(*s, dtype=torch.bfloat16) * 0.8)(D, Hq * Dh) * (D ** -0.5)),
            "wo": _w((lambda *s: torch.randn(*s, dtype=torch.bfloat16) * 0.05)(Hq * Dh, D) * ((Hq * Dh) ** -0.5)),
        }
    _A_in = _A_setup()

    def _B_setup():
        B, Sq, D = 8, 208, 640
        Hq, Hkv, Sk, Dh = 32, 32, 1024, 128
        assert Hq % Hkv == 0, (Hq, Hkv)
        return {
            "x": _w((lambda *s: torch.randn(*s, dtype=torch.bfloat16) * 0.8)(B, Sq, D)),
            "k_cache": _w((lambda *s: torch.randn(*s, dtype=torch.bfloat16) * 0.8)(B, Hkv, Sk, Dh)),
            "v_cache": _w((lambda *s: torch.randn(*s, dtype=torch.bfloat16) * 0.05)(B, Hkv, Sk, Dh)),
            "wq": _w((lambda *s: torch.randn(*s, dtype=torch.bfloat16) * 0.8)(D, Hq * Dh) * (D ** -0.5)),
            "wo": _w((lambda *s: torch.randn(*s, dtype=torch.bfloat16) * 0.05)(Hq * Dh, D) * ((Hq * Dh) ** -0.5)),
        }
    _B_in = _B_setup()

    return {
        "A_x": _A_in["x"],
        "A_k_cache": _A_in["k_cache"],
        "A_v_cache": _A_in["v_cache"],
        "A_wq": _A_in["wq"],
        "A_wo": _A_in["wo"],
        "B_x": _B_in["x"],
        "B_k_cache": _B_in["k_cache"],
        "B_v_cache": _B_in["v_cache"],
        "B_wq": _B_in["wq"],
        "B_wo": _B_in["wo"],
    }

def reference(A_x, A_k_cache, A_v_cache, A_wq, A_wo, B_x, B_k_cache, B_v_cache, B_wq, B_wo):
    def _A_ref(x, k_cache, v_cache, wq, wo):
        B, Sq, D = x.shape
        Hq, Dh = 32, 128
        Hkv = k_cache.shape[1]
        rep = Hq // Hkv
        q = (lambda x, *d: x.permute(*d))((x @ wq).reshape(B, Sq, Hq, Dh), 0, 2, 1, 3)
        k = (lambda x, n, axis: torch.repeat_interleave(x, n, dim=axis))(k_cache, rep, axis=1)
        v = (lambda x, n, axis: torch.repeat_interleave(x, n, dim=axis))(v_cache, rep, axis=1)
        scores = torch.einsum("bhqd,bhkd->bhqk", q, k) * (Dh ** -0.5)
        p = _softmax(scores, axis=-1)
        o = torch.einsum("bhqk,bhkd->bhqd", p, v)
        return (lambda x, *d: x.permute(*d))(o, 0, 2, 1, 3).reshape(B, Sq, Hq * Dh) @ wo

    def _B_ref(x, k_cache, v_cache, wq, wo):
        B, Sq, D = x.shape
        Hq, Dh = 32, 128
        Hkv = k_cache.shape[1]
        rep = Hq // Hkv
        q = (lambda x, *d: x.permute(*d))((x @ wq).reshape(B, Sq, Hq, Dh), 0, 2, 1, 3)
        k = (lambda x, n, axis: torch.repeat_interleave(x, n, dim=axis))(k_cache, rep, axis=1)
        v = (lambda x, n, axis: torch.repeat_interleave(x, n, dim=axis))(v_cache, rep, axis=1)
        scores = torch.einsum("bhqd,bhkd->bhqk", q, k) * (Dh ** -0.5)
        p = _softmax(scores, axis=-1)
        o = torch.einsum("bhqk,bhkd->bhqd", p, v)
        return (lambda x, *d: x.permute(*d))(o, 0, 2, 1, 3).reshape(B, Sq, Hq * Dh) @ wo

    a = _A_ref(A_x, A_k_cache, A_v_cache, A_wq, A_wo)
    b = _B_ref(B_x, B_k_cache, B_v_cache, B_wq, B_wo)
    return torch.concatenate([a, b], axis=-1)
```

```cpp
#include <hip/hip_runtime.h>
#include <hip/hip_bf16.h>

typedef __attribute__((ext_vector_type(8))) short bf16x8;
typedef __attribute__((ext_vector_type(4))) short s16x4;
typedef __attribute__((ext_vector_type(4))) float f32x4;
typedef __attribute__((ext_vector_type(16))) float f32x16;
typedef __attribute__((ext_vector_type(4))) unsigned u32x4;
typedef unsigned short u16;

constexpr int NB = 8, SQ = 208, DM = 640, H = 32, SK = 1024, DH = 128, HD = H * DH;
constexpr int MROWS = NB * SQ;

__device__ __forceinline__ unsigned cvtpk(float lo, float hi) {
  unsigned r; asm volatile("v_cvt_pk_bf16_f32 %0, %1, %2" : "=v"(r) : "v"(lo), "v"(hi)); return r;
}
__device__ __forceinline__ float bf2f(u16 x) { return __uint_as_float(((unsigned)x) << 16); }

__global__ __launch_bounds__(256) void gemm_simple(const u16* __restrict__ A0, const u16* __restrict__ B0, u16* __restrict__ C0,
                                                   const u16* __restrict__ A1, const u16* __restrict__ B1, u16* __restrict__ C1,
                                                   int K, int lda, int ldb, int ldc, float alpha) {
  const u16* A = blockIdx.z ? A1 : A0; const u16* B = blockIdx.z ? B1 : B0; u16* C = blockIdx.z ? C1 : C0;
  __shared__ __attribute__((aligned(16))) u16 As[64][40];
  __shared__ __attribute__((aligned(16))) u16 Bs[64][40];
  const int tid = threadIdx.x, lane = tid & 63, wid = tid >> 6;
  const int m0 = blockIdx.y * 64, n0 = blockIdx.x * 64;
  const int wr = wid >> 1, wc = wid & 1;
  f32x4 acc[2][2] = {};
  for (int k0 = 0; k0 < K; k0 += 32) {
    { int r = tid >> 2, c = (tid & 3) * 8;
      *(bf16x8*)&As[r][c] = *(const bf16x8*)&A[(size_t)(m0 + r) * lda + k0 + c]; }
    { int k = tid >> 3, c = (tid & 7) * 8;
      bf16x8 v = *(const bf16x8*)&B[(size_t)(k0 + k) * ldb + n0 + c];
      for (int j = 0; j < 8; ++j) Bs[c + j][k] = (u16)v[j]; }
    __syncthreads();
    bf16x8 a[2], b[2];
    for (int i = 0; i < 2; ++i) a[i] = *(const bf16x8*)&As[wr * 32 + i * 16 + (lane & 15)][(lane >> 4) * 8];
    for (int i = 0; i < 2; ++i) b[i] = *(const bf16x8*)&Bs[wc * 32 + i * 16 + (lane & 15)][(lane >> 4) * 8];
    for (int i = 0; i < 2; ++i) for (int j = 0; j < 2; ++j)
      acc[i][j] = __builtin_amdgcn_mfma_f32_16x16x32_bf16(a[i], b[j], acc[i][j], 0, 0, 0);
    __syncthreads();
  }
  for (int i = 0; i < 2; ++i) for (int j = 0; j < 2; ++j) for (int r = 0; r < 4; ++r) {
    int row = m0 + wr * 32 + i * 16 + (lane >> 4) * 4 + r, col = n0 + wc * 32 + j * 16 + (lane & 15);
    C[(size_t)row * ldc + col] = (u16)(cvtpk(acc[i][j][r] * alpha, 0.f) & 0xffff);
  }
}

__global__ __launch_bounds__(256) void attn_simple(const u16* __restrict__ Q, const u16* __restrict__ K0, const u16* __restrict__ V0,
                                                   const u16* __restrict__ K1, const u16* __restrict__ V1, u16* __restrict__ O) {
  const int br = blockIdx.z, bh = blockIdx.y, b = bh / H, h = bh % H;
  const u16* Kp = (br ? K1 : K0) + (size_t)bh * SK * DH; const u16* Vp = (br ? V1 : V0) + (size_t)bh * SK * DH;
  const u16* Qp = Q + (size_t)br * MROWS * HD; u16* Op = O + (size_t)br * MROWS * HD;
  __shared__ __attribute__((aligned(16))) u16 Ks[32][136];
  __shared__ __attribute__((aligned(16))) u16 Vt[128][40];
  const int tid = threadIdx.x, lane = tid & 63, wid = tid >> 6, g = lane >> 4, l15 = lane & 15;
  int qt = blockIdx.x * 4 + wid; bool live = qt < 13; if (!live) qt = 12;
  const int qrow = b * SQ + qt * 16 + l15;
  bf16x8 qf[4];
  for (int s = 0; s < 4; ++s) qf[s] = *(const bf16x8*)&Qp[(size_t)qrow * HD + h * DH + s * 32 + g * 8];
  f32x4 o[8] = {}; float m = -1e30f, l = 0.f;
  for (int k0 = 0; k0 < SK; k0 += 32) {
    __syncthreads();
    for (int i = 0; i < 2; ++i) { int idx = tid + 256 * i, r = idx >> 4, c = (idx & 15) * 8;
      *(bf16x8*)&Ks[r][c] = *(const bf16x8*)&Kp[(size_t)(k0 + r) * DH + c];
      bf16x8 v = *(const bf16x8*)&Vp[(size_t)(k0 + r) * DH + c];
      for (int j = 0; j < 8; ++j) Vt[c + j][r] = (u16)v[j]; }
    __syncthreads();
    f32x4 s[2] = {};
    for (int u = 0; u < 2; ++u) for (int ks = 0; ks < 4; ++ks) {
      bf16x8 kf = *(const bf16x8*)&Ks[u * 16 + l15][ks * 32 + g * 8];
      s[u] = __builtin_amdgcn_mfma_f32_16x16x32_bf16(kf, qf[ks], s[u], 0, 0, 0);
    }
    float mx = -1e30f;
    for (int u = 0; u < 2; ++u) for (int r = 0; r < 4; ++r) mx = fmaxf(mx, s[u][r]);
    mx = fmaxf(mx, __shfl_xor(mx, 16)); mx = fmaxf(mx, __shfl_xor(mx, 32));
    float mn = fmaxf(m, mx), al = exp2f(m - mn); m = mn;
    float ps = 0.f; float p[8];
    for (int u = 0; u < 2; ++u) for (int r = 0; r < 4; ++r) { p[u * 4 + r] = exp2f(s[u][r] - mn); ps += p[u * 4 + r]; }
    ps += __shfl_xor(ps, 16); ps += __shfl_xor(ps, 32);
    l = l * al + ps;
    u32x4 pw = {cvtpk(p[0], p[1]), cvtpk(p[2], p[3]), cvtpk(p[4], p[5]), cvtpk(p[6], p[7])};
    bf16x8 pf = *(bf16x8*)&pw;
    for (int c = 0; c < 8; ++c) {
      s16x4 lo = *(const s16x4*)&Vt[c * 16 + l15][4 * g], hi = *(const s16x4*)&Vt[c * 16 + l15][16 + 4 * g];
      bf16x8 vf = {lo[0], lo[1], lo[2], lo[3], hi[0], hi[1], hi[2], hi[3]};
      for (int r = 0; r < 4; ++r) o[c][r] *= al;
      o[c] = __builtin_amdgcn_mfma_f32_16x16x32_bf16(vf, pf, o[c], 0, 0, 0);
    }
  }
  if (live) {
    float il = 1.f / l;
    for (int c = 0; c < 8; ++c) {
      uint2 w = {cvtpk(o[c][0] * il, o[c][1] * il), cvtpk(o[c][2] * il, o[c][3] * il)};
      *(uint2*)&Op[(size_t)qrow * HD + h * DH + c * 16 + 4 * g] = w;
    }
  }
}

extern "C" void kernel_launch(void* const* d_in, const int* in_sizes, int n_in, void* d_out, int out_size, void* d_ws, size_t ws_size, hipStream_t stream) {
  const u16* xA = (const u16*)d_in[0]; const u16* kA = (const u16*)d_in[1]; const u16* vA = (const u16*)d_in[2];
  const u16* wqA = (const u16*)d_in[3]; const u16* woA = (const u16*)d_in[4];
  const u16* xB = (const u16*)d_in[5]; const u16* kB = (const u16*)d_in[6]; const u16* vB = (const u16*)d_in[7];
  const u16* wqB = (const u16*)d_in[8]; const u16* woB = (const u16*)d_in[9];
  u16* out = (u16*)d_out;
  u16* Q = (u16*)d_ws; u16* O = Q + (size_t)2 * MROWS * HD;
  const float c = 0.08838834764831845f * 1.4426950408889634f;
  gemm_simple<<<dim3(HD / 64, MROWS / 64, 2), 256, 0, stream>>>(xA, wqA, Q, xB, wqB, Q + (size_t)MROWS * HD, DM, DM, HD, HD, c);
  attn_simple<<<dim3(4, H * NB, 2), 256, 0, stream>>>(Q, kA, vA, kB, vB, O);
  gemm_simple<<<dim3(DM / 64, MROWS / 64, 2), 256, 0, stream>>>(O, woA, out, O + (size_t)MROWS * HD, woB, out + DM, HD, HD, DM, 2 * DM, 1.f);
}
```

```cpp
#include <hip/hip_runtime.h>
#include <hip/hip_bf16.h>
#include <utility>

namespace kk {
typedef unsigned short u16;
typedef short bf16x8 __attribute__((ext_vector_type(8)));
typedef short s16x4 __attribute__((ext_vector_type(4)));
typedef float f32x4 __attribute__((ext_vector_type(4)));
typedef unsigned u32x4 __attribute__((ext_vector_type(4)));
typedef unsigned u32x2 __attribute__((ext_vector_type(2)));
typedef __amdgpu_buffer_rsrc_t rsrc_t;
#define LAS __attribute__((address_space(3)))
#define DI __device__ __forceinline__
#define SBAR() __builtin_amdgcn_sched_barrier(0)
#define PIN(x) asm volatile("" : "+v"(x))
#define MFMA16(a, b, c) __builtin_amdgcn_mfma_f32_16x16x32_bf16(a, b, c, 0, 0, 0)
#define MFMA16K16(a, b, c) __builtin_amdgcn_mfma_f32_16x16x16bf16_1k(a, b, c, 0, 0, 0)
#define LGKM0() __builtin_amdgcn_s_waitcnt(0xC07F)
#define VMW(N) asm volatile("s_waitcnt vmcnt(" #N ")" ::: "memory")
#define BAR() do { LGKM0(); asm volatile("s_barrier" ::: "memory"); } while (0)
template <int... Is> using iseq = std::integer_sequence<int, Is...>;
template <int N> using mkseq = std::make_integer_sequence<int, N>;
template <int N> using ic = std::integral_constant<int, N>;

constexpr int NB = 8, SQ = 208, DM = 640, H = 32, SK = 1024, DH = 128, HD = H * DH, MROWS = NB * SQ, NRT = 13;
constexpr unsigned SLOT = 16384, XST = 26624, XOFF = 5 * SLOT, Q2OFF = 6 * SLOT, MISC = Q2OFF + NRT * 4096, LDS_TOTAL = 163840;
static_assert(XOFF + 3 * XST <= LDS_TOTAL && MISC + 3 * 4096 == LDS_TOTAL, "lds map");
constexpr unsigned OBLK = NRT * 4096;
constexpr float QSCALE = 0.08838834764831845f * 1.4426950408889634f;
constexpr float L_LIMIT = 1.0e24f;
__host__ __device__ constexpr unsigned q1addr(int rt) { return rt < 8 ? 4 * SLOT + 4096 * rt : rt < 11 ? MISC + 4096 * (rt - 8) : SLOT + 4096 * (rt - 11); }

typedef float f32x2 __attribute__((ext_vector_type(2)));
typedef __bf16 bf16x2 __attribute__((ext_vector_type(2)));
DI unsigned cvtpk(float lo, float hi) { f32x2 v = {lo, hi}; return __builtin_bit_cast(unsigned, __builtin_convertvector(v, bf16x2)); }
DI float fadd_s(float a, float b) { float r; asm("v_add_f32_e32 %0, %1, %2" : "=v"(r) : "v"(a), "v"(b)); return r; }
DI float ex2(float x) { return __builtin_amdgcn_exp2f(x); }
DI bf16x8 rd128(unsigned base, int imm) { return *(const LAS bf16x8*)((const LAS char*)base + imm); }
DI s16x4 rdtr(unsigned base, int imm) { return __builtin_amdgcn_ds_read_tr16_b64_v4i16((LAS s16x4*)((LAS char*)base + imm)); }
DI bf16x8 cat(s16x4 lo, s16x4 hi) { return __builtin_shufflevector(lo, hi, 0, 1, 2, 3, 4, 5, 6, 7); }
DI void wr128f(unsigned base, int imm, f32x4 v) { *(LAS f32x4*)((LAS char*)base + imm) = v; }
DI f32x4 rd128f(unsigned base, int imm) { return *(const LAS f32x4*)((const LAS char*)base + imm); }
DI float red_add(float x) {
  auto a = __builtin_amdgcn_permlane16_swap(__float_as_uint(x), __float_as_uint(x), false, false); x = __uint_as_float(a[0]) + __uint_as_float(a[1]);
  auto b = __builtin_amdgcn_permlane32_swap(__float_as_uint(x), __float_as_uint(x), false, false); return __uint_as_float(b[0]) + __uint_as_float(b[1]); }
DI float red_max(float x) {
  auto a = __builtin_amdgcn_permlane16_swap(__float_as_uint(x), __float_as_uint(x), false, false); x = fmaxf(__uint_as_float(a[0]), __uint_as_float(a[1]));
  auto b = __builtin_amdgcn_permlane32_swap(__float_as_uint(x), __float_as_uint(x), false, false); return fmaxf(__uint_as_float(b[0]), __uint_as_float(b[1])); }
DI int lane_id() { int l; asm volatile("v_mbcnt_lo_u32_b32 %0, -1, 0\n\tv_mbcnt_hi_u32_b32 %0, -1, %0" : "=v"(l)); return l; }

template <int IMM> DI void dma_nt(unsigned m0v, unsigned voff, rsrc_t rs, unsigned soff) {
  asm volatile("s_mov_b32 m0, %0\n\ts_nop 0\n\tbuffer_load_dwordx4 %1, %2, %3 offen offset:%4 nt lds" ::"s"(m0v), "v"(voff), "s"(rs), "s"(soff), "i"(IMM) : "memory");
}
DI void dma_df(unsigned m0v, unsigned voff, rsrc_t rs, unsigned soff) {
  asm volatile("s_mov_b32 m0, %0\n\ts_nop 0\n\tbuffer_load_dwordx4 %1, %2, %3 offen lds" ::"s"(m0v), "v"(voff), "s"(rs), "s"(soff) : "memory");
}

DI void mfma2(f32x4& c, const bf16x8& a0, const bf16x8& b0, const bf16x8& a1, const bf16x8& b1) {
  asm volatile("v_mfma_f32_16x16x32_bf16 %0, %1, %2, %0\n\tv_mfma_f32_16x16x32_bf16 %0, %3, %4, %0" : "+v"(c) : "v"(a0), "v"(b0), "v"(a1), "v"(b1));
}
#define WODMA dma_df
#define STAMP(i)
struct Args { const u16 *x0, *k0, *v0, *wq0, *x1, *k1, *v1, *wq1; u16* o; };

DI void slow16(const u16* Kg, const u16* Vg, int key0, int kstride, int nstep, const bf16x8 (&q)[4], f32x4 (&o)[8], float& m, float& l) {
  const int lane = lane_id(), r = lane & 15, g = lane >> 4;
  m = -3.0e38f; l = 0.f;
#pragma unroll
  for (int d = 0; d < 8; ++d) o[d] = f32x4{};
#pragma nounroll
  for (int st = 0; st < nstep; ++st) {
    const int kb = key0 + st * kstride;
    f32x4 S = {};
#pragma unroll
    for (int ks = 0; ks < 4; ++ks) S = MFMA16(*(const bf16x8*)(Kg + (size_t)(kb + r) * DH + 32 * ks + 8 * g), q[ks], S);
    const float mx = red_max(fmaxf(fmaxf(S[0], S[1]), fmaxf(S[2], S[3])));
    const float mn = fmaxf(m, mx), al = ex2(m - mn); m = mn;
    float ps = 0.f;
#pragma unroll
    for (int i = 0; i < 4; ++i) { S[i] = ex2(S[i] - mn); ps += S[i]; }
    l = l * al + ps;
    u32x2 pw = {cvtpk(S[0], S[1]), cvtpk(S[2], S[3])};
#pragma unroll
    for (int d = 0; d < 8; ++d) {
      s16x4 vf;
#pragma unroll
      for (int j = 0; j < 4; ++j) vf[j] = (short)Vg[(size_t)(kb + 4 * g + j) * DH + 16 * d + r];
      o[d] *= al;
      o[d] = MFMA16K16(vf, __builtin_bit_cast(s16x4, pw), o[d]);
    }
  }
  __builtin_amdgcn_s_waitcnt(0);
}
DI float nanprobe(const f32x4 (&o)[8]) {
  f32x4 t = {};
#pragma unroll
  for (int d = 0; d < 8; ++d) t += o[d] * 0.f;
  return (t[0] + t[1]) + (t[2] + t[3]);
}

template <int NR> struct QP { f32x4 acc[NR][4]; bf16x8 w[2][4][2], x[NR][2]; };
template <int NR, int BUF, int I> DI void qp_rdw(QP<NR>& a, unsigned wb0, unsigned wb1) {
  constexpr int i = I >> 1, ks = I & 1, imm = 32768 * BUF + 8192 * ks + 512 * (i >> 1);
  const unsigned b = (i & 1) ? wb1 : wb0;
  a.w[BUF][i][ks] = cat(rdtr(b, imm), rdtr(b, imm + 256));
}
template <int NR, int RT> DI void qp_rdx(QP<NR>& a, unsigned xp0, unsigned xp1, int slot) { a.x[slot][0] = rd128(xp0, 2048 * RT); a.x[slot][1] = rd128(xp1, 2048 * RT); }
template <int NR, int BUF, int RT, int I> DI void qp_mm(QP<NR>& a, int slot) {
  mfma2(a.acc[RT][I], a.w[BUF][I][0], a.x[slot][0], a.w[BUF][I][1], a.x[slot][1]);
}
template <int NR> constexpr int xslot(int buf, int rt) { return rt; }
template <int NR, int BUF, int RT> DI void qp_tile(QP<NR>& a) {
  constexpr int sl = xslot<NR>(BUF, RT);
  qp_mm<NR, BUF, RT, 0>(a, sl); qp_mm<NR, BUF, RT, 1>(a, sl); qp_mm<NR, BUF, RT, 2>(a, sl); qp_mm<NR, BUF, RT, 3>(a, sl);
}
template <int NR, int BUF, bool RD, bool MM> DI void qp_head(QP<NR>& a, unsigned wb0, unsigned wb1, unsigned xp0, unsigned xp1) {
  constexpr int PB = BUF ^ 1, s0 = xslot<NR>(PB, NR - 2), s1 = xslot<NR>(PB, NR - 1);
  static_assert(xslot<NR>(BUF, 0) != s0 && xslot<NR>(BUF, 0) != s1 && xslot<NR>(BUF, 1) != s0 && xslot<NR>(BUF, 1) != s1, "x slots");
  auto one = [&](auto K) {
    constexpr int k = decltype(K)::value;
    if constexpr (MM) { if constexpr (k < 4) qp_mm<NR, PB, NR - 2, k>(a, s0); else qp_mm<NR, PB, NR - 1, k - 4>(a, s1); }
    SBAR();
    if constexpr (RD) {
      if constexpr (k == 0) qp_rdx<NR, 0>(a, xp0, xp1, xslot<NR>(BUF, 0));
      if constexpr (k < 4) { qp_rdw<NR, BUF, 2 * k>(a, wb0, wb1); qp_rdw<NR, BUF, 2 * k + 1>(a, wb0, wb1); }
      if constexpr (k == 4) qp_rdx<NR, 1>(a, xp0, xp1, xslot<NR>(BUF, 1));
    }
    SBAR();
  };
  one(ic<0>{}); one(ic<1>{}); one(ic<2>{}); one(ic<3>{}); one(ic<4>{}); one(ic<5>{}); one(ic<6>{}); one(ic<7>{});
}
template <int NR, int BUF, int... RTs> DI void qp_body_(QP<NR>& a, unsigned xp0, unsigned xp1, iseq<RTs...>) {
  auto one = [&](auto R) {
    constexpr int rt = decltype(R)::value;
    qp_rdx<NR, rt + 2>(a, xp0, xp1, xslot<NR>(BUF, rt + 2));
    SBAR();
    qp_tile<NR, BUF, rt>(a);
    SBAR();
  };
  (one(ic<RTs>{}), ...);
}
template <int NR, class FX, class FW> DI void qproj(unsigned ldsb, int cg, FX&& after_bar1, FW&& after_bar2) {
  QP<NR> a;
  const unsigned lane = lane_id(), r16 = lane & 15, g = lane >> 4, q4 = (lane & 15) >> 2, p = lane & 3;
  const unsigned wb0 = ldsb + SLOT * (cg >> 1) + 1024 * (cg & 1) + 2048 * g + 64 * q4 + 16 * ((p >> 1) ^ (2 * (g & 1))) + 8 * (p & 1), wb1 = wb0 ^ 32;
  const unsigned xb0 = ldsb + XOFF + (NR == 7 ? 0 : 7 * 2048) + 128 * r16 + 16 * (g ^ ((r16 >> 1) & 7));
#pragma unroll
  for (int r = 0; r < NR; ++r)
#pragma unroll
    for (int i = 0; i < 4; ++i) a.acc[r][i] = f32x4{};
  asm volatile("s_nop 3" ::: "memory"); SBAR();
  unsigned xs = 0;
  auto step = [&](auto B, auto F, int s) {
    constexpr int BUF = decltype(B)::value; constexpr bool FIRST = decltype(F)::value;
    after_bar1(s);
    const unsigned xp0 = xb0 + xs, xp1 = xp0 ^ 64;
    SBAR();
    qp_head<NR, BUF, true, !FIRST>(a, wb0, wb1, xp0, xp1);
    BAR();
    after_bar2(s);
    SBAR();
    qp_body_<NR, BUF>(a, xp0, xp1, mkseq<NR - 2>{});
    xs = xs == 2 * XST ? 0 : xs + XST;
  };
  step(ic<0>{}, ic<1>{}, 0);
#pragma nounroll
  for (int s = 1; s < 9; s += 2) { step(ic<1>{}, ic<0>{}, s); step(ic<0>{}, ic<0>{}, s + 1); }
  step(ic<1>{}, ic<0>{}, 9);
  BAR();
  qp_head<NR, 0, false, true>(a, 0, 0, 0, 0);
  asm volatile("s_nop 7\n\ts_nop 7" ::: "memory"); SBAR();
  const unsigned ln = lane_id(), rr = ln & 15, gg = ln >> 4;
  const unsigned qw = ldsb + 2048 * (cg & 1) + 128 * rr + 16 * ((gg >> 1) ^ ((rr >> 1) & 7)) + 8 * (gg & 1);
#pragma unroll
  for (int r = 0; r < NR; ++r) {
    const int rt = (NR == 7 ? 0 : 7) + r;
    const unsigned blk = (cg >> 1) ? Q2OFF + 4096 * rt : q1addr(rt);
#pragma unroll
    for (int i = 0; i < 4; ++i) {
      const f32x4 v = a.acc[r][i] * QSCALE;
      u32x2 w = {cvtpk(v[0], v[1]), cvtpk(v[2], v[3])};
      *(LAS u32x2*)((qw ^ (32 * i)) + blk) = w;
    }
  }
}

struct AS {
  f32x4 o[3][8], S[2][3], negm[3]; bf16x8 q[3][4], F[2][4]; u32x4 P[2][3]; float l[3];
  unsigned kp[2], vp[2], kn[2];
};
template <int N, int I> DI void a_frag(AS& a) {
  constexpr int sub = (N >> 2) & 1, b = N & 3;
  if constexpr ((b & 1) == 0) {
    constexpr int kt = 2 * sub + (b >> 1);
    a.F[N & 1][I] = rd128(N == 8 ? a.kn[I & 1] : a.kp[I & 1], 8192 * (I >> 1) + 2048 * kt);
  } else {
    constexpr int c = 4 * (b >> 1) + I, imm = 8192 * sub + 512 * (c >> 1);
    a.F[N & 1][I] = cat(rdtr(a.vp[c & 1], imm), rdtr(a.vp[c & 1], imm + 4096));
  }
}
template <int N, bool MM, bool SM, int NX, bool Z, int G> DI void a_gap(AS& a) {
  constexpr int sub = N >> 2, b = N & 3;
  if constexpr (MM) {
    if constexpr ((b & 1) == 0) {
      constexpr int rt = G >> 2, ks = G & 3, sb = b >> 1;
      if constexpr (ks == 0) { if constexpr (Z) a.S[sb][rt] = MFMA16(a.F[N & 1][0], a.q[rt][0], f32x4{}); else a.S[sb][rt] = MFMA16(a.F[N & 1][0], a.q[rt][0], a.negm[rt]); }
      else a.S[sb][rt] = MFMA16(a.F[N & 1][ks], a.q[rt][ks], a.S[sb][rt]);
    } else {
      constexpr int i = G / 3, rt = G % 3, c = 4 * (b >> 1) + i;
      a.o[rt][c] = MFMA16(a.F[N & 1][i], __builtin_bit_cast(bf16x8, a.P[sub ^ 1][rt]), a.o[rt][c]);
    }
    SBAR();
  }
  if constexpr (NX >= 0 && G < 4) a_frag<NX, G>(a);
  if constexpr (SM) {
    constexpr int sb = (b == 1 || b == 2) ? 0 : 1, e0 = (b & 1) ? 0 : 6, pb = b == 0 ? sub ^ 1 : sub;
    if constexpr ((G & 1) == 0) { constexpr int e = e0 + (G >> 1); a.S[sb][e >> 2][e & 3] = ex2(a.S[sb][e >> 2][e & 3]); PIN(a.S[sb][e >> 2]); }
    else {
      constexpr int e = e0 + (G >> 1), rt = e >> 2;
      a.l[rt] = a.l[rt] + a.S[sb][rt][e & 3]; PIN(a.l[rt]);
      if constexpr (e & 1) { a.P[pb][rt][2 * sb + ((e & 3) >> 1)] = cvtpk(a.S[sb][rt][(e & 3) - 1], a.S[sb][rt][e & 3]); PIN(a.P[pb][rt]); }
    }
  }
  SBAR();
}
template <int N, bool MM, bool SM, int NX, bool Z, int... Gs> DI void a_block_(AS& a, iseq<Gs...>) { LGKM0(); SBAR(); (a_gap<N, MM, SM, NX, Z, Gs>(a), ...); }
template <int N, bool MM, bool SM, int NX, bool Z = false> DI void a_block(AS& a) { a_block_<N, MM, SM, NX, Z>(a, mkseq<12>{}); }
template <bool FIRST> DI void a_period(AS& a) {
  if constexpr (FIRST) {
    a_block<0, true, false, -1, true>(a);
#pragma unroll
    for (int rt = 0; rt < 3; ++rt) {
      const float mx = red_max(fmaxf(fmaxf(a.S[0][rt][0], a.S[0][rt][1]), fmaxf(a.S[0][rt][2], a.S[0][rt][3])));
      a.negm[rt] = f32x4{-mx, -mx, -mx, -mx}; a.S[0][rt] -= mx; PIN(a.negm[rt]);
    }
    SBAR();
    a_block<1, false, true, 2>(a); a_block<2, true, true, -1>(a); a_block<3, false, true, 4>(a);
  } else {
    a_block<0, true, true, 1>(a); a_block<1, true, true, 2>(a); a_block<2, true, true, 3>(a); a_block<3, true, true, 4>(a);
  }
  a_block<4, true, true, 5>(a); a_block<5, true, true, 6>(a); a_block<6, true, true, 7>(a);
  BAR();
  a_block<7, true, true, 8>(a);
}

DI void path_a(const u16* xg, const u16* kg, const u16* vg, char* og, unsigned ldsb, int wi, unsigned long long* stp) {
  STAMP(0);
  {
    const rsrc_t rx = __builtin_amdgcn_make_buffer_rsrc((void*)xg, 0, SQ * DM * 2, 0x00020000);
    const unsigned lane = lane_id(), vox = 1280u * (lane >> 3) + 16u * ((lane & 7) ^ ((4 * (wi & 1) + (lane >> 4)) & 7));
    auto xdma = [&](int s, unsigned stage) {
      const unsigned m0v = ldsb + XOFF + stage + 1024 * wi, so = 128u * s + 10240u * wi;
      if (wi < 2) dma_df(m0v + 6 * 4096, vox, rx, so + 6 * 40960);
#pragma unroll
      for (int n = 0; n < 6; ++n) dma_df(m0v + n * 4096, vox, rx, so + n * 40960);
    };
    xdma(0, 0); xdma(1, XST);
    unsigned xf = 2 * XST;
    qproj<7>(ldsb, wi,
      [&](int s) { if (s == 9) VMW(0); else if (wi < 2) VMW(7); else VMW(6); BAR(); if (s == 0) { STAMP(1); }
 if (s < 8) xdma(s + 2, xf);
 xf = xf == 2 * XST ? 0 : xf + XST; },
      [&](int s) {});
  }
  BAR();
  AS a;
  unsigned kb0, vb0;
  { const unsigned lane = lane_id(), r16 = lane & 15, g = lane >> 4, q4 = (lane & 15) >> 2, p = lane & 3;
    kb0 = ldsb + 128 * r16 + 16 * (g ^ ((r16 >> 1) & 7));
    vb0 = ldsb + SLOT + 2048 * (g >> 1) + 64 * (4 * (g & 1) + q4) + 16 * ((p >> 1) ^ (2 * (g & 1))) + 8 * (p & 1); }
  unsigned sl = 0;
#pragma nounroll
  for (int hd = 0; hd < 2; ++hd) {
#pragma unroll
    for (int rt = 0; rt < 3; ++rt) {
      const int t = 3 * wi + rt;
      const unsigned blk = hd ? Q2OFF + 4096 * t : (t < 8 ? 4 * SLOT + 4096 * t : t < 11 ? MISC + 4096 * (t - 8) : SLOT + 4096 * (t - 11));
      const unsigned q0 = kb0 + blk, q1 = q0 ^ 64;
      a.q[rt][0] = rd128(q0, 0); a.q[rt][1] = rd128(q1, 0); a.q[rt][2] = rd128(q0, 2048); a.q[rt][3] = rd128(q1, 2048);
#pragma unroll
      for (int c = 0; c < 8; ++c) a.o[rt][c] = f32x4{};
      a.l[rt] = 0.f;
      a.P[0][rt] = u32x4{}; a.P[1][rt] = u32x4{};
    }
    a.kn[0] = kb0 + sl; a.kn[1] = a.kn[0] ^ 64;
    a_frag<8, 0>(a); a_frag<8, 1>(a); a_frag<8, 2>(a); a_frag<8, 3>(a);
    if (hd == 0) BAR();
    STAMP(2 + 3 * hd);
    auto setp = [&]() {
      a.kp[0] = a.kn[0]; a.kp[1] = a.kn[1]; a.vp[0] = vb0 + sl; a.vp[1] = a.vp[0] ^ 32;
      sl = sl == 4 * SLOT ? 0 : sl + 2 * SLOT;
      a.kn[0] = kb0 + sl; a.kn[1] = a.kn[0] ^ 64;
      SBAR();
    };
    setp();
    a_period<true>(a);
#pragma nounroll
    for (int t = 1; t < 16; ++t) { setp(); a_period<false>(a); }
    a.vp[0] = vb0 + sl; a.vp[1] = a.vp[0] ^ 32;
    SBAR();
    a_block<0, false, true, 1>(a); a_block<1, true, false, -1>(a);
    a_frag<3, 0>(a); a_frag<3, 1>(a); a_frag<3, 2>(a); a_frag<3, 3>(a);
    a_block<3, true, false, -1>(a);
    STAMP(3 + 3 * hd);
    const unsigned lane = lane_id();
    char* ob = og + (size_t)hd * OBLK + (size_t)(3 * wi) * 2048 + 16 * lane;
#pragma unroll
    for (int rt = 0; rt < 3; ++rt) {
      float l = red_add(a.l[rt]);
      if (__builtin_expect(__any(!(l + nanprobe(a.o[rt]) < L_LIMIT)), 0)) {
        float m;
        slow16(kg + (size_t)hd * SK * DH, vg + (size_t)hd * SK * DH, 0, 16, 64, a.q[rt], a.o[rt], m, l);
        l = red_add(l);
      }
      const float il = __builtin_amdgcn_rcpf(l);
#pragma unroll
      for (int kk = 0; kk < 4; ++kk) {
        const f32x4 u = a.o[rt][2 * kk] * il, v = a.o[rt][2 * kk + 1] * il;
        u32x4 w = {cvtpk(u[0], u[1]), cvtpk(u[2], u[3]), cvtpk(v[0], v[1]), cvtpk(v[2], v[3])};
        *(u32x4*)(ob + 2048 * rt + 1024 * (kk & 1) + (OBLK / 2) * (kk >> 1)) = w;
      }
    }
    STAMP(4 + 3 * hd);
  }
  BAR();
}

DI void path_b(const u16* wqg, const u16* kg, const u16* vg, char* og, unsigned ldsb, int j, unsigned long long* stp) {
  STAMP(0);
  const rsrc_t rk = __builtin_amdgcn_make_buffer_rsrc((void*)kg, 0, 2 * SK * DH * 2, 0x00020000);
  const rsrc_t rv = __builtin_amdgcn_make_buffer_rsrc((void*)vg, 0, 2 * SK * DH * 2, 0x00020000);
  unsigned vk0, vk1, vv;
  { const unsigned i = lane_id();
    vk0 = 256 * (i >> 3) + 16 * ((i & 7) ^ (i >> 4)); vk1 = (vk0 ^ 64) + 2048;
    vv = 256 * ((i >> 2) & 7) + 64 * (i >> 5) + 16 * ((i & 3) ^ (((i >> 4) & 1) << 1)); }
  auto kdma = [&](int t, unsigned slot) {
    const unsigned m = ldsb + slot + 2048 * j, so = (unsigned)(64 * t + 16 * j) * 256u;
    dma_nt<0>(m, vk0, rk, so); dma_nt<128>(m + 8064, vk0, rk, so); dma_nt<0>(m + 1024, vk1, rk, so); dma_nt<128>(m + 9088, vk1, rk, so);
  };
  auto vdma = [&](int t, unsigned slot) {
    const unsigned m = ldsb + slot + 4096 * j, so = (unsigned)(64 * t - 32 + 16 * j) * 256u;
    dma_nt<0>(m, vv, rv, so); dma_nt<128>(m + 896, vv, rv, so); dma_nt<2048>(m, vv, rv, so); dma_nt<2176>(m + 896, vv, rv, so);
  };
  {
    const rsrc_t rw = __builtin_amdgcn_make_buffer_rsrc((void*)wqg, 0, (DM - 1) * HD * 2 + 512, 0x00020000);
    const unsigned i = lane_id(), vw0 = 8192 * ((i >> 2) & 7) + 64 * (i >> 5) + 16 * (i & 3), vw1 = (vw0 ^ 32) + 65536;
    auto wdma = [&](int s) {
#pragma unroll
      for (int e = 0; e < 2; ++e) {
        const unsigned m = ldsb + (2 * (s & 1) + e) * SLOT + 4096 * j, so = (unsigned)(64 * s + 16 * j) * 8192u + 256 * e;
        dma_nt<0>(m, vw0, rw, so); dma_nt<128>(m + 896, vw0, rw, so); dma_nt<0>(m + 2048, vw1, rw, so); dma_nt<128>(m + 2944, vw1, rw, so);
      }
    };
    wdma(0); wdma(1);
    qproj<6>(ldsb, j,
      [&](int s) { if (s < 9) VMW(8); else VMW(4); BAR(); if (s == 0) { STAMP(1); } },
      [&](int s) { if (s < 8) {
 wdma(s + 2);
 } else if (s == 8) { if (j >= 2) vdma(0, SLOT); kdma(0, 0); } else { kdma(1, 2 * SLOT); vdma(1, 3 * SLOT); } });
  }
  VMW(8);
  BAR();
  unsigned qb, kb0, vb0;
  { const unsigned lane = lane_id(), r16 = lane & 15, g = lane >> 4, q4 = (lane & 15) >> 2, p = lane & 3;
    qb = ldsb + 128 * r16 + 16 * (g ^ ((r16 >> 1) & 7));
    kb0 = qb + 2048 * j;
    vb0 = ldsb + SLOT + 4096 * ((j + 2) & 3) + 2048 * (g >> 1) + 64 * (4 * (g & 1) + q4) + 16 * ((p >> 1) ^ (2 * (g & 1))) + 8 * (p & 1); }
  bf16x8 q[4]; f32x4 o[8], os[8], negm = {}; float l = 0.f, ms = 0.f, ls = 0.f; u32x2 P = {};
  auto rdq = [&](unsigned blk) { const unsigned q0 = qb + blk, q1 = q0 ^ 64; q[0] = rd128(q0, 0); q[1] = rd128(q1, 0); q[2] = rd128(q0, 2048); q[3] = rd128(q1, 2048); };
  auto pv = [&](unsigned sl) {
    const unsigned v0 = vb0 + sl, v1 = v0 ^ 32;
    s16x4 vf[8];
#pragma unroll
    for (int c = 0; c < 8; ++c) vf[c] = rdtr((c & 1) ? v1 : v0, 512 * (c >> 1));
#pragma unroll
    for (int c = 0; c < 8; ++c) o[c] = MFMA16K16(vf[c], __builtin_bit_cast(s16x4, P), o[c]);
  };
  auto finish = [&](int hd) {
    ls = red_add(l); ms = -negm[0];
    if (__builtin_expect(__any(!(ls + nanprobe(o) < L_LIMIT)), 0)) {
      slow16(kg + (size_t)hd * SK * DH, vg + (size_t)hd * SK * DH, 16 * j, 64, 16, q, o, ms, ls);
      ls = red_add(ls);
    }
#pragma unroll
    for (int c = 0; c < 8; ++c) { os[c] = o[c]; o[c] = f32x4{}; }
    l = 0.f;
  };
  auto put = [&]() {
    const unsigned lane = lane_id(), sc = ldsb + Q2OFF + 16 * lane + 8192 * j;
#pragma unroll
    for (int c = 0; c < 8; ++c) wr128f(sc, 1024 * c, os[c]);
    if (lane < 16) { *(LAS float*)(ldsb + Q2OFF + 32768 + 128 * j + 4 * lane) = ms; *(LAS float*)(ldsb + Q2OFF + 32768 + 128 * j + 64 + 4 * lane) = ls; }
  };
  auto merge = [&](int hd) {
    const unsigned lane = lane_id(), sc = ldsb + Q2OFF + 16 * lane + 2048 * j, ml = ldsb + Q2OFF + 32768 + 4 * (lane & 15);
    float mw[4], lw[4];
#pragma unroll
    for (int w = 0; w < 4; ++w) { mw[w] = *(const LAS float*)(ml + 128 * w); lw[w] = *(const LAS float*)(ml + 128 * w + 64); }
    const float mt = fmaxf(fmaxf(mw[0], mw[1]), fmaxf(mw[2], mw[3]));
    float lt = 0.f;
#pragma unroll
    for (int w = 0; w < 4; ++w) { mw[w] = ex2(mw[w] - mt); lt += mw[w] * lw[w]; }
    const float ir = __builtin_amdgcn_rcpf(lt);
    f32x4 u = {}, v = {};
#pragma unroll
    for (int w = 0; w < 4; ++w) { u += rd128f(sc, 8192 * w) * (mw[w] * ir); v += rd128f(sc, 8192 * w + 1024) * (mw[w] * ir); }
    u32x4 w4 = {cvtpk(u[0], u[1]), cvtpk(u[2], u[3]), cvtpk(v[0], v[1]), cvtpk(v[2], v[3])};
    *(u32x4*)(og + (size_t)hd * OBLK + 12 * 2048 + 1024 * (j & 1) + (OBLK / 2) * (j >> 1) + 16 * lane) = w4;
  };
#pragma unroll
  for (int c = 0; c < 8; ++c) { o[c] = f32x4{}; os[c] = f32x4{}; }
  rdq(q1addr(12));
  __builtin_amdgcn_s_setprio(3);
  BAR();
  STAMP(2);
  unsigned sl = 0, fr = 4 * SLOT;
#pragma nounroll
  for (int t = 0; t < 32; ++t) {
    { kdma(t + 2, fr); vdma(t + 2, fr + SLOT); }
    if (j >= 2 && t > 0) pv(sl);
    if (t == 16) { finish(0); rdq(Q2OFF + 12 * 4096); }
    if (t == 17) put();
    if (t == 18) merge(0);
    {
      const unsigned k0 = kb0 + sl, k1 = k0 ^ 64;
      const bf16x8 f0 = rd128(k0, 0), f1 = rd128(k1, 0), f2 = rd128(k0, 8192), f3 = rd128(k1, 8192);
      if ((t & 15) == 0) negm = f32x4{};
      f32x4 S = MFMA16(f0, q[0], negm); S = MFMA16(f1, q[1], S); S = MFMA16(f2, q[2], S); S = MFMA16(f3, q[3], S);
      if ((t & 15) == 0) { const float mx = red_max(fmaxf(fmaxf(S[0], S[1]), fmaxf(S[2], S[3]))); negm = f32x4{-mx, -mx, -mx, -mx}; S -= mx; }
#pragma unroll
      for (int i = 0; i < 4; ++i) S[i] = ex2(S[i]);
      l = fadd_s(l, fadd_s(fadd_s(S[0], S[1]), fadd_s(S[2], S[3])));
      P = u32x2{cvtpk(S[0], S[1]), cvtpk(S[2], S[3])};
      asm volatile("s_nop 1" : "+v"(P));
    }
    if (j < 2) pv(sl);
    VMW(8);
    BAR();
    fr = sl; sl = sl == 4 * SLOT ? 0 : sl + 2 * SLOT;
  }
  if (j >= 2) pv(sl);
  finish(1);
  put();
  BAR();
  merge(1);
  STAMP(7);
}

__global__ __launch_bounds__(512, 2) void attn_k(Args ar) {
  extern __shared__ __attribute__((aligned(16))) char lds[];
  const int wid = __builtin_amdgcn_readfirstlane(threadIdx.x >> 6);
  const unsigned ldsb = (unsigned)(size_t)(LAS char*)lds;
  const int xcd = blockIdx.x & 7, idx = blockIdx.x >> 3, br = xcd >> 2, b = 2 * (xcd & 3) + (idx >> 4), hp = idx & 15;
  const u16* xg = (br ? ar.x1 : ar.x0) + (size_t)b * SQ * DM;
  const u16* kg = (br ? ar.k1 : ar.k0) + ((size_t)b * H + 2 * hp) * SK * DH;
  const u16* vg = (br ? ar.v1 : ar.v0) + ((size_t)b * H + 2 * hp) * SK * DH;
  const u16* wq = (br ? ar.wq1 : ar.wq0) + 2 * hp * DH;
  char* og = (char*)ar.o + (size_t)((br * NB + b) * H + 2 * hp) * OBLK;
  unsigned long long* stp = (unsigned long long*)((char*)ar.o + (64u << 20)) + (blockIdx.x * 8 + wid) * 32;
  if (wid < 4) path_a(xg, kg, vg, og, ldsb, wid, stp);
  else path_b(wq, kg, vg, og, ldsb, wid - 4, stp);
}

constexpr unsigned PST = 47104, POFF = 26624;
constexpr unsigned PART_OFF = 32u << 20;
struct OP { f32x4 acc[3][5], accx[2]; bf16x8 w[2][5][2], x[2][4][2]; };
template <int BUF, bool RD, bool MM, class FD> DI void op_step(OP& a, const unsigned (&wb)[5], unsigned xb, unsigned xe, unsigned st, bool two, FD&& dma) {
  constexpr int PB = BUF ^ 1;
  unsigned wp[5];
#pragma unroll
  for (int i = 0; i < 5; ++i) wp[i] = wb[i] + st;
  const unsigned xp = xb + st, xq = xe + st;
  SBAR();
  auto one = [&](auto K) {
    constexpr int k = decltype(K)::value, rt = k / 5, i = k % 5;
    if constexpr (MM) mfma2(a.acc[rt][i], a.w[PB][i][0], a.x[PB][rt][0], a.w[PB][i][1], a.x[PB][rt][1]);
    SBAR();
    if constexpr (RD) {
      if constexpr (k < 10) { constexpr int wi = k >> 1, ks = k & 1; a.w[BUF][wi][ks] = cat(rdtr(wp[wi], 10240 * ks), rdtr(wp[wi], 10240 * ks + 5120)); }
      if constexpr (k >= 4 && k < 10) a.x[BUF][(k - 4) >> 1][k & 1] = rd128(xp, 1024 * (k - 4));
      if constexpr (k == 10 || k == 11) a.x[BUF][3][k & 1] = rd128(xq, 1024 * (k & 1));
      dma(k);
    }
    SBAR();
  };
  one(ic<0>{}); one(ic<1>{}); one(ic<2>{}); one(ic<3>{}); one(ic<4>{}); one(ic<5>{}); one(ic<6>{}); one(ic<7>{});
  one(ic<8>{}); one(ic<9>{}); one(ic<10>{}); one(ic<11>{}); one(ic<12>{}); one(ic<13>{}); one(ic<14>{});
  if constexpr (MM) {
    mfma2(a.accx[0], a.w[PB][0][0], a.x[PB][3][0], a.w[PB][0][1], a.x[PB][3][1]);
    if (two) mfma2(a.accx[1], a.w[PB][1][0], a.x[PB][3][0], a.w[PB][1][1], a.x[PB][3][1]);
  }
  SBAR();
}
template <int CH> DI void oproj_body(const char* __restrict__ src, char* __restrict__ part, unsigned ldsb, int nt, int rg, unsigned long long* stp) {
  STAMP(8);
  const unsigned lane = lane_id();
#define ABL_W 1
#define ABL_O 1
  const rsrc_t rs = __builtin_amdgcn_make_buffer_rsrc((void*)src, 0, CH == 0 ? ABL_W * 1024 * DM * 2 : ABL_O * 8 * OBLK, 0x00020000);
  const unsigned xr = 16 * ((lane & 3) ^ (((lane >> 4) & 1) << 1));
  const unsigned vp = CH == 0 ? 1280 * (16 * rg + ((lane >> 2) & 7)) + 64 * (lane >> 5) + xr + 320 * nt + ((nt & 1) ? 64 : 0) : 16 * lane + 1024 * rg;
  const unsigned vo = CH == 0 ? 1280 * (16 * rg + (lane >> 2)) + xr + 320 * nt + ((nt & 1) ? 0 : 256) : 16 * lane + 1024 * (24 + (rg & 1));
  const unsigned mb = CH == 0 ? ldsb + POFF + 5120 * rg : ldsb + 1024 * rg, ml = ldsb + 1024 * (24 + (rg & 1));
  auto piece = [&](unsigned so, unsigned st, int n) {
    if constexpr (CH == 0) {
      const unsigned m = mb + st;
      if (n == 0) WODMA(m, vp, rs, so);
      if (n == 1) WODMA(m + 1024, vp, rs, so + 128);
      if (n == 2) WODMA(m + 3072, vp, rs, so + 10240);
      if (n == 3) WODMA(m + 4096, vp, rs, so + 10240 + 128);
      if (n == 4) WODMA(m + 2048, vo, rs, so);
    } else {
      if (n < 6) dma_df(mb + st + 4096 * n, vp, rs, so + 4096 * n); else dma_df(ml + st, vo, rs, so);
    }
  };
  constexpr unsigned SSTEP = CH == 0 ? 64 * 1280 : OBLK / 2;
#pragma unroll
  for (int n = 0; n < 7; ++n) piece(0, 0, n);
#pragma unroll
  for (int n = 0; n < 7; ++n) piece(SSTEP, PST, n);
  OP a;
  const int rot = CH == 0 ? (rg == 0 ? 0 : rg + 1) : rg;
  const bool two = CH == 0 ? rg == 0 : rg == 3;
  unsigned wb[5];
  {
    const unsigned g = lane >> 4, q4 = (lane & 15) >> 2, p = lane & 3;
    const unsigned in = 64 * (4 * (g & 1) + q4) + 16 * ((p >> 1) ^ (2 * (g & 1))) + 8 * (p & 1);
#pragma unroll
    for (int i = 0; i < 5; ++i) {
      int c = i + rot; c = 5 * CH + (c >= 5 ? c - 5 : c);
      const unsigned off = c < 8 ? 512 * (c >> 1) + 3072 * (g >> 1) : 2048 + 512 * (g >> 1);
      wb[i] = ldsb + POFF + off + (in ^ (32 * (c & 1)));
    }
  }
  const unsigned xb = ldsb + 16 * lane + 6144 * rg, xe = ldsb + 16 * lane + 24576;
#pragma unroll
  for (int r = 0; r < 3; ++r)
#pragma unroll
    for (int i = 0; i < 5; ++i) a.acc[r][i] = f32x4{};
  a.accx[0] = f32x4{}; a.accx[1] = f32x4{};
  unsigned st = 0, fr = 2 * PST, so = 2 * SSTEP;
  auto step = [&](auto B, auto F) {
    constexpr int BUF = decltype(B)::value; constexpr bool FIRST = decltype(F)::value;
    if constexpr (CH == 0) VMW(5); else VMW(7);
    BAR();
    op_step<BUF, true, !FIRST>(a, wb, xb, xe, st, two, [&](int k) { if constexpr (CH == 0) { if (k % 3 == 1) piece(so, fr, k / 3); } else { if ((k & 1) == 0 && k < 14) piece(so, fr, k >> 1); } });
    fr = st; st = st == 2 * PST ? 0 : st + PST; so += SSTEP;
  };
  step(ic<0>{}, ic<1>{});
  STAMP(9);
#pragma nounroll
  for (int s = 1; s < 15; s += 2) { step(ic<1>{}, ic<0>{}); step(ic<0>{}, ic<0>{}); }
  STAMP(10);
  step(ic<1>{}, ic<0>{});
  LGKM0();
  op_step<0, false, true>(a, wb, xb, xe, 0, two, [&](int) {});
  asm volatile("s_nop 7\n\ts_nop 7" ::: "memory"); SBAR();
  STAMP(11);
  char* pb = part + 8 * lane_id();
#pragma unroll
  for (int i = 0; i < 5; ++i) {
    int c = i + rot; c = 5 * CH + (c >= 5 ? c - 5 : c);
#pragma unroll
    for (int r = 0; r < 3; ++r) { const f32x4 v = a.acc[r][i]; *(u32x2*)(pb + ((3 * rg + r) * 10 + c) * 512) = u32x2{cvtpk(v[0], v[1]), cvtpk(v[2], v[3])}; }
    if (i == 0 || (i == 1 && two)) { const f32x4 v = a.accx[i & 1]; *(u32x2*)(pb + (120 + c) * 512) = u32x2{cvtpk(v[0], v[1]), cvtpk(v[2], v[3])}; }
  }
  VMW(0);
  STAMP(12);
}
__global__ __launch_bounds__(512, 2) void oproj_k(const char* __restrict__ ws, char* __restrict__ part, const u16* __restrict__ wo0, const u16* __restrict__ wo1) {
  extern __shared__ __attribute__((aligned(16))) char lds[];
  const int w = __builtin_amdgcn_readfirstlane(threadIdx.x >> 6);
  const unsigned ldsb = (unsigned)(size_t)(LAS char*)lds;
  const int xcd = blockIdx.x & 7, idx = blockIdx.x >> 3, br = xcd >> 2, b = 2 * (xcd & 3) + (idx >> 4), nt = (idx >> 2) & 3, kq = idx & 3;
  char* pb = part + (size_t)blockIdx.x * (130 * 512);
  unsigned long long* stp = (unsigned long long*)((char*)ws + (64u << 20)) + (blockIdx.x * 8 + w) * 32;
  if (w < 4) oproj_body<0>((const char*)((br ? wo1 : wo0) + (size_t)kq * 1024 * DM), pb, ldsb, nt, w, stp);
  else oproj_body<1>(ws + (size_t)((br * NB + b) * H + 8 * kq) * OBLK, pb, ldsb, nt, w - 4, stp);
}
__global__ __launch_bounds__(512) void reduce_k(const char* __restrict__ part, u16* __restrict__ out) {
  const int xcd = blockIdx.x & 7, idx = blockIdx.x >> 3, br = xcd >> 2, b = 2 * (xcd & 3) + (idx >> 4), nt = (idx >> 2) & 3, kq = idx & 3;
  const int lane = threadIdx.x & 63, w = threadIdx.x >> 6;
  const char* pb = part + (size_t)(xcd + 8 * (idx & ~3)) * (130 * 512) + 8 * lane;
  u32x2 v[5][4];
#pragma unroll
  for (int n = 0; n < 5; ++n) {
    const int t = kq + 4 * (w + 8 * n), tt = t < 130 ? t : 0;
#pragma unroll
    for (int q = 0; q < 4; ++q) v[n][q] = *(const u32x2*)(pb + (size_t)q * 8 * (130 * 512) + tt * 512);
  }
#pragma unroll
  for (int n = 0; n < 5; ++n) {
    const int t = kq + 4 * (w + 8 * n);
    if (t < 130) {
      float s0 = 0.f, s1 = 0.f, s2 = 0.f, s3 = 0.f;
#pragma unroll
      for (int q = 0; q < 4; ++q) {
        s0 += __uint_as_float(v[n][q][0] << 16); s1 += __uint_as_float(v[n][q][0] & 0xffff0000u);
        s2 += __uint_as_float(v[n][q][1] << 16); s3 += __uint_as_float(v[n][q][1] & 0xffff0000u);
      }
      const int rt = t / 10, c = t % 10;
      const int col = 160 * nt + (c < 8 ? ((nt & 1) ? 32 : 0) + 16 * c : ((nt & 1) ? 0 : 128) + 16 * (c - 8)) + 4 * (lane >> 4);
      *(u32x2*)(out + (size_t)(b * SQ + 16 * rt + (lane & 15)) * (2 * DM) + br * DM + col) = u32x2{cvtpk(s0, s1), cvtpk(s2, s3)};
    }
  }
}
}

extern "C" void kernel_launch(void* const* d_in, const int* in_sizes, int n_in, void* d_out, int out_size, void* d_ws, size_t ws_size, hipStream_t stream) {
  using namespace kk;
  Args a;
  a.x0 = (const u16*)d_in[0]; a.k0 = (const u16*)d_in[1]; a.v0 = (const u16*)d_in[2]; a.wq0 = (const u16*)d_in[3];
  a.x1 = (const u16*)d_in[5]; a.k1 = (const u16*)d_in[6]; a.v1 = (const u16*)d_in[7]; a.wq1 = (const u16*)d_in[8];
  a.o = (u16*)d_ws;
  (void)hipFuncSetAttribute((const void*)attn_k, hipFuncAttributeMaxDynamicSharedMemorySize, (int)LDS_TOTAL);
  attn_k<<<256, 512, LDS_TOTAL, stream>>>(a);
  (void)hipFuncSetAttribute((const void*)oproj_k, hipFuncAttributeMaxDynamicSharedMemorySize, (int)(3 * PST));
  oproj_k<<<256, 512, 3 * PST, stream>>>((const char*)d_ws, (char*)d_ws + PART_OFF, (const u16*)d_in[4], (const u16*)d_in[9]);
  reduce_k<<<256, 512, 0, stream>>>((const char*)d_ws + PART_OFF, (u16*)d_out);
}
```

```cpp
#include <hip/hip_runtime.h>
#include <hip/hip_bf16.h>
#include <utility>

namespace kk {
typedef unsigned short u16;
typedef short bf16x8 __attribute__((ext_vector_type(8)));
typedef short s16x4 __attribute__((ext_vector_type(4)));
typedef float f32x4 __attribute__((ext_vector_type(4)));
typedef float f32x2 __attribute__((ext_vector_type(2)));
typedef __bf16 bf16x2 __attribute__((ext_vector_type(2)));
typedef unsigned u32x4 __attribute__((ext_vector_type(4)));
typedef unsigned u32x2 __attribute__((ext_vector_type(2)));
typedef __amdgpu_buffer_rsrc_t rsrc_t;
typedef __attribute__((address_space(1))) unsigned gu32;
#define LAS __attribute__((address_space(3)))
#define DI __device__ __forceinline__
#define SBAR() __builtin_amdgcn_sched_barrier(0)
#define PIN(x) asm volatile("" : "+v"(x))
#define MFMA16(a, b, c) __builtin_amdgcn_mfma_f32_16x16x32_bf16(a, b, c, 0, 0, 0)
#define MFMA16K16(a, b, c) __builtin_amdgcn_mfma_f32_16x16x16bf16_1k(a, b, c, 0, 0, 0)
#define LGKM0() __builtin_amdgcn_s_waitcnt(0xC07F)
#define VMW(N) asm volatile("s_waitcnt vmcnt(" #N ")" ::: "memory")
#define BAR() do { LGKM0(); asm volatile("s_barrier" ::: "memory"); } while (0)
#define MFMA_DONE() do { asm volatile("s_nop 7\n\ts_nop 3" ::: "memory"); SBAR(); } while (0)
template <int... Is> using iseq = std::integer_sequence<int, Is...>;
template <int N> using mkseq = std::make_integer_sequence<int, N>;
template <int N> using ic = std::integral_constant<int, N>;

constexpr int NB = 8, SQ = 208, DM = 640, H = 32, SK = 1024, DH = 128, HD = H * DH, NRT = 13;
constexpr unsigned SLOT = 16384, XST = 26624, XOFF = 5 * SLOT, Q2OFF = 6 * SLOT, MISC = Q2OFF + NRT * 4096, LDS_TOTAL = 163840;
static_assert(XOFF + 3 * XST <= LDS_TOTAL && MISC + 3 * 4096 == LDS_TOTAL, "lds map");
constexpr unsigned OBLK = NRT * 4096;
constexpr unsigned PST = 47104, POFF = 26624;
constexpr unsigned PS0 = LDS_TOTAL - PST, PS1 = 0, PS2 = PST, SCR2 = PST;
static_assert(SCR2 + 32768 + 512 <= 5 * SLOT && PS2 + PST <= PS0, "lds map 2");
constexpr size_t WS_CNT = 0, CNT_BYTES = 16384, WS_O = 1u << 20, WS_PART = 32u << 20, WS_STAMP = 64u << 20;
constexpr float QSCALE = 0.08838834764831845f * 1.4426950408889634f;
constexpr float L_LIMIT = 1.0e24f;
__host__ __device__ constexpr unsigned q1addr(int rt) { return rt < 8 ? 4 * SLOT + 4096 * rt : rt < 11 ? MISC + 4096 * (rt - 8) : SLOT + 4096 * (rt - 11); }

DI unsigned cvtpk(float lo, float hi) { f32x2 v = {lo, hi}; return __builtin_bit_cast(unsigned, __builtin_convertvector(v, bf16x2)); }
DI float fmul_s(float a, float b) { float r; asm("v_mul_f32_e32 %0, %1, %2" : "=v"(r) : "s"(b), "v"(a)); return r; }
DI float ex2(float x) { return __builtin_amdgcn_exp2f(x); }
DI bf16x8 rd128(unsigned base, int imm) { return *(const LAS bf16x8*)((const LAS char*)base + imm); }
DI s16x4 rdtr(unsigned base, int imm) { return __builtin_amdgcn_ds_read_tr16_b64_v4i16((LAS s16x4*)((LAS char*)base + imm)); }
DI bf16x8 cat(s16x4 lo, s16x4 hi) { return __builtin_shufflevector(lo, hi, 0, 1, 2, 3, 4, 5, 6, 7); }
DI void wr128f(unsigned base, int imm, f32x4 v) { *(LAS f32x4*)((LAS char*)base + imm) = v; }
DI f32x4 rd128f(unsigned base, int imm) { return *(const LAS f32x4*)((const LAS char*)base + imm); }
DI float red_add(float x) {
  auto a = __builtin_amdgcn_permlane16_swap(__float_as_uint(x), __float_as_uint(x), false, false); x = __uint_as_float(a[0]) + __uint_as_float(a[1]);
  auto b = __builtin_amdgcn_permlane32_swap(__float_as_uint(x), __float_as_uint(x), false, false); return __uint_as_float(b[0]) + __uint_as_float(b[1]); }
DI float red_max(float x) {
  auto a = __builtin_amdgcn_permlane16_swap(__float_as_uint(x), __float_as_uint(x), false, false); x = fmaxf(__uint_as_float(a[0]), __uint_as_float(a[1]));
  auto b = __builtin_amdgcn_permlane32_swap(__float_as_uint(x), __float_as_uint(x), false, false); return fmaxf(__uint_as_float(b[0]), __uint_as_float(b[1])); }
DI int lane_id() { int l; asm volatile("v_mbcnt_lo_u32_b32 %0, -1, 0\n\tv_mbcnt_hi_u32_b32 %0, -1, %0" : "=v"(l)); return l; }
DI rsrc_t mkrs(const void* p, unsigned bytes) { return __builtin_amdgcn_make_buffer_rsrc((void*)p, 0, bytes, 0x00020000); }

template <int IMM> DI void dma_nt(unsigned m0v, unsigned voff, rsrc_t rs, unsigned soff) {
  asm volatile("s_mov_b32 m0, %0\n\ts_nop 0\n\tbuffer_load_dwordx4 %1, %2, %3 offen offset:%4 nt lds" ::"s"(m0v), "v"(voff), "s"(rs), "s"(soff), "i"(IMM) : "memory");
}
DI void dma_df(unsigned m0v, unsigned voff, rsrc_t rs, unsigned soff) {
  asm volatile("s_mov_b32 m0, %0\n\ts_nop 0\n\tbuffer_load_dwordx4 %1, %2, %3 offen lds" ::"s"(m0v), "v"(voff), "s"(rs), "s"(soff) : "memory");
}
DI void mfma2(f32x4& c, const bf16x8& a0, const bf16x8& b0, const bf16x8& a1, const bf16x8& b1) {
  asm volatile("v_mfma_f32_16x16x32_bf16 %0, %1, %2, %0\n\tv_mfma_f32_16x16x32_bf16 %0, %3, %4, %0" : "+v"(c) : "v"(a0), "v"(b0), "v"(a1), "v"(b1));
}
DI void wait_quad(const unsigned* cnt) {
  for (int spin = 0; spin < (1 << 22); ++spin) {
    const unsigned v = __hip_atomic_load((gu32*)cnt, __ATOMIC_RELAXED, __HIP_MEMORY_SCOPE_AGENT);
    if (__builtin_amdgcn_readfirstlane(v) >= 4u) break;
    __builtin_amdgcn_s_sleep(4);
  }
  __builtin_amdgcn_fence(__ATOMIC_ACQUIRE, "agent");
  VMW(0);
}
DI void signal_quad(unsigned* cnt) { if (lane_id() == 0) __hip_atomic_fetch_add((gu32*)cnt, 1u, __ATOMIC_RELAXED, __HIP_MEMORY_SCOPE_AGENT); }

#define STAMP(i)
#define FST(i)
struct Args { const u16 *x0, *k0, *v0, *wq0, *wo0, *x1, *k1, *v1, *wq1, *wo1; char* ws; };
struct Ctx {
  const u16 *x, *k, *v, *wq; const char *wo, *warm; char *oq, *part; unsigned* cnt; unsigned long long* stp; unsigned ldsb; int own, nt;
};

DI void slow16(const u16* Kg, const u16* Vg, int key0, int kstride, int nstep, const bf16x8 (&q)[4], f32x4 (&o)[8], float& m, float& l) {
  const int lane = lane_id(), r = lane & 15, g = lane >> 4;
  m = -3.0e38f; l = 0.f;
#pragma unroll
  for (int d = 0; d < 8; ++d) o[d] = f32x4{};
#pragma nounroll
  for (int st = 0; st < nstep; ++st) {
    const int kb = key0 + st * kstride;
    f32x4 S = {};
#pragma unroll
    for (int ks = 0; ks < 4; ++ks) S = MFMA16(*(const bf16x8*)(Kg + (size_t)(kb + r) * DH + 32 * ks + 8 * g), q[ks], S);
    const float mx = red_max(fmaxf(fmaxf(S[0], S[1]), fmaxf(S[2], S[3])));
    const float mn = fmaxf(m, mx), al = ex2(m - mn); m = mn;
    float ps = 0.f;
#pragma unroll
    for (int i = 0; i < 4; ++i) { S[i] = ex2(S[i] - mn); ps += S[i]; }
    l = l * al + ps;
    u32x2 pw = {cvtpk(S[0], S[1]), cvtpk(S[2], S[3])};
#pragma unroll
    for (int d = 0; d < 8; ++d) {
      s16x4 vf;
#pragma unroll
      for (int j = 0; j < 4; ++j) vf[j] = (short)Vg[(size_t)(kb + 4 * g + j) * DH + 16 * d + r];
      o[d] *= al;
      o[d] = MFMA16K16(vf, __builtin_bit_cast(s16x4, pw), o[d]);
    }
  }
  __builtin_amdgcn_s_waitcnt(0);
}
DI float nanprobe(const f32x4 (&o)[8]) {
  f32x4 t = {};
#pragma unroll
  for (int d = 0; d < 8; ++d) t += o[d] * 0.f;
  return (t[0] + t[1]) + (t[2] + t[3]);
}

template <int NR, class FD, class FW> DI void qproj(const Ctx& c, int cg, FD&& dma, FW&& wait) {
  f32x4 acc[NR][4]; bf16x8 w[4][2], x[NR][2];
  const unsigned lane = lane_id(), r16 = lane & 15, g = lane >> 4, q4 = (lane & 15) >> 2, p = lane & 3;
  const unsigned wb0 = c.ldsb + 1024 * (cg & 1) + 2048 * g + 64 * q4 + 16 * ((p >> 1) ^ (2 * (g & 1))) + 8 * (p & 1);
  const unsigned xb0 = c.ldsb + XOFF + (NR == 7 ? 0 : 7 * 2048) + 128 * r16 + 16 * (g ^ ((r16 >> 1) & 7));
#pragma unroll
  for (int r = 0; r < NR; ++r)
#pragma unroll
    for (int i = 0; i < 4; ++i) acc[r][i] = f32x4{};
  unsigned xs = 0, ws = SLOT * (cg >> 1), xp0, xp1, wp0, wp1;
  auto ldw = [&](int i) { const unsigned b = (i & 1) ? wp1 : wp0; const int im = 512 * (i >> 1);
    w[i][0] = cat(rdtr(b, im), rdtr(b, im + 256)); w[i][1] = cat(rdtr(b, im + 8192), rdtr(b, im + 8448)); };
  auto ldx = [&](int r) { x[r][0] = rd128(xp0, 2048 * r); x[r][1] = rd128(xp1, 2048 * r); };
  auto row = [&](int r) {
#pragma unroll
    for (int i = 0; i < 4; ++i) mfma2(acc[r][i], w[i][0], x[r][0], w[i][1], x[r][1]);
  };
  auto step = [&](auto K, auto F, int s) {
    constexpr bool FIRST = decltype(F)::value;
    wait(s); BAR();
    FST(s);
    if constexpr (FIRST) { STAMP(1); }
    xp0 = xb0 + xs; xp1 = xp0 ^ 64; wp0 = wb0 + ws; wp1 = wp0 ^ 32;
    SBAR();
    if constexpr (NR == 7) {
#pragma unroll
      for (int r = 0; r < 7; ++r) { if constexpr (!FIRST) row(r); SBAR(); ldx(r); if (r & 1) dma(K, s, r >> 1); SBAR(); }
#pragma unroll
      for (int i = 0; i < 4; ++i) { ldw(i); dma(K, s, 3 + i); SBAR(); }
    } else {
#pragma unroll
      for (int i = 0; i < 4; ++i) { dma(K, s, i); ldw(i); SBAR(); }
#pragma unroll
      for (int r = 0; r < 6; ++r) { if (r == 2) dma(K, s, 4); ldx(r); SBAR(); }
#pragma unroll
      for (int r = 0; r < 6; ++r) { row(r); SBAR(); if ((r & 1) == 0) dma(K, s, 5 + (r >> 1)); SBAR(); }
    }
    xs = xs == 2 * XST ? 0 : xs + XST; ws += 2 * SLOT; ws = ws >= 5 * SLOT ? ws - 5 * SLOT : ws;
  };
  asm volatile("s_nop 1" ::: "memory");
  step(ic<NR == 7 ? 0 : 3>{}, ic<1>{}, 0);
#pragma nounroll
  for (int s = 1; s < 8; ++s) step(ic<0>{}, ic<0>{}, s);
  step(ic<1>{}, ic<0>{}, 8);
  step(ic<2>{}, ic<0>{}, 9);
  BAR();
  FST(10);
  dma(ic<4>{}, 10, 0);
  const unsigned ln = lane_id(), rr = ln & 15, gg = ln >> 4;
  const unsigned qw = c.ldsb + 2048 * (cg & 1) + 128 * rr + 16 * ((gg >> 1) ^ ((rr >> 1) & 7)) + 8 * (gg & 1);
  auto put = [&](int r) {
    const int rt = (NR == 7 ? 0 : 7) + r;
    const unsigned blk = (cg >> 1) ? Q2OFF + 4096 * rt : q1addr(rt);
#pragma unroll
    for (int i = 0; i < 4; ++i) {
      const f32x4 v = acc[r][i];
      *(LAS u32x2*)((qw ^ (32 * i)) + blk) = u32x2{cvtpk(fmul_s(v[0], QSCALE), fmul_s(v[1], QSCALE)), cvtpk(fmul_s(v[2], QSCALE), fmul_s(v[3], QSCALE))};
    }
  };
  SBAR();
  if constexpr (NR == 7) {
#pragma unroll
    for (int r = 0; r < 7; ++r) { row(r); SBAR(); if (r) put(r - 1); SBAR(); }
    MFMA_DONE();
    put(6);
  } else {
    MFMA_DONE();
#pragma unroll
    for (int r = 0; r < 6; ++r) put(r);
  }
}

struct OD { rsrc_t rw, ro; unsigned vwp, vwo, vo, mw, mo, lo; int rg; };
DI OD od_make(const Ctx& c, int rg) {
  OD d; const unsigned lane = lane_id(), xr = 16 * ((lane & 3) ^ (((lane >> 4) & 1) << 1));
  d.rw = mkrs(c.wo, 1024 * DM * 2); d.ro = mkrs(c.oq, 8 * OBLK);
  d.vwp = 1280 * (16 * rg + ((lane >> 2) & 7)) + 64 * (lane >> 5) + xr + 320 * c.nt + ((c.nt & 1) ? 64 : 0);
  d.vwo = 1280 * (16 * rg + (lane >> 2)) + xr + 320 * c.nt + ((c.nt & 1) ? 0 : 256);
  d.vo = 16 * lane; d.mw = c.ldsb + POFF + 5120 * rg; d.mo = c.ldsb + 1024 * rg; d.lo = 1024 * (24 + (rg & 1)); d.rg = rg;
  return d;
}
DI unsigned od_blk(int s) { return (unsigned)(s < 16 ? 4 * ((s & 7) >> 1) + 2 * (s >> 3) + (s & 1) : 64); }
DI void od_piece(const Ctx& c, const OD& d, unsigned u, unsigned st, int n) {
  const unsigned sw = u * (64 * 1280), so = u * (OBLK / 2), m = d.mw + st;
  if (n == 0) dma_df(m, d.vwp, d.rw, sw);
  else if (n == 1) dma_df(m + 1024, d.vwp, d.rw, sw + 128);
  else if (n == 2) dma_df(m + 3072, d.vwp, d.rw, sw + 10240);
  else if (n == 3) dma_df(m + 4096, d.vwp, d.rw, sw + 10240 + 128);
  else if (n == 4) dma_df(m + 2048, d.vwo, d.rw, sw);
  else if (n < 11) dma_df(d.mo + st + 4096 * (n - 5), d.vo, d.ro, so + 1024 * d.rg + 4096 * (n - 5));
  else dma_df(c.ldsb + d.lo + st, d.vo, d.ro, so + d.lo);
}
template <int NC, int NR> DI void oproj(const Ctx& c, int rg) {
  const OD d = od_make(c, rg);
  f32x4 acc[NR][NC]; bf16x8 w[NC][2], x[NR][2];
  const int c0 = NC == 3 ? 3 * (rg >> 1) : 6 + 2 * (rg >> 1), r0 = NR == 7 ? 0 : 7;
  unsigned wb[NC], xb;
  {
    const unsigned lane = lane_id(), g = lane >> 4, q4 = (lane & 15) >> 2, p = lane & 3;
    const unsigned in = 64 * (4 * (g & 1) + q4) + 16 * ((p >> 1) ^ (2 * (g & 1))) + 8 * (p & 1);
#pragma unroll
    for (int i = 0; i < NC; ++i) {
      const int ct = c0 + i;
      const unsigned off = ct < 8 ? 512 * (ct >> 1) + 3072 * (g >> 1) : 2048 + 512 * (g >> 1);
      wb[i] = c.ldsb + POFF + off + (in ^ (32 * (ct & 1)));
    }
    xb = c.ldsb + 16 * lane + 2048 * r0;
  }
#pragma unroll
  for (int r = 0; r < NR; ++r)
#pragma unroll
    for (int i = 0; i < NC; ++i) acc[r][i] = f32x4{};
  unsigned st = PS0, nx = PS1, fr = PS2;
  auto ldw = [&](int i) { const unsigned wp = wb[i] + st; w[i][0] = cat(rdtr(wp, 0), rdtr(wp, 5120)); w[i][1] = cat(rdtr(wp, 10240), rdtr(wp, 15360)); };
  auto ldx = [&](int r) { const unsigned xp = xb + st; x[r][0] = rd128(xp, 2048 * r); x[r][1] = rd128(xp, 2048 * r + 1024); };
  auto row = [&](int r) {
#pragma unroll
    for (int i = 0; i < NC; ++i) mfma2(acc[r][i], w[i][0], x[r][0], w[i][1], x[r][1]);
  };
  auto step = [&](auto F, int s) {
    constexpr bool FIRST = decltype(F)::value;
    const unsigned u = od_blk(s + 2);
    SBAR();
    if constexpr (NC == 3) {
#pragma unroll
      for (int r = 0; r < NR; ++r) { if constexpr (!FIRST) row(r); SBAR(); ldx(r); if (r == 1 || r == 4) od_piece(c, d, u, fr, r / 3); SBAR(); }
#pragma unroll
      for (int i = 0; i < 3; ++i) { ldw(i); if (i) od_piece(c, d, u, fr, 1 + i); SBAR(); }
    } else {
#pragma unroll
      for (int i = 0; i < 2; ++i) { od_piece(c, d, u, fr, 4 + i); ldw(i); SBAR(); }
#pragma unroll
      for (int r = 0; r < NR; ++r) { if ((r & 1) == 0 && r < 6) od_piece(c, d, u, fr, 6 + (r >> 1)); ldx(r); SBAR(); }
#pragma unroll
      for (int r = 0; r < NR; ++r) { row(r); SBAR(); if ((r & 1) == 0 && r < 6) od_piece(c, d, u, fr, 9 + (r >> 1)); SBAR(); }
    }
    const unsigned t = st; st = nx; nx = fr; fr = t;
  };
  int fi = 14;
  auto sync = [&]() { if constexpr (NC == 3) VMW(4); else VMW(8); BAR(); FST(fi); ++fi; };
  asm volatile("s_nop 1" ::: "memory");
  if constexpr (NC == 2) VMW(13);
  BAR();
  STAMP(8); FST(13);
  step(ic<1>{}, 0);
  sync();
  if (NC == 2 && rg == 0) signal_quad(c.cnt + 32);
#pragma nounroll
  for (int s = 1; s < 5; ++s) { step(ic<0>{}, s); sync(); }
  step(ic<0>{}, 5);
  if (NC == 3 && rg == 1) wait_quad(c.cnt + 32);
  sync();
  STAMP(9);
#pragma nounroll
  for (int s = 6; s < 15; ++s) { step(ic<0>{}, s); sync(); }
  step(ic<0>{}, 15);
  if constexpr (NC == 3) {
    LGKM0(); SBAR();
#pragma unroll
    for (int r = 0; r < NR; ++r) row(r);
  }
  MFMA_DONE();
  STAMP(10);
  char* pb = c.part + 8 * lane_id();
#pragma unroll
  for (int r = 0; r < NR; ++r)
#pragma unroll
    for (int i = 0; i < NC; ++i) { const f32x4 v = acc[r][i]; *(u32x2*)(pb + ((r0 + r) * 10 + c0 + i) * 512) = u32x2{cvtpk(v[0], v[1]), cvtpk(v[2], v[3])}; }
  STAMP(11);
}

struct AS {
  f32x4 o[3][8], S[2][3], negm[3]; bf16x8 q[3][4], F[2][4]; u32x4 P[2][3]; float l[3];
  unsigned kp[2], vp[2], kn[2];
};
template <int N, int I> DI void a_frag(AS& a) {
  constexpr int sub = (N >> 2) & 1, b = N & 3;
  if constexpr ((b & 1) == 0) {
    constexpr int kt = 2 * sub + (b >> 1);
    a.F[N & 1][I] = rd128(N == 8 ? a.kn[I & 1] : a.kp[I & 1], 8192 * (I >> 1) + 2048 * kt);
  } else {
    constexpr int cc = 4 * (b >> 1) + I, imm = 8192 * sub + 512 * (cc >> 1);
    a.F[N & 1][I] = cat(rdtr(a.vp[cc & 1], imm), rdtr(a.vp[cc & 1], imm + 4096));
  }
}
template <int N, bool MM, bool SM, int NX, bool Z, int G> DI void a_gap(AS& a) {
  constexpr int sub = N >> 2, b = N & 3;
  if constexpr (MM) {
    if constexpr ((b & 1) == 0) {
      constexpr int rt = G >> 2, ks = G & 3, sb = b >> 1;
      if constexpr (ks == 0) { if constexpr (Z) a.S[sb][rt] = MFMA16(a.F[N & 1][0], a.q[rt][0], f32x4{}); else a.S[sb][rt] = MFMA16(a.F[N & 1][0], a.q[rt][0], a.negm[rt]); }
      else a.S[sb][rt] = MFMA16(a.F[N & 1][ks], a.q[rt][ks], a.S[sb][rt]);
    } else {
      constexpr int i = G / 3, rt = G % 3, cc = 4 * (b >> 1) + i;
      if constexpr (Z) a.o[rt][cc] = MFMA16(a.F[N & 1][i], __builtin_bit_cast(bf16x8, a.P[sub ^ 1][rt]), f32x4{});
      else a.o[rt][cc] = MFMA16(a.F[N & 1][i], __builtin_bit_cast(bf16x8, a.P[sub ^ 1][rt]), a.o[rt][cc]);
    }
    SBAR();
  }
  if constexpr (NX >= 0 && G < 4) a_frag<NX, G>(a);
  if constexpr (SM) {
    constexpr int sb = (b == 1 || b == 2) ? 0 : 1, e0 = (b & 1) ? 0 : 6, pb = b == 0 ? sub ^ 1 : sub;
    if constexpr ((G & 1) == 0) { constexpr int e = e0 + (G >> 1); a.S[sb][e >> 2][e & 3] = ex2(a.S[sb][e >> 2][e & 3]); PIN(a.S[sb][e >> 2]); }
    else {
      constexpr int e = e0 + (G >> 1), rt = e >> 2;
      a.l[rt] = a.l[rt] + a.S[sb][rt][e & 3]; PIN(a.l[rt]);
      if constexpr (e & 1) { a.P[pb][rt][2 * sb + ((e & 3) >> 1)] = cvtpk(a.S[sb][rt][(e & 3) - 1], a.S[sb][rt][e & 3]); PIN(a.P[pb][rt]); }
    }
  }
  SBAR();
}
template <int N, bool MM, bool SM, int NX, bool Z, int... Gs> DI void a_block_(AS& a, iseq<Gs...>) { LGKM0(); SBAR(); (a_gap<N, MM, SM, NX, Z, Gs>(a), ...); }
template <int N, bool MM, bool SM, int NX, bool Z = false> DI void a_block(AS& a) { a_block_<N, MM, SM, NX, Z>(a, mkseq<12>{}); }
template <bool FIRST> DI void a_period(AS& a) {
  if constexpr (FIRST) {
    a_block<0, true, false, -1, true>(a);
#pragma unroll
    for (int rt = 0; rt < 3; ++rt) {
      const float mx = red_max(fmaxf(fmaxf(a.S[0][rt][0], a.S[0][rt][1]), fmaxf(a.S[0][rt][2], a.S[0][rt][3])));
      a.negm[rt] = f32x4{-mx, -mx, -mx, -mx}; a.S[0][rt] -= mx; PIN(a.negm[rt]);
    }
    SBAR();
    a_block<1, false, true, 2>(a); a_block<2, true, true, -1>(a); a_block<3, false, true, 4>(a);
  } else {
    a_block<0, true, true, 1>(a); a_block<1, true, true, 2>(a); a_block<2, true, true, 3>(a); a_block<3, true, true, 4>(a);
  }
  a_block<4, true, true, 5>(a); a_block<5, true, true, 6, FIRST>(a); a_block<6, true, true, 7>(a);
  if constexpr (FIRST) VMW(0);
  BAR();
  a_block<7, true, true, 8, FIRST>(a);
}

DI void path_a(const Ctx& c, int wi) {
  STAMP(0);
  {
    const rsrc_t rx = mkrs(c.x, SQ * DM * 2);
    const unsigned lane = lane_id(), vox = 1280u * (lane >> 3) + 16u * ((lane & 7) ^ ((4 * (wi & 1) + (lane >> 4)) & 7));
    auto xp = [&](int s, unsigned stage, int n) { const int pc = n < 6 ? wi + 4 * n : 24 + (wi & 1); dma_df(c.ldsb + XOFF + stage + 1024 * pc, vox, rx, 128u * s + 10240u * pc); };
#pragma unroll
    for (int n = 0; n < 7; ++n) xp(0, 0, n);
#pragma unroll
    for (int n = 0; n < 7; ++n) xp(1, XST, n);
    unsigned xf = XST;
    qproj<7>(c, wi,
      [&](auto K, int s, int n) { if constexpr (decltype(K)::value == 0) xp(s + 2, xf, n); },
      [&](int s) { if (s == 9) VMW(0); else VMW(7); xf = xf == 2 * XST ? 0 : xf + XST; });
  }
  FST(11);
  BAR();
  FST(12);
  AS a;
  unsigned kb0, vb0;
  { const unsigned lane = lane_id(), r16 = lane & 15, g = lane >> 4, q4 = (lane & 15) >> 2, p = lane & 3;
    kb0 = c.ldsb + 128 * r16 + 16 * (g ^ ((r16 >> 1) & 7));
    vb0 = c.ldsb + SLOT + 2048 * (g >> 1) + 64 * (4 * (g & 1) + q4) + 16 * ((p >> 1) ^ (2 * (g & 1))) + 8 * (p & 1); }
  unsigned sl = 0;
#pragma nounroll
  for (int hd = 0; hd < 2; ++hd) {
#pragma unroll
    for (int rt = 0; rt < 3; ++rt) {
      const int t = 3 * wi + rt;
      const unsigned blk = hd ? Q2OFF + 4096 * t : (t < 8 ? 4 * SLOT + 4096 * t : t < 11 ? MISC + 4096 * (t - 8) : SLOT + 4096 * (t - 11));
      const unsigned q0 = kb0 + blk, q1 = q0 ^ 64;
      a.q[rt][0] = rd128(q0, 0); a.q[rt][1] = rd128(q1, 0); a.q[rt][2] = rd128(q0, 2048); a.q[rt][3] = rd128(q1, 2048);
      a.l[rt] = 0.f; a.P[0][rt] = u32x4{}; a.P[1][rt] = u32x4{};
    }
    a.kn[0] = kb0 + sl; a.kn[1] = a.kn[0] ^ 64;
    a_frag<8, 0>(a); a_frag<8, 1>(a); a_frag<8, 2>(a); a_frag<8, 3>(a);
    if (hd == 0) BAR();
    STAMP(2 + 3 * hd);
    auto setp = [&]() {
      a.kp[0] = a.kn[0]; a.kp[1] = a.kn[1]; a.vp[0] = vb0 + sl; a.vp[1] = a.vp[0] ^ 32;
      sl = sl == 4 * SLOT ? 0 : sl + 2 * SLOT;
      a.kn[0] = kb0 + sl; a.kn[1] = a.kn[0] ^ 64;
      SBAR();
    };
    setp();
    a_period<true>(a);
#pragma nounroll
    for (int t = 1; t < 16; ++t) { setp(); a_period<false>(a); }
    a.vp[0] = vb0 + sl; a.vp[1] = a.vp[0] ^ 32;
    SBAR();
    a_block<0, false, true, 1>(a); a_block<1, true, false, -1>(a);
    a_frag<3, 0>(a); a_frag<3, 1>(a); a_frag<3, 2>(a); a_frag<3, 3>(a);
    a_block<3, true, false, -1>(a);
    if (hd) BAR();
    STAMP(3 + 3 * hd);
    const rsrc_t ro = mkrs(c.oq + (size_t)(2 * c.own + hd) * OBLK, OBLK);
    const unsigned ov = 16 * lane_id() + 6144 * wi;
#pragma unroll
    for (int rt = 0; rt < 3; ++rt) {
      float l = red_add(a.l[rt]);
      if (__builtin_expect(__any(!(l + nanprobe(a.o[rt]) < L_LIMIT)), 0)) {
        float m;
        slow16(c.k + (size_t)hd * SK * DH, c.v + (size_t)hd * SK * DH, 0, 16, 64, a.q[rt], a.o[rt], m, l);
        l = red_add(l);
      }
      const float il = __builtin_amdgcn_rcpf(l);
#pragma unroll
      for (int kk = 0; kk < 4; ++kk) {
        const f32x4 u = a.o[rt][2 * kk] * il, v = a.o[rt][2 * kk + 1] * il;
        u32x4 w = {cvtpk(u[0], u[1]), cvtpk(u[2], u[3]), cvtpk(v[0], v[1]), cvtpk(v[2], v[3])};
        __builtin_amdgcn_raw_buffer_store_b128(w, ro, ov + 2048 * rt + 1024 * (kk & 1) + (OBLK / 2) * (kk >> 1), 0, 16);
      }
    }
    STAMP(4 + 3 * hd);
  }
  if (wi & 1) oproj<3, 6>(c, wi); else oproj<3, 7>(c, wi);
}

template <bool LATE> DI void path_b(const Ctx& c, int j) {
  STAMP(0);
  const rsrc_t rk = mkrs(c.k, 2 * SK * DH * 2), rv = mkrs(c.v, 2 * SK * DH * 2);
  unsigned vk0, vk1, vv;
  { const unsigned i = lane_id();
    vk0 = 256 * (i >> 3) + 16 * ((i & 7) ^ (i >> 4)); vk1 = (vk0 ^ 64) + 2048;
    vv = 256 * ((i >> 2) & 7) + 64 * (i >> 5) + 16 * ((i & 3) ^ (((i >> 4) & 1) << 1)); }
  auto kpc = [&](int t, unsigned slot, int n) {
    const unsigned m = c.ldsb + slot + 2048 * j, so = (unsigned)(64 * t + 16 * j) * 256u;
    if (n == 0) dma_nt<0>(m, vk0, rk, so); if (n == 1) dma_nt<128>(m + 8064, vk0, rk, so); if (n == 2) dma_nt<0>(m + 1024, vk1, rk, so); if (n == 3) dma_nt<128>(m + 9088, vk1, rk, so);
  };
  auto vpc = [&](int t, unsigned slot, int n) {
    const unsigned m = c.ldsb + slot + 4096 * j, so = (unsigned)(64 * t - 32 + 16 * j) * 256u;
    if (n == 0) dma_nt<0>(m, vv, rv, so); if (n == 1) dma_nt<128>(m + 896, vv, rv, so); if (n == 2) dma_nt<2048>(m, vv, rv, so); if (n == 3) dma_nt<2176>(m + 896, vv, rv, so);
  };
  auto kdma = [&](int t, unsigned slot) { kpc(t, slot, 0); kpc(t, slot, 1); kpc(t, slot, 2); kpc(t, slot, 3); };
  auto vdma = [&](int t, unsigned slot) { vpc(t, slot, 0); vpc(t, slot, 1); vpc(t, slot, 2); vpc(t, slot, 3); };
  {
    const rsrc_t rw = mkrs(c.wq, (DM - 1) * HD * 2 + 512);
    const unsigned i = lane_id(), vw0 = 8192 * ((i >> 2) & 7) + 64 * (i >> 5) + 16 * (i & 3), vw1 = (vw0 ^ 32) + 65536;
    auto wpc = [&](int it, unsigned slot, int n) {
      const unsigned m = c.ldsb + slot + 4096 * j, so = (unsigned)(64 * (it >> 1) + 16 * j) * 8192u + 256 * (it & 1);
      if (n == 0) dma_nt<0>(m, vw0, rw, so); if (n == 1) dma_nt<128>(m + 896, vw0, rw, so); if (n == 2) dma_nt<0>(m + 2048, vw1, rw, so); if (n == 3) dma_nt<128>(m + 2944, vw1, rw, so);
    };
#pragma unroll
    for (int it = 0; it < 5; ++it) { wpc(it, it * SLOT, 0); wpc(it, it * SLOT, 1); wpc(it, it * SLOT, 2); wpc(it, it * SLOT, 3); }
    unsigned f0 = SLOT, f1 = 2 * SLOT;
    auto adv = [](unsigned s_) { s_ += 2 * SLOT; return s_ >= 5 * SLOT ? s_ - 5 * SLOT : s_; };
    qproj<6>(c, j,
      [&](auto K, int s, int n) {
        constexpr int k = decltype(K)::value;
        if constexpr (k == 0) { if (n < 4) wpc(2 * s + 3, f0, n); else wpc(2 * s + 4, f1, n - 4); }
        if constexpr (k == 1) { if (n < 4) wpc(19, 4 * SLOT, n); else kpc(0, 0, n - 4); }
        if constexpr (k == 2) { if (n < 4) { if (LATE) vpc(0, SLOT, n); } else kpc(1, 2 * SLOT, n - 4); }
        if constexpr (k == 4) vdma(1, 3 * SLOT);
      },
      [&](int s) { if (s == 0) VMW(12); else VMW(4); f0 = adv(f0); f1 = adv(f1); });
  }
  VMW(8);
  BAR();
  unsigned qb, kb0, vb0;
  { const unsigned lane = lane_id(), r16 = lane & 15, g = lane >> 4, q4 = (lane & 15) >> 2, p = lane & 3;
    qb = c.ldsb + 128 * r16 + 16 * (g ^ ((r16 >> 1) & 7));
    kb0 = qb + 2048 * j;
    vb0 = c.ldsb + SLOT + 4096 * ((j + 2) & 3) + 2048 * (g >> 1) + 64 * (4 * (g & 1) + q4) + 16 * ((p >> 1) ^ (2 * (g & 1))) + 8 * (p & 1); }
  bf16x8 q[4]; f32x4 o[8], os[8], negm = {}; float l = 0.f, ms = 0.f, ls = 0.f; u32x2 P = {};
  unsigned sl = 0, fr = 4 * SLOT;
  auto rdq = [&](unsigned blk) { const unsigned q0 = qb + blk, q1 = q0 ^ 64; q[0] = rd128(q0, 0); q[1] = rd128(q1, 0); q[2] = rd128(q0, 2048); q[3] = rd128(q1, 2048); };
  auto pv = [&]() {
    const unsigned v0 = vb0 + sl, v1 = v0 ^ 32;
    s16x4 vf[8];
#pragma unroll
    for (int cc = 0; cc < 8; ++cc) vf[cc] = rdtr((cc & 1) ? v1 : v0, 512 * (cc >> 1));
#pragma unroll
    for (int cc = 0; cc < 8; ++cc) o[cc] = MFMA16K16(vf[cc], __builtin_bit_cast(s16x4, P), o[cc]);
  };
  auto qk = [&](auto F) {
    constexpr bool FIRST = decltype(F)::value;
    const unsigned k0 = kb0 + sl, k1 = k0 ^ 64;
    const bf16x8 f0 = rd128(k0, 0), f1 = rd128(k1, 0), f2 = rd128(k0, 8192), f3 = rd128(k1, 8192);
    f32x4 S;
    if constexpr (FIRST) S = MFMA16(f0, q[0], f32x4{}); else S = MFMA16(f0, q[0], negm);
    S = MFMA16(f1, q[1], S); S = MFMA16(f2, q[2], S); S = MFMA16(f3, q[3], S);
    if constexpr (FIRST) { const float mx = red_max(fmaxf(fmaxf(S[0], S[1]), fmaxf(S[2], S[3]))); negm = f32x4{-mx, -mx, -mx, -mx}; S -= mx; }
#pragma unroll
    for (int i = 0; i < 4; ++i) { S[i] = ex2(S[i]); l += S[i]; PIN(l); }
    P = u32x2{cvtpk(S[0], S[1]), cvtpk(S[2], S[3])};
  };
  auto finish = [&](int hd) {
    ls = red_add(l); ms = -negm[0];
    if (__builtin_expect(__any(!(ls + nanprobe(o) < L_LIMIT)), 0)) {
      slow16(c.k + (size_t)hd * SK * DH, c.v + (size_t)hd * SK * DH, 16 * j, 64, 16, q, o, ms, ls);
      ls = red_add(ls);
    }
#pragma unroll
    for (int cc = 0; cc < 8; ++cc) { os[cc] = o[cc]; o[cc] = f32x4{}; }
    l = 0.f;
  };
  auto put = [&](unsigned scr) {
    const unsigned lane = lane_id(), sc = c.ldsb + scr + 16 * lane + 8192 * j;
#pragma unroll
    for (int cc = 0; cc < 8; ++cc) wr128f(sc, 1024 * cc, os[cc]);
    if (lane < 16) { *(LAS float*)(c.ldsb + scr + 32768 + 128 * j + 4 * lane) = ms; *(LAS float*)(c.ldsb + scr + 32768 + 128 * j + 64 + 4 * lane) = ls; }
  };
  auto merge = [&](int hd, unsigned scr) {
    const unsigned lane = lane_id(), sc = c.ldsb + scr + 16 * lane + 2048 * j, ml = c.ldsb + scr + 32768 + 4 * (lane & 15);
    float mw[4], lw[4];
#pragma unroll
    for (int w = 0; w < 4; ++w) { mw[w] = *(const LAS float*)(ml + 128 * w); lw[w] = *(const LAS float*)(ml + 128 * w + 64); }
    const float mt = fmaxf(fmaxf(mw[0], mw[1]), fmaxf(mw[2], mw[3]));
    float lt = 0.f;
#pragma unroll
    for (int w = 0; w < 4; ++w) { mw[w] = ex2(mw[w] - mt); lt += mw[w] * lw[w]; }
    const float ir = __builtin_amdgcn_rcpf(lt);
    f32x4 u = {}, v = {};
#pragma unroll
    for (int w = 0; w < 4; ++w) { u += rd128f(sc, 8192 * w) * (mw[w] * ir); v += rd128f(sc, 8192 * w + 1024) * (mw[w] * ir); }
    u32x4 w4 = {cvtpk(u[0], u[1]), cvtpk(u[2], u[3]), cvtpk(v[0], v[1]), cvtpk(v[2], v[3])};
    __builtin_amdgcn_raw_buffer_store_b128(w4, mkrs(c.oq + (size_t)(2 * c.own + hd) * OBLK, OBLK), 16 * lane + 12 * 2048 + 1024 * (j & 1) + (OBLK / 2) * (j >> 1), 0, 16);
  };
  auto rot = [&]() { fr = sl; sl = sl == 4 * SLOT ? 0 : sl + 2 * SLOT; };
  auto period = [&](int t, auto&& extra) {
    kdma(t + 2, fr); vdma(t + 2, fr + SLOT);
    if constexpr (LATE) pv();
    extra();
    qk(ic<0>{});
    if constexpr (!LATE) pv();
    VMW(8); BAR(); rot();
  };
  auto none = []() {};
#pragma unroll
  for (int cc = 0; cc < 8; ++cc) o[cc] = f32x4{};
  rdq(q1addr(12));
  __builtin_amdgcn_s_setprio(3);
  BAR();
  kdma(2, fr); vdma(2, fr + SLOT); qk(ic<1>{}); if constexpr (!LATE) pv(); VMW(8); BAR(); rot();
  {
    unsigned w0, w1;
    const char* p = c.warm + 10240 * j + 128 * lane_id();
    asm volatile("global_load_dword %0, %1, off" : "=v"(w0) : "v"(p) : "memory");
    asm volatile("global_load_dword %0, %1, off offset:2048" : "=v"(w1) : "v"(p) : "memory");
    period(1, none);
    asm volatile("" ::"v"(w0), "v"(w1));
  }
#pragma nounroll
  for (int t = 2; t < 16; ++t) period(t, none);
  kdma(18, fr); vdma(18, fr + SLOT);
  if constexpr (LATE) pv();
  finish(0); rdq(Q2OFF + 12 * 4096);
  qk(ic<1>{}); if constexpr (!LATE) pv(); VMW(8); BAR(); rot();
  period(17, [&]() { put(Q2OFF); });
  period(18, [&]() { merge(0, Q2OFF); });
  period(19, none);
  if (j == 0) signal_quad(c.cnt);
#pragma nounroll
  for (int t = 20; t < 27; ++t) period(t, none);
  if (j == 0) wait_quad(c.cnt);
  period(27, none);
  const OD d = od_make(c, j);
  {
#pragma unroll
    for (int n = 0; n < 12; ++n) od_piece(c, d, od_blk(0), PS0, n);
  }
  period(28, none);
  period(29, none);
  if constexpr (!LATE) vdma(32, fr + SLOT);
  if constexpr (LATE) pv(); qk(ic<0>{}); if constexpr (!LATE) pv();
  if constexpr (LATE) VMW(0); else VMW(4);
  BAR(); rot();
  if constexpr (LATE) pv(); qk(ic<0>{}); if constexpr (!LATE) pv();
  VMW(0); BAR(); rot();
#pragma unroll
  for (int n = 0; n < 12; ++n) od_piece(c, d, od_blk(1), PS1, n);
  if constexpr (LATE) pv();
  finish(1);
  put(SCR2);
  BAR();
  merge(1, SCR2);
  __builtin_amdgcn_s_setprio(0);
  if (j & 1) oproj<2, 7>(c, j); else oproj<2, 6>(c, j);
}

__global__ __launch_bounds__(512, 2) void fused_k(Args ar) {
  extern __shared__ __attribute__((aligned(16))) char lds[];
  const int wid = __builtin_amdgcn_readfirstlane(threadIdx.x >> 6);
  const int xcd = blockIdx.x & 7, idx = blockIdx.x >> 3, br = xcd >> 2, b = 2 * (xcd & 3) + (idx >> 4), hp = idx & 15, kq = hp >> 2;
  Ctx c;
  c.ldsb = (unsigned)(size_t)(LAS char*)lds;
  c.x = (br ? ar.x1 : ar.x0) + (size_t)b * SQ * DM;
  c.k = (br ? ar.k1 : ar.k0) + ((size_t)b * H + 2 * hp) * SK * DH;
  c.v = (br ? ar.v1 : ar.v0) + ((size_t)b * H + 2 * hp) * SK * DH;
  c.wq = (br ? ar.wq1 : ar.wq0) + 2 * hp * DH;
  const char* wo = (const char*)(br ? ar.wo1 : ar.wo0);
  c.wo = wo + (size_t)kq * 1024 * DM * 2;
  c.warm = wo + (size_t)((xcd & 3) * 32 + idx) * 40960;
  c.oq = ar.ws + WS_O + (size_t)((br * NB + b) * H + 8 * kq) * OBLK;
  c.part = ar.ws + WS_PART + (size_t)blockIdx.x * (130 * 512);
  c.cnt = (unsigned*)(ar.ws + WS_CNT) + ((br * NB + b) * 4 + kq) * 64;
  c.stp = (unsigned long long*)(ar.ws + WS_STAMP) + (blockIdx.x * 8 + wid) * 32;
  c.own = hp & 3; c.nt = hp & 3;
  if (wid < 4) path_a(c, wid);
  else if (wid < 6) path_b<false>(c, wid - 4);
  else path_b<true>(c, wid - 4);
}

__global__ __launch_bounds__(512) void reduce_k(const char* __restrict__ part, u16* __restrict__ out) {
  const int xcd = blockIdx.x & 7, idx = blockIdx.x >> 3, br = xcd >> 2, b = 2 * (xcd & 3) + (idx >> 4), nt = idx & 3, kq = (idx >> 2) & 3;
  const int lane = threadIdx.x & 63, w = threadIdx.x >> 6;
  const char* pb = part + (size_t)(xcd + 8 * (idx & ~12)) * (130 * 512) + 8 * lane;
  u32x2 v[5][4];
#pragma unroll
  for (int n = 0; n < 5; ++n) {
    const int t = kq + 4 * (w + 8 * n), tt = t < 130 ? t : 0;
#pragma unroll
    for (int q = 0; q < 4; ++q) v[n][q] = *(const u32x2*)(pb + (size_t)q * 32 * (130 * 512) + tt * 512);
  }
#pragma unroll
  for (int n = 0; n < 5; ++n) {
    const int t = kq + 4 * (w + 8 * n);
    if (t < 130) {
      float s0 = 0.f, s1 = 0.f, s2 = 0.f, s3 = 0.f;
#pragma unroll
      for (int q = 0; q < 4; ++q) {
        s0 += __uint_as_float(v[n][q][0] << 16); s1 += __uint_as_float(v[n][q][0] & 0xffff0000u);
        s2 += __uint_as_float(v[n][q][1] << 16); s3 += __uint_as_float(v[n][q][1] & 0xffff0000u);
      }
      const int rt = t / 10, ct = t % 10;
      const int col = 160 * nt + (ct < 8 ? ((nt & 1) ? 32 : 0) + 16 * ct : ((nt & 1) ? 0 : 128) + 16 * (ct - 8)) + 4 * (lane >> 4);
      *(u32x2*)(out + (size_t)(b * SQ + 16 * rt + (lane & 15)) * (2 * DM) + br * DM + col) = u32x2{cvtpk(s0, s1), cvtpk(s2, s3)};
    }
  }
}
}

extern "C" void kernel_launch(void* const* d_in, const int* in_sizes, int n_in, void* d_out, int out_size, void* d_ws, size_t ws_size, hipStream_t stream) {
  using namespace kk;
  Args a;
  a.x0 = (const u16*)d_in[0]; a.k0 = (const u16*)d_in[1]; a.v0 = (const u16*)d_in[2]; a.wq0 = (const u16*)d_in[3]; a.wo0 = (const u16*)d_in[4];
  a.x1 = (const u16*)d_in[5]; a.k1 = (const u16*)d_in[6]; a.v1 = (const u16*)d_in[7]; a.wq1 = (const u16*)d_in[8]; a.wo1 = (const u16*)d_in[9];
  a.ws = (char*)d_ws;
  (void)hipFuncSetAttribute((const void*)fused_k, hipFuncAttributeMaxDynamicSharedMemorySize, (int)LDS_TOTAL);
  (void)hipMemsetAsync((char*)d_ws + WS_CNT, 0, CNT_BYTES, stream);
  fused_k<<<256, 512, LDS_TOTAL, stream>>>(a);
  reduce_k<<<256, 512, 0, stream>>>((const char*)d_ws + WS_PART, (u16*)d_out);
}
```

```cpp
#define PACE 110
#include <hip/hip_runtime.h>
#include <hip/hip_bf16.h>
#include <utility>

namespace kk {
typedef unsigned short u16;
typedef short bf16x8 __attribute__((ext_vector_type(8)));
typedef short s16x4 __attribute__((ext_vector_type(4)));
typedef float f32x4 __attribute__((ext_vector_type(4)));
typedef float f32x2 __attribute__((ext_vector_type(2)));
typedef __bf16 bf16x2 __attribute__((ext_vector_type(2)));
typedef unsigned u32x4 __attribute__((ext_vector_type(4)));
typedef unsigned u32x2 __attribute__((ext_vector_type(2)));
typedef __amdgpu_buffer_rsrc_t rsrc_t;
typedef __attribute__((address_space(1))) unsigned gu32;
#define LAS __attribute__((address_space(3)))
#define DI __device__ __forceinline__
#define SBAR() __builtin_amdgcn_sched_barrier(0)
#define PIN(x) asm volatile("" : "+v"(x))
#define MFMA16(a, b, c) __builtin_amdgcn_mfma_f32_16x16x32_bf16(a, b, c, 0, 0, 0)
#define MFMA16K16(a, b, c) __builtin_amdgcn_mfma_f32_16x16x16bf16_1k(a, b, c, 0, 0, 0)
#define LGKM0() __builtin_amdgcn_s_waitcnt(0xC07F)
#define VMW(N) asm volatile("s_waitcnt vmcnt(" #N ")" ::: "memory")
#define BAR() do { LGKM0(); asm volatile("s_barrier" ::: "memory"); } while (0)
#define MFMA_DONE() do { asm volatile("s_nop 7\n\ts_nop 3" ::: "memory"); SBAR(); } while (0)
template <int... Is> using iseq = std::integer_sequence<int, Is...>;
template <int N> using mkseq = std::make_integer_sequence<int, N>;
template <int N> using ic = std::integral_constant<int, N>;

constexpr int NB = 8, SQ = 208, DM = 640, H = 32, SK = 1024, DH = 128, HD = H * DH, NRT = 13;
constexpr unsigned SLOT = 16384, XST = 26624, XOFF = 5 * SLOT, Q2OFF = 6 * SLOT, MISC = Q2OFF + NRT * 4096, LDS_TOTAL = 163840;
static_assert(XOFF + 3 * XST <= LDS_TOTAL && MISC + 3 * 4096 == LDS_TOTAL, "lds map");
constexpr unsigned OBLK = NRT * 4096;
constexpr unsigned PST = 47104, POFF = 26624;
constexpr unsigned PS0 = LDS_TOTAL - PST, PS1 = 0, PS2 = PST, SCR2 = PST;
static_assert(SCR2 + 32768 + 512 <= 5 * SLOT && PS2 + PST <= PS0, "lds map 2");
constexpr size_t WS_CNT = 0, CNT_BYTES = 16384, WS_O = 1u << 20, WS_PART = 32u << 20, WS_STAMP = 64u << 20;
constexpr float QSCALE = 0.08838834764831845f * 1.4426950408889634f;
constexpr float L_LIMIT = 1.0e24f;
__host__ __device__ constexpr unsigned q1addr(int rt) { return rt < 8 ? 4 * SLOT + 4096 * rt : rt < 11 ? MISC + 4096 * (rt - 8) : SLOT + 4096 * (rt - 11); }

DI unsigned cvtpk(float lo, float hi) { f32x2 v = {lo, hi}; return __builtin_bit_cast(unsigned, __builtin_convertvector(v, bf16x2)); }
DI float fmul_s(float a, float b) { float r; asm("v_mul_f32_e32 %0, %1, %2" : "=v"(r) : "s"(b), "v"(a)); return r; }
DI float ex2(float x) { return __builtin_amdgcn_exp2f(x); }
DI bf16x8 rd128(unsigned base, int imm) { return *(const LAS bf16x8*)((const LAS char*)base + imm); }
DI s16x4 rdtr(unsigned base, int imm) { return __builtin_amdgcn_ds_read_tr16_b64_v4i16((LAS s16x4*)((LAS char*)base + imm)); }
DI bf16x8 cat(s16x4 lo, s16x4 hi) { return __builtin_shufflevector(lo, hi, 0, 1, 2, 3, 4, 5, 6, 7); }
DI void wr128f(unsigned base, int imm, f32x4 v) { *(LAS f32x4*)((LAS char*)base + imm) = v; }
DI f32x4 rd128f(unsigned base, int imm) { return *(const LAS f32x4*)((const LAS char*)base + imm); }
DI float red_add(float x) {
  auto a = __builtin_amdgcn_permlane16_swap(__float_as_uint(x), __float_as_uint(x), false, false); x = __uint_as_float(a[0]) + __uint_as_float(a[1]);
  auto b = __builtin_amdgcn_permlane32_swap(__float_as_uint(x), __float_as_uint(x), false, false); return __uint_as_float(b[0]) + __uint_as_float(b[1]); }
DI float red_max(float x) {
  auto a = __builtin_amdgcn_permlane16_swap(__float_as_uint(x), __float_as_uint(x), false, false); x = fmaxf(__uint_as_float(a[0]), __uint_as_float(a[1]));
  auto b = __builtin_amdgcn_permlane32_swap(__float_as_uint(x), __float_as_uint(x), false, false); return fmaxf(__uint_as_float(b[0]), __uint_as_float(b[1])); }
DI int lane_id() { int l; asm volatile("v_mbcnt_lo_u32_b32 %0, -1, 0\n\tv_mbcnt_hi_u32_b32 %0, -1, %0" : "=v"(l)); return l; }
DI rsrc_t mkrs(const void* p, unsigned bytes) { return __builtin_amdgcn_make_buffer_rsrc((void*)p, 0, bytes, 0x00020000); }

template <int IMM> DI void dma_nt(unsigned m0v, unsigned voff, rsrc_t rs, unsigned soff) {
  asm volatile("s_mov_b32 m0, %0\n\ts_nop 0\n\tbuffer_load_dwordx4 %1, %2, %3 offen offset:%4 nt lds" ::"s"(m0v), "v"(voff), "s"(rs), "s"(soff), "i"(IMM) : "memory");
}
DI void dma_df(unsigned m0v, unsigned voff, rsrc_t rs, unsigned soff) {
  asm volatile("s_mov_b32 m0, %0\n\ts_nop 0\n\tbuffer_load_dwordx4 %1, %2, %3 offen lds" ::"s"(m0v), "v"(voff), "s"(rs), "s"(soff) : "memory");
}
DI void touch80(unsigned& w0, unsigned& w1, const char* p) {
  const unsigned vo = 128 * lane_id();
  asm volatile("s_nop 4\n\tglobal_load_dword %0, %2, %3\n\tglobal_load_dword %1, %2, %3 offset:2048" : "=&v"(w0), "=&v"(w1) : "v"(vo), "s"(p) : "memory");
}
#define OAUX 16
#define XDMA dma_df
DI void mfma2(f32x4& c, const bf16x8& a0, const bf16x8& b0, const bf16x8& a1, const bf16x8& b1) {
  asm volatile("v_mfma_f32_16x16x32_bf16 %0, %1, %2, %0\n\tv_mfma_f32_16x16x32_bf16 %0, %3, %4, %0" : "+v"(c) : "v"(a0), "v"(b0), "v"(a1), "v"(b1));
}
DI void poll_issue(unsigned& v, const unsigned* cnt) { const unsigned z = 0; asm volatile("s_nop 4\n\tglobal_load_dword %0, %1, %2 sc1" : "=v"(v) : "v"(z), "s"(cnt) : "memory"); }
DI void poll_acquire(unsigned v, const unsigned* cnt) {
  asm volatile("" : "+v"(v)::"memory");
  if (__builtin_amdgcn_readfirstlane(v) < 4u) {
#pragma nounroll
    for (int spin = 0; spin < (1 << 22); ++spin) {
      const unsigned u = __hip_atomic_load((gu32*)cnt, __ATOMIC_RELAXED, __HIP_MEMORY_SCOPE_AGENT);
      if (__builtin_amdgcn_readfirstlane(u) >= 4u) break;
      __builtin_amdgcn_s_sleep(2);
    }
  }
  asm volatile("buffer_inv sc1" ::: "memory");
}
DI void signal_quad(unsigned* cnt) { if (lane_id() == 0) __hip_atomic_fetch_add((gu32*)cnt, 1u, __ATOMIC_RELAXED, __HIP_MEMORY_SCOPE_AGENT); }

#define STAMP(i)
#define FST(i)
#define PST_(i)
struct Args { const u16 *x0, *k0, *v0, *wq0, *wo0, *x1, *k1, *v1, *wq1, *wo1; char* ws; };
struct Ctx {
  const u16 *x, *k, *v, *wq; const char *wo, *warm; char *oq, *part; unsigned* cnt; unsigned long long* stp; unsigned ldsb; int own, nt;
};

DI void slow16(const u16* Kg, const u16* Vg, int key0, int kstride, int nstep, const bf16x8 (&q)[4], f32x4 (&o)[8], float& m, float& l) {
  const int lane = lane_id(), r = lane & 15, g = lane >> 4;
  m = -3.0e38f; l = 0.f;
#pragma unroll
  for (int d = 0; d < 8; ++d) o[d] = f32x4{};
#pragma nounroll
  for (int st = 0; st < nstep; ++st) {
    const int kb = key0 + st * kstride;
    f32x4 S = {};
#pragma unroll
    for (int ks = 0; ks < 4; ++ks) S = MFMA16(*(const bf16x8*)(Kg + (size_t)(kb + r) * DH + 32 * ks + 8 * g), q[ks], S);
    const float mx = red_max(fmaxf(fmaxf(S[0], S[1]), fmaxf(S[2], S[3])));
    const float mn = fmaxf(m, mx), al = ex2(m - mn); m = mn;
    float ps = 0.f;
#pragma unroll
    for (int i = 0; i < 4; ++i) { S[i] = ex2(S[i] - mn); ps += S[i]; }
    l = l * al + ps;
    u32x2 pw = {cvtpk(S[0], S[1]), cvtpk(S[2], S[3])};
#pragma unroll
    for (int d = 0; d < 8; ++d) {
      s16x4 vf;
#pragma unroll
      for (int j = 0; j < 4; ++j) vf[j] = (short)Vg[(size_t)(kb + 4 * g + j) * DH + 16 * d + r];
      o[d] *= al;
      o[d] = MFMA16K16(vf, __builtin_bit_cast(s16x4, pw), o[d]);
    }
  }
  __builtin_amdgcn_s_waitcnt(0);
}
DI float nanprobe(const f32x4 (&o)[8]) {
  f32x4 t = {};
#pragma unroll
  for (int d = 0; d < 8; ++d) t += o[d] * 0.f;
  return (t[0] + t[1]) + (t[2] + t[3]);
}

template <int NR, class FD, class FW> DI void qproj(const Ctx& c, int cg, FD&& dma, FW&& wait) {
  f32x4 acc[NR][4]; bf16x8 w[4][2], x[NR][2];
  const unsigned lane = lane_id(), r16 = lane & 15, g = lane >> 4, q4 = (lane & 15) >> 2, p = lane & 3;
  const unsigned wb0 = c.ldsb + 1024 * (cg & 1) + 2048 * g + 64 * q4 + 16 * ((p >> 1) ^ (2 * (g & 1))) + 8 * (p & 1);
  const unsigned xb0 = c.ldsb + XOFF + (NR == 7 ? 0 : 7 * 2048) + 128 * r16 + 16 * (g ^ ((r16 >> 1) & 7));
#pragma unroll
  for (int r = 0; r < NR; ++r)
#pragma unroll
    for (int i = 0; i < 4; ++i) acc[r][i] = f32x4{};
  unsigned xs = 0, ws = SLOT * (cg >> 1), xp0, xp1, wp0, wp1;
  auto ldw = [&](int i) { const unsigned b = (i & 1) ? wp1 : wp0; const int im = 512 * (i >> 1);
    w[i][0] = cat(rdtr(b, im), rdtr(b, im + 256)); w[i][1] = cat(rdtr(b, im + 8192), rdtr(b, im + 8448)); };
  auto ldx = [&](int r) { x[r][0] = rd128(xp0, 2048 * r); x[r][1] = rd128(xp1, 2048 * r); };
  auto row = [&](int r) {
#pragma unroll
    for (int i = 0; i < 4; ++i) mfma2(acc[r][i], w[i][0], x[r][0], w[i][1], x[r][1]);
  };
  auto step = [&](auto K, auto F, int s) {
    constexpr bool FIRST = decltype(F)::value;
    wait(s); BAR();
    FST(s);
    if constexpr (FIRST) { STAMP(1); }
    xp0 = xb0 + xs; xp1 = xp0 ^ 64; wp0 = wb0 + ws; wp1 = wp0 ^ 32;
    SBAR();
    if constexpr (NR == 7) {
#pragma unroll
      for (int r = 0; r < 7; ++r) { if constexpr (!FIRST) row(r); SBAR(); ldx(r); if (r & 1) dma(K, s, r >> 1); SBAR(); }
#pragma unroll
      for (int i = 0; i < 4; ++i) { ldw(i); dma(K, s, 3 + i); SBAR(); }
    } else {
#pragma unroll
      for (int i = 0; i < 4; ++i) { dma(K, s, i); ldw(i); SBAR(); }
#pragma unroll
      for (int r = 0; r < 6; ++r) { if (r == 2) dma(K, s, 4); ldx(r); SBAR(); }
#pragma unroll
      for (int r = 0; r < 6; ++r) { row(r); SBAR(); if ((r & 1) == 0) dma(K, s, 5 + (r >> 1)); SBAR(); }
    }
    xs = xs == 2 * XST ? 0 : xs + XST; ws += 2 * SLOT; ws = ws >= 5 * SLOT ? ws - 5 * SLOT : ws;
  };
  asm volatile("s_nop 1" ::: "memory");
  step(ic<NR == 7 ? 0 : 3>{}, ic<1>{}, 0);
#pragma nounroll
  for (int s = 1; s < 8; ++s) step(ic<0>{}, ic<0>{}, s);
  step(ic<1>{}, ic<0>{}, 8);
  step(ic<2>{}, ic<0>{}, 9);
  BAR();
  FST(10);
  dma(ic<4>{}, 10, 0);
  const unsigned ln = lane_id(), rr = ln & 15, gg = ln >> 4;
  const unsigned qw = c.ldsb + 2048 * (cg & 1) + 128 * rr + 16 * ((gg >> 1) ^ ((rr >> 1) & 7)) + 8 * (gg & 1);
  auto put = [&](int r) {
    const int rt = (NR == 7 ? 0 : 7) + r;
    const unsigned blk = (cg >> 1) ? Q2OFF + 4096 * rt : q1addr(rt);
#pragma unroll
    for (int i = 0; i < 4; ++i) {
      const f32x4 v = acc[r][i];
      *(LAS u32x2*)((qw ^ (32 * i)) + blk) = u32x2{cvtpk(fmul_s(v[0], QSCALE), fmul_s(v[1], QSCALE)), cvtpk(fmul_s(v[2], QSCALE), fmul_s(v[3], QSCALE))};
    }
  };
  SBAR();
  if constexpr (NR == 7) {
#pragma unroll
    for (int r = 0; r < 7; ++r) { row(r); SBAR(); if (r) put(r - 1); SBAR(); }
    MFMA_DONE();
    put(6);
  } else {
    MFMA_DONE();
#pragma unroll
    for (int r = 0; r < 6; ++r) put(r);
  }
}

struct OD { rsrc_t rw, ro; unsigned vwp, vwo, vo, mw, mo, lo; int rg; };
DI OD od_make(const Ctx& c, int rg) {
  OD d; const unsigned lane = lane_id(), xr = 16 * ((lane & 3) ^ (((lane >> 4) & 1) << 1));
  d.rw = mkrs(c.wo, 1024 * DM * 2); d.ro = mkrs(c.oq, 8 * OBLK);
  d.vwp = 1280 * (16 * rg + ((lane >> 2) & 7)) + 64 * (lane >> 5) + xr + 320 * c.nt + ((c.nt & 1) ? 64 : 0);
  d.vwo = 1280 * (16 * rg + (lane >> 2)) + xr + 320 * c.nt + ((c.nt & 1) ? 0 : 256);
  d.vo = 16 * lane; d.mw = c.ldsb + POFF + 5120 * rg; d.mo = c.ldsb + 1024 * rg; d.lo = 1024 * (24 + (rg & 1)); d.rg = rg;
  return d;
}
DI unsigned od_blk(const Ctx& c, int s) { return (unsigned)(s < 16 ? 4 * (((s >> 1) + c.own) & 3) + 2 * (s >> 3) + (s & 1) : 64); }
DI void od_piece(const Ctx& c, const OD& d, unsigned u, unsigned st, int n) {
  const unsigned sw = u * (64 * 1280), so = u * (OBLK / 2), m = d.mw + st;
  if (n == 0) dma_df(m, d.vwp, d.rw, sw);
  else if (n == 1) dma_df(m + 1024, d.vwp, d.rw, sw + 128);
  else if (n == 2) dma_df(m + 3072, d.vwp, d.rw, sw + 10240);
  else if (n == 3) dma_df(m + 4096, d.vwp, d.rw, sw + 10240 + 128);
  else if (n == 4) dma_df(m + 2048, d.vwo, d.rw, sw);
  else if (n < 11) dma_df(d.mo + st + 4096 * (n - 5), d.vo, d.ro, so + 1024 * d.rg + 4096 * (n - 5));
  else dma_df(c.ldsb + d.lo + st, d.vo, d.ro, so + d.lo);
}
template <int NC, int NR> DI void oproj(const Ctx& c, int rg) {
  const OD d = od_make(c, rg);
  f32x4 acc[NR][NC]; bf16x8 w[NC][2], x[NR][2];
  const int c0 = NC == 3 ? 3 * (rg >> 1) : 6 + 2 * (rg >> 1), r0 = NR == 7 ? 0 : 7;
  unsigned wb[NC], xb;
  {
    const unsigned lane = lane_id(), g = lane >> 4, q4 = (lane & 15) >> 2, p = lane & 3;
    const unsigned in = 64 * (4 * (g & 1) + q4) + 16 * ((p >> 1) ^ (2 * (g & 1))) + 8 * (p & 1);
#pragma unroll
    for (int i = 0; i < NC; ++i) {
      const int ct = c0 + i;
      const unsigned off = ct < 8 ? 512 * (ct >> 1) + 3072 * (g >> 1) : 2048 + 512 * (g >> 1);
      wb[i] = c.ldsb + POFF + off + (in ^ (32 * (ct & 1)));
    }
    xb = c.ldsb + 16 * lane + 2048 * r0;
  }
#pragma unroll
  for (int r = 0; r < NR; ++r)
#pragma unroll
    for (int i = 0; i < NC; ++i) acc[r][i] = f32x4{};
  unsigned st = PS0, nx = PS1, fr = PS2;
  auto ldw = [&](int i) { const unsigned wp = wb[i] + st; w[i][0] = cat(rdtr(wp, 0), rdtr(wp, 5120)); w[i][1] = cat(rdtr(wp, 10240), rdtr(wp, 15360)); };
  auto ldx = [&](int r) { const unsigned xp = xb + st; x[r][0] = rd128(xp, 2048 * r); x[r][1] = rd128(xp, 2048 * r + 1024); };
  auto row = [&](int r) {
#pragma unroll
    for (int i = 0; i < NC; ++i) mfma2(acc[r][i], w[i][0], x[r][0], w[i][1], x[r][1]);
  };
  auto step = [&](auto F, int s) {
    constexpr bool FIRST = decltype(F)::value;
    const unsigned u = od_blk(c, s + 2);
    SBAR();
    if constexpr (NC == 3) {
#pragma unroll
      for (int r = 0; r < NR; ++r) { if constexpr (!FIRST) row(r); SBAR(); ldx(r); if (r == 1 || r == 4) od_piece(c, d, u, fr, r / 3); SBAR(); }
#pragma unroll
      for (int i = 0; i < 3; ++i) { ldw(i); if (i) od_piece(c, d, u, fr, 1 + i); SBAR(); }
    } else {
#pragma unroll
      for (int i = 0; i < 2; ++i) { od_piece(c, d, u, fr, 4 + i); ldw(i); SBAR(); }
#pragma unroll
      for (int r = 0; r < NR; ++r) { if ((r & 1) == 0 && r < 6) od_piece(c, d, u, fr, 6 + (r >> 1)); ldx(r); SBAR(); }
#pragma unroll
      for (int r = 0; r < NR; ++r) { row(r); SBAR(); if ((r & 1) == 0 && r < 6) od_piece(c, d, u, fr, 9 + (r >> 1)); SBAR(); }
    }
    const unsigned t = st; st = nx; nx = fr; fr = t;
  };
  int fi = 14;
  auto sync = [&]() { if constexpr (NC == 3) VMW(4); else VMW(8); BAR(); FST(fi); ++fi; };
  asm volatile("s_nop 1" ::: "memory");
  if constexpr (NC == 2) VMW(13);
  BAR();
  STAMP(8); FST(13);
  step(ic<1>{}, 0);
  sync();
  if (NC == 2 && rg == 0) signal_quad(c.cnt + 32);
  if constexpr (NC == 3 && NR == 6) {
#pragma nounroll
    for (int s = 1; s < 5; ++s) { step(ic<0>{}, s); sync(); }
    unsigned pv = 4;
    if (rg == 1) poll_issue(pv, c.cnt + 32);
    step(ic<0>{}, 5);
    VMW(4);
    if (rg == 1) poll_acquire(pv, c.cnt + 32);
    asm volatile("" ::"v"(pv));
    BAR();
    step(ic<0>{}, 6);
    if (rg == 1) VMW(5); else VMW(4);
    BAR();
    step(ic<0>{}, 7);
    if (rg == 1) VMW(0); else VMW(4);
    BAR();
#pragma nounroll
    for (int s = 8; s < 15; ++s) { step(ic<0>{}, s); sync(); }
  } else {
#pragma nounroll
    for (int s = 1; s < 15; ++s) { step(ic<0>{}, s); sync(); }
  }
  STAMP(9);
  step(ic<0>{}, 15);
  if constexpr (NC == 3) {
    LGKM0(); SBAR();
#pragma unroll
    for (int r = 0; r < NR; ++r) row(r);
  }
  MFMA_DONE();
  STAMP(10);
  char* pb = c.part + 8 * lane_id();
#pragma unroll
  for (int r = 0; r < NR; ++r)
#pragma unroll
    for (int i = 0; i < NC; ++i) { const f32x4 v = acc[r][i]; *(u32x2*)(pb + ((r0 + r) * 10 + c0 + i) * 512) = u32x2{cvtpk(v[0], v[1]), cvtpk(v[2], v[3])}; }
  STAMP(11);
}

struct AS {
  f32x4 o[3][8], S[2][3], negm[3]; bf16x8 q[3][4], F[2][4]; u32x4 P[2][3]; float l[3];
  unsigned kp[2], vp[2], kn[2];
};
template <int N, int I> DI void a_frag(AS& a) {
  constexpr int sub = (N >> 2) & 1, b = N & 3;
  if constexpr ((b & 1) == 0) {
    constexpr int kt = 2 * sub + (b >> 1);
    a.F[N & 1][I] = rd128(N == 8 ? a.kn[I & 1] : a.kp[I & 1], 8192 * (I >> 1) + 2048 * kt);
  } else {
    constexpr int cc = 4 * (b >> 1) + I, imm = 8192 * sub + 512 * (cc >> 1);
    a.F[N & 1][I] = cat(rdtr(a.vp[cc & 1], imm), rdtr(a.vp[cc & 1], imm + 4096));
  }
}
template <int N, bool MM, bool SM, int NX, bool Z, int G> DI void a_gap(AS& a) {
  constexpr int sub = N >> 2, b = N & 3;
  if constexpr (MM) {
    if constexpr ((b & 1) == 0) {
      constexpr int rt = G >> 2, ks = G & 3, sb = b >> 1;
      if constexpr (ks == 0) { if constexpr (Z) a.S[sb][rt] = MFMA16(a.F[N & 1][0], a.q[rt][0], f32x4{}); else a.S[sb][rt] = MFMA16(a.F[N & 1][0], a.q[rt][0], a.negm[rt]); }
      else a.S[sb][rt] = MFMA16(a.F[N & 1][ks], a.q[rt][ks], a.S[sb][rt]);
    } else {
      constexpr int i = G / 3, rt = G % 3, cc = 4 * (b >> 1) + i;
      if constexpr (Z) a.o[rt][cc] = MFMA16(a.F[N & 1][i], __builtin_bit_cast(bf16x8, a.P[sub ^ 1][rt]), f32x4{});
      else a.o[rt][cc] = MFMA16(a.F[N & 1][i], __builtin_bit_cast(bf16x8, a.P[sub ^ 1][rt]), a.o[rt][cc]);
    }
    SBAR();
  }
  if constexpr (NX >= 0 && G < 4) a_frag<NX, G>(a);
  if constexpr (SM) {
    constexpr int sb = (b == 1 || b == 2) ? 0 : 1, e0 = (b & 1) ? 0 : 6, pb = b == 0 ? sub ^ 1 : sub;
    if constexpr ((G & 1) == 0) { constexpr int e = e0 + (G >> 1); a.S[sb][e >> 2][e & 3] = ex2(a.S[sb][e >> 2][e & 3]); PIN(a.S[sb][e >> 2]); }
    else {
      constexpr int e = e0 + (G >> 1), rt = e >> 2;
      a.l[rt] = a.l[rt] + a.S[sb][rt][e & 3]; PIN(a.l[rt]);
      if constexpr (e & 1) { a.P[pb][rt][2 * sb + ((e & 3) >> 1)] = cvtpk(a.S[sb][rt][(e & 3) - 1], a.S[sb][rt][e & 3]); PIN(a.P[pb][rt]); }
    }
  }
  SBAR();
}
template <int N, bool MM, bool SM, int NX, bool Z, int... Gs> DI void a_block_(AS& a, iseq<Gs...>) { LGKM0(); SBAR(); (a_gap<N, MM, SM, NX, Z, Gs>(a), ...); }
template <int N, bool MM, bool SM, int NX, bool Z = false> DI void a_block(AS& a) { a_block_<N, MM, SM, NX, Z>(a, mkseq<12>{}); }
template <bool FIRST> DI void a_period(AS& a) {
  if constexpr (FIRST) {
    a_block<0, true, false, -1, true>(a);
#pragma unroll
    for (int rt = 0; rt < 3; ++rt) {
      const float mx = red_max(fmaxf(fmaxf(a.S[0][rt][0], a.S[0][rt][1]), fmaxf(a.S[0][rt][2], a.S[0][rt][3])));
      a.negm[rt] = f32x4{-mx, -mx, -mx, -mx}; a.S[0][rt] -= mx; PIN(a.negm[rt]);
    }
    SBAR();
    a_block<1, false, true, 2>(a); a_block<2, true, true, -1>(a); a_block<3, false, true, 4>(a);
  } else {
    a_block<0, true, true, 1>(a); a_block<1, true, true, 2>(a); a_block<2, true, true, 3>(a); a_block<3, true, true, 4>(a);
  }
  a_block<4, true, true, 5>(a); a_block<5, true, true, 6, FIRST>(a); a_block<6, true, true, 7>(a);
  if constexpr (FIRST) VMW(0);
  BAR();
  a_block<7, true, true, 8, FIRST>(a);
}

DI void path_a(const Ctx& c, int wi) {
  STAMP(0);
  {
    const rsrc_t rx = mkrs(c.x, SQ * DM * 2);
    const unsigned lane = lane_id(), vox = 1280u * (lane >> 3) + 16u * ((lane & 7) ^ ((4 * (wi & 1) + (lane >> 4)) & 7));
    auto xp = [&](int s, unsigned stage, int n) { const int pc = n < 6 ? wi + 4 * n : 24 + (wi & 1); XDMA(c.ldsb + XOFF + stage + 1024 * pc, vox, rx, 128u * s + 10240u * pc); };
#pragma unroll
    for (int n = 0; n < 7; ++n) xp(0, 0, n);
#pragma unroll
    for (int n = 0; n < 7; ++n) xp(1, XST, n);
    unsigned xf = XST;
    qproj<7>(c, wi,
      [&](auto K, int s, int n) { if constexpr (decltype(K)::value == 0) xp(s + 2, xf, n); },
      [&](int s) { if (s == 9) VMW(0); else VMW(7); xf = xf == 2 * XST ? 0 : xf + XST; });
  }
  FST(11);
  BAR();
  FST(12);
  AS a;
  unsigned kb0, vb0;
  { const unsigned lane = lane_id(), r16 = lane & 15, g = lane >> 4, q4 = (lane & 15) >> 2, p = lane & 3;
    kb0 = c.ldsb + 128 * r16 + 16 * (g ^ ((r16 >> 1) & 7));
    vb0 = c.ldsb + SLOT + 2048 * (g >> 1) + 64 * (4 * (g & 1) + q4) + 16 * ((p >> 1) ^ (2 * (g & 1))) + 8 * (p & 1); }
  unsigned sl = 0;
#pragma nounroll
  for (int hd = 0; hd < 2; ++hd) {
#pragma unroll
    for (int rt = 0; rt < 3; ++rt) {
      const int t = 3 * wi + rt;
      const unsigned blk = hd ? Q2OFF + 4096 * t : (t < 8 ? 4 * SLOT + 4096 * t : t < 11 ? MISC + 4096 * (t - 8) : SLOT + 4096 * (t - 11));
      const unsigned q0 = kb0 + blk, q1 = q0 ^ 64;
      a.q[rt][0] = rd128(q0, 0); a.q[rt][1] = rd128(q1, 0); a.q[rt][2] = rd128(q0, 2048); a.q[rt][3] = rd128(q1, 2048);
      a.l[rt] = 0.f; a.P[0][rt] = u32x4{}; a.P[1][rt] = u32x4{};
    }
    a.kn[0] = kb0 + sl; a.kn[1] = a.kn[0] ^ 64;
    a_frag<8, 0>(a); a_frag<8, 1>(a); a_frag<8, 2>(a); a_frag<8, 3>(a);
    if (hd == 0) BAR();
    STAMP(2 + 3 * hd);
    auto setp = [&]() {
      a.kp[0] = a.kn[0]; a.kp[1] = a.kn[1]; a.vp[0] = vb0 + sl; a.vp[1] = a.vp[0] ^ 32;
      sl = sl == 4 * SLOT ? 0 : sl + 2 * SLOT;
      a.kn[0] = kb0 + sl; a.kn[1] = a.kn[0] ^ 64;
      SBAR();
    };
    setp();
    a_period<true>(a);
#pragma nounroll
    for (int t = 1; t < 16; ++t) { PST_(16 * hd + t); setp(); a_period<false>(a); }
    a.vp[0] = vb0 + sl; a.vp[1] = a.vp[0] ^ 32;
    SBAR();
    a_block<0, false, true, 1>(a); a_block<1, true, false, -1>(a);
    a_frag<3, 0>(a); a_frag<3, 1>(a); a_frag<3, 2>(a); a_frag<3, 3>(a);
    a_block<3, true, false, -1>(a);
    if (hd) BAR();
    STAMP(3 + 3 * hd);
    const rsrc_t ro = mkrs(c.oq + (size_t)(2 * c.own + hd) * OBLK, OBLK);
    const unsigned ov = 16 * lane_id() + 6144 * wi;
#pragma unroll
    for (int rt = 0; rt < 3; ++rt) {
      float l = red_add(a.l[rt]);
      if (__builtin_expect(__any(!(l + nanprobe(a.o[rt]) < L_LIMIT)), 0)) {
        float m;
        slow16(c.k + (size_t)hd * SK * DH, c.v + (size_t)hd * SK * DH, 0, 16, 64, a.q[rt], a.o[rt], m, l);
        l = red_add(l);
      }
      const float il = __builtin_amdgcn_rcpf(l);
#pragma unroll
      for (int kk = 0; kk < 4; ++kk) {
        const f32x4 u = a.o[rt][2 * kk] * il, v = a.o[rt][2 * kk + 1] * il;
        u32x4 w = {cvtpk(u[0], u[1]), cvtpk(u[2], u[3]), cvtpk(v[0], v[1]), cvtpk(v[2], v[3])};
        __builtin_amdgcn_raw_buffer_store_b128(w, ro, ov + 2048 * rt + 1024 * (kk & 1) + (OBLK / 2) * (kk >> 1), 0, OAUX);
      }
    }
    STAMP(4 + 3 * hd);
  }
  if (wi & 1) oproj<3, 6>(c, wi); else oproj<3, 7>(c, wi);
}

template <bool LATE> DI void path_b(const Ctx& c, int j) {
  STAMP(0);
  const rsrc_t rk = mkrs(c.k, 2 * SK * DH * 2), rv = mkrs(c.v, 2 * SK * DH * 2);
  unsigned vk0, vk1, vv, wm0, wm1;
  { const unsigned i = lane_id();
    vk0 = 256 * (i >> 3) + 16 * ((i & 7) ^ (i >> 4)); vk1 = (vk0 ^ 64) + 2048;
    vv = 256 * ((i >> 2) & 7) + 64 * (i >> 5) + 16 * ((i & 3) ^ (((i >> 4) & 1) << 1)); }
  auto kpc = [&](int t, unsigned slot, int n) {
    const unsigned m = c.ldsb + slot + 2048 * j, so = (unsigned)(64 * t + 16 * j) * 256u;
    if (n == 0) dma_nt<0>(m, vk0, rk, so); if (n == 1) dma_nt<128>(m + 8064, vk0, rk, so); if (n == 2) dma_nt<0>(m + 1024, vk1, rk, so); if (n == 3) dma_nt<128>(m + 9088, vk1, rk, so);
  };
  auto vpc = [&](int t, unsigned slot, int n) {
    const unsigned m = c.ldsb + slot + 4096 * j, so = (unsigned)(64 * t - 32 + 16 * j) * 256u;
    if (n == 0) dma_nt<0>(m, vv, rv, so); if (n == 1) dma_nt<128>(m + 896, vv, rv, so); if (n == 2) dma_nt<2048>(m, vv, rv, so); if (n == 3) dma_nt<2176>(m + 896, vv, rv, so);
  };
  auto kdma = [&](int t, unsigned slot) { kpc(t, slot, 0); kpc(t, slot, 1); kpc(t, slot, 2); kpc(t, slot, 3); };
  auto vdma = [&](int t, unsigned slot) { vpc(t, slot, 0); vpc(t, slot, 1); vpc(t, slot, 2); vpc(t, slot, 3); };
  {
    const rsrc_t rw = mkrs(c.wq, (DM - 1) * HD * 2 + 512);
    const unsigned i = lane_id(), vw0 = 8192 * ((i >> 2) & 7) + 64 * (i >> 5) + 16 * (i & 3), vw1 = (vw0 ^ 32) + 65536;
    auto wpc = [&](int it, unsigned slot, int n) {
      const unsigned m = c.ldsb + slot + 4096 * j, so = (unsigned)(64 * (it >> 1) + 16 * j) * 8192u + 256 * (it & 1);
      if (n == 0) dma_nt<0>(m, vw0, rw, so); if (n == 1) dma_nt<128>(m + 896, vw0, rw, so); if (n == 2) dma_nt<0>(m + 2048, vw1, rw, so); if (n == 3) dma_nt<128>(m + 2944, vw1, rw, so);
    };
#pragma unroll
    for (int it = 0; it < 5; ++it) { wpc(it, it * SLOT, 0); wpc(it, it * SLOT, 1); wpc(it, it * SLOT, 2); wpc(it, it * SLOT, 3); }
    unsigned f0 = SLOT, f1 = 2 * SLOT;
    auto adv = [](unsigned s_) { s_ += 2 * SLOT; return s_ >= 5 * SLOT ? s_ - 5 * SLOT : s_; };
    qproj<6>(c, j,
      [&](auto K, int s, int n) {
        constexpr int k = decltype(K)::value;
        if constexpr (k == 0) { if (n < 4) wpc(2 * s + 3, f0, n); else wpc(2 * s + 4, f1, n - 4); }
        if constexpr (k == 1) { if (n < 4) wpc(19, 4 * SLOT, n); else kpc(0, 0, n - 4);
          if (n == 7) touch80(wm0, wm1, c.warm + 10240 * j); }
        if constexpr (k == 2) { if (n < 4) { if (LATE) vpc(0, SLOT, n); } else kpc(1, 2 * SLOT, n - 4); }
        if constexpr (k == 4) vdma(1, 3 * SLOT);
      },
      [&](int s) { if (s == 0) VMW(12); else if (s == 9) VMW(6); else VMW(4); f0 = adv(f0); f1 = adv(f1); });
  }
  VMW(8);
  asm volatile("" ::"v"(wm0), "v"(wm1));
  BAR();
  unsigned qb, kb0, vb0;
  { const unsigned lane = lane_id(), r16 = lane & 15, g = lane >> 4, q4 = (lane & 15) >> 2, p = lane & 3;
    qb = c.ldsb + 128 * r16 + 16 * (g ^ ((r16 >> 1) & 7));
    kb0 = qb + 2048 * j;
    vb0 = c.ldsb + SLOT + 4096 * ((j + 2) & 3) + 2048 * (g >> 1) + 64 * (4 * (g & 1) + q4) + 16 * ((p >> 1) ^ (2 * (g & 1))) + 8 * (p & 1); }
  bf16x8 q[4]; f32x4 o[8], os[8], negm = {}; float l = 0.f, ms = 0.f, ls = 0.f; u32x2 P = {};
  unsigned sl = 0, fr = 4 * SLOT;
  auto rdq = [&](unsigned blk) { const unsigned q0 = qb + blk, q1 = q0 ^ 64; q[0] = rd128(q0, 0); q[1] = rd128(q1, 0); q[2] = rd128(q0, 2048); q[3] = rd128(q1, 2048); };
  auto pv = [&]() {
    const unsigned v0 = vb0 + sl, v1 = v0 ^ 32;
    s16x4 vf[8];
#pragma unroll
    for (int cc = 0; cc < 8; ++cc) vf[cc] = rdtr((cc & 1) ? v1 : v0, 512 * (cc >> 1));
#pragma unroll
    for (int cc = 0; cc < 8; ++cc) o[cc] = MFMA16K16(vf[cc], __builtin_bit_cast(s16x4, P), o[cc]);
  };
  auto qk = [&](auto F) {
    constexpr bool FIRST = decltype(F)::value;
    const unsigned k0 = kb0 + sl, k1 = k0 ^ 64;
    const bf16x8 f0 = rd128(k0, 0), f1 = rd128(k1, 0), f2 = rd128(k0, 8192), f3 = rd128(k1, 8192);
    f32x4 S;
    if constexpr (FIRST) S = MFMA16(f0, q[0], f32x4{}); else S = MFMA16(f0, q[0], negm);
    S = MFMA16(f1, q[1], S); S = MFMA16(f2, q[2], S); S = MFMA16(f3, q[3], S);
    if constexpr (FIRST) { const float mx = red_max(fmaxf(fmaxf(S[0], S[1]), fmaxf(S[2], S[3]))); negm = f32x4{-mx, -mx, -mx, -mx}; S -= mx; }
#pragma unroll
    for (int i = 0; i < 4; ++i) { S[i] = ex2(S[i]); l += S[i]; PIN(l); }
    P = u32x2{cvtpk(S[0], S[1]), cvtpk(S[2], S[3])};
  };
  auto finish = [&](int hd) {
    ls = red_add(l); ms = -negm[0];
    if (__builtin_expect(__any(!(ls + nanprobe(o) < L_LIMIT)), 0)) {
      slow16(c.k + (size_t)hd * SK * DH, c.v + (size_t)hd * SK * DH, 16 * j, 64, 16, q, o, ms, ls);
      ls = red_add(ls);
    }
#pragma unroll
    for (int cc = 0; cc < 8; ++cc) { os[cc] = o[cc]; o[cc] = f32x4{}; }
    l = 0.f;
  };
  auto put = [&](unsigned scr) {
    const unsigned lane = lane_id(), sc = c.ldsb + scr + 16 * lane + 8192 * j;
#pragma unroll
    for (int cc = 0; cc < 8; ++cc) wr128f(sc, 1024 * cc, os[cc]);
    if (lane < 16) { *(LAS float*)(c.ldsb + scr + 32768 + 128 * j + 4 * lane) = ms; *(LAS float*)(c.ldsb + scr + 32768 + 128 * j + 64 + 4 * lane) = ls; }
  };
  auto merge = [&](int hd, unsigned scr) {
    const unsigned lane = lane_id(), sc = c.ldsb + scr + 16 * lane + 2048 * j, ml = c.ldsb + scr + 32768 + 4 * (lane & 15);
    float mw[4], lw[4];
#pragma unroll
    for (int w = 0; w < 4; ++w) { mw[w] = *(const LAS float*)(ml + 128 * w); lw[w] = *(const LAS float*)(ml + 128 * w + 64); }
    const float mt = fmaxf(fmaxf(mw[0], mw[1]), fmaxf(mw[2], mw[3]));
    float lt = 0.f;
#pragma unroll
    for (int w = 0; w < 4; ++w) { mw[w] = ex2(mw[w] - mt); lt += mw[w] * lw[w]; }
    const float ir = __builtin_amdgcn_rcpf(lt);
    f32x4 u = {}, v = {};
#pragma unroll
    for (int w = 0; w < 4; ++w) { u += rd128f(sc, 8192 * w) * (mw[w] * ir); v += rd128f(sc, 8192 * w + 1024) * (mw[w] * ir); }
    u32x4 w4 = {cvtpk(u[0], u[1]), cvtpk(u[2], u[3]), cvtpk(v[0], v[1]), cvtpk(v[2], v[3])};
    __builtin_amdgcn_raw_buffer_store_b128(w4, mkrs(c.oq + (size_t)(2 * c.own + hd) * OBLK, OBLK), 16 * lane + 12 * 2048 + 1024 * (j & 1) + (OBLK / 2) * (j >> 1), 0, OAUX);
  };
  auto rot = [&]() { fr = sl; sl = sl == 4 * SLOT ? 0 : sl + 2 * SLOT; };
  auto period = [&](int t, auto&& extra) {
    const unsigned long long ts = __builtin_amdgcn_s_memrealtime();
    kdma(t + 2, fr); vdma(t + 2, fr + SLOT);
    if constexpr (LATE) pv();
    extra();
    qk(ic<0>{});
    if constexpr (!LATE) pv();
    if constexpr (LATE) { while (__builtin_amdgcn_s_memrealtime() - ts < PACE) __builtin_amdgcn_s_sleep(1); }
    VMW(8); BAR(); rot();
  };
  auto none = []() {};
#pragma unroll
  for (int cc = 0; cc < 8; ++cc) o[cc] = f32x4{};
  rdq(q1addr(12));
  __builtin_amdgcn_s_setprio(3);
  BAR();
  kdma(2, fr); vdma(2, fr + SLOT); qk(ic<1>{}); if constexpr (!LATE) pv(); VMW(8); BAR(); rot();
#pragma nounroll
  for (int t = 1; t < 16; ++t) period(t, none);
  kdma(18, fr); vdma(18, fr + SLOT);
  if constexpr (LATE) pv();
  finish(0); rdq(Q2OFF + 12 * 4096);
  qk(ic<1>{}); if constexpr (!LATE) pv(); VMW(8); BAR(); rot();
  period(17, [&]() { put(Q2OFF); });
  period(18, [&]() { merge(0, Q2OFF); });
  period(19, none);
  if (j == 0) signal_quad(c.cnt);
#pragma nounroll
  for (int t = 20; t < 28; ++t) period(t, none);
  const OD d = od_make(c, j);
#pragma unroll
  for (int n = 0; n < 12; ++n) od_piece(c, d, od_blk(c, 0), PS0, n);
  period(28, none);
  unsigned pq = 4;
  if (!LATE && j == 0) poll_issue(pq, c.cnt);
  kdma(31, fr); vdma(31, fr + SLOT);
  if constexpr (LATE) pv(); qk(ic<0>{}); if constexpr (!LATE) pv();
  VMW(8);
  if (!LATE && j == 0) poll_acquire(pq, c.cnt);
  asm volatile("" ::"v"(pq));
  BAR(); rot();
  if constexpr (!LATE) vdma(32, fr + SLOT);
  if constexpr (LATE) pv(); qk(ic<0>{}); if constexpr (!LATE) pv();
  if constexpr (LATE) VMW(0); else { if (j == 0) VMW(5); else VMW(4); }
  BAR(); rot();
  if constexpr (LATE) pv(); qk(ic<0>{}); if constexpr (!LATE) pv();
  VMW(0); BAR(); rot();
#pragma unroll
  for (int n = 0; n < 12; ++n) od_piece(c, d, od_blk(c, 1), PS1, n);
  if constexpr (LATE) pv();
  finish(1);
  put(SCR2);
  BAR();
  merge(1, SCR2);
  __builtin_amdgcn_s_setprio(0);
  if (j & 1) oproj<2, 7>(c, j); else oproj<2, 6>(c, j);
}

__global__ __launch_bounds__(512, 2) void fused_k(Args ar) {
  extern __shared__ __attribute__((aligned(16))) char lds[];
  const int wid = __builtin_amdgcn_readfirstlane(threadIdx.x >> 6);
  const int xcd = blockIdx.x & 7, idx = blockIdx.x >> 3, br = xcd >> 2, b = 2 * (xcd & 3) + (idx >> 4), hp = idx & 15, kq = hp >> 2;
  Ctx c;
  c.ldsb = (unsigned)(size_t)(LAS char*)lds;
  c.x = (br ? ar.x1 : ar.x0) + (size_t)b * SQ * DM;
  c.k = (br ? ar.k1 : ar.k0) + ((size_t)b * H + 2 * hp) * SK * DH;
  c.v = (br ? ar.v1 : ar.v0) + ((size_t)b * H + 2 * hp) * SK * DH;
  c.wq = (br ? ar.wq1 : ar.wq0) + 2 * hp * DH;
  const char* wo = (const char*)(br ? ar.wo1 : ar.wo0);
  c.wo = wo + (size_t)kq * 1024 * DM * 2;
  c.warm = wo + (size_t)((xcd & 3) * 32 + idx) * 40960;
  c.oq = ar.ws + WS_O + (size_t)((br * NB + b) * H + 8 * kq) * OBLK;
  c.part = ar.ws + WS_PART + (size_t)blockIdx.x * (130 * 512);
  c.cnt = (unsigned*)(ar.ws + WS_CNT) + ((br * NB + b) * 4 + kq) * 64;
  c.stp = (unsigned long long*)(ar.ws + WS_STAMP) + (blockIdx.x * 8 + wid) * 32;
  c.own = hp & 3; c.nt = hp & 3;
  if (wid < 4) path_a(c, wid);
  else if (wid < 6) path_b<false>(c, wid - 4);
  else path_b<true>(c, wid - 4);
}

__global__ __launch_bounds__(512) void reduce_k(const char* __restrict__ part, u16* __restrict__ out) {
  const int xcd = blockIdx.x & 7, idx = blockIdx.x >> 3, br = xcd >> 2, b = 2 * (xcd & 3) + (idx >> 4), nt = idx & 3, kq = (idx >> 2) & 3;
  const int lane = threadIdx.x & 63, w = threadIdx.x >> 6;
  const char* pb = part + (size_t)(xcd + 8 * (idx & ~12)) * (130 * 512) + 8 * lane;
  u32x2 v[5][4];
#pragma unroll
  for (int n = 0; n < 5; ++n) {
    const int t = kq + 4 * (w + 8 * n), tt = t < 130 ? t : 0;
#pragma unroll
    for (int q = 0; q < 4; ++q) v[n][q] = *(const u32x2*)(pb + (size_t)q * 32 * (130 * 512) + tt * 512);
  }
#pragma unroll
  for (int n = 0; n < 5; ++n) {
    const int t = kq + 4 * (w + 8 * n);
    if (t < 130) {
      float s0 = 0.f, s1 = 0.f, s2 = 0.f, s3 = 0.f;
#pragma unroll
      for (int q = 0; q < 4; ++q) {
        s0 += __uint_as_float(v[n][q][0] << 16); s1 += __uint_as_float(v[n][q][0] & 0xffff0000u);
        s2 += __uint_as_float(v[n][q][1] << 16); s3 += __uint_as_float(v[n][q][1] & 0xffff0000u);
      }
      const int rt = t / 10, ct = t % 10;
      const int col = 160 * nt + (ct < 8 ? ((nt & 1) ? 32 : 0) + 16 * ct : ((nt & 1) ? 0 : 128) + 16 * (ct - 8)) + 4 * (lane >> 4);
      *(u32x2*)(out + (size_t)(b * SQ + 16 * rt + (lane & 15)) * (2 * DM) + br * DM + col) = u32x2{cvtpk(s0, s1), cvtpk(s2, s3)};
    }
  }
}
}

extern "C" void kernel_launch(void* const* d_in, const int* in_sizes, int n_in, void* d_out, int out_size, void* d_ws, size_t ws_size, hipStream_t stream) {
  using namespace kk;
  Args a;
  a.x0 = (const u16*)d_in[0]; a.k0 = (const u16*)d_in[1]; a.v0 = (const u16*)d_in[2]; a.wq0 = (const u16*)d_in[3]; a.wo0 = (const u16*)d_in[4];
  a.x1 = (const u16*)d_in[5]; a.k1 = (const u16*)d_in[6]; a.v1 = (const u16*)d_in[7]; a.wq1 = (const u16*)d_in[8]; a.wo1 = (const u16*)d_in[9];
  a.ws = (char*)d_ws;
  (void)hipFuncSetAttribute((const void*)fused_k, hipFuncAttributeMaxDynamicSharedMemorySize, (int)LDS_TOTAL);
  (void)hipMemsetAsync((char*)d_ws + WS_CNT, 0, CNT_BYTES, stream);
  fused_k<<<256, 512, LDS_TOTAL, stream>>>(a);
  reduce_k<<<256, 512, 0, stream>>>((const char*)d_ws + WS_PART, (u16*)d_out);
}
```

```cpp
#define PACE 112
#define PROBE 0
#include <hip/hip_runtime.h>
#include <hip/hip_bf16.h>
#include <utility>

namespace kk {
typedef unsigned short u16;
typedef short bf16x8 __attribute__((ext_vector_type(8)));
typedef short s16x4 __attribute__((ext_vector_type(4)));
typedef float f32x4 __attribute__((ext_vector_type(4)));
typedef float f32x2 __attribute__((ext_vector_type(2)));
typedef __bf16 bf16x2 __attribute__((ext_vector_type(2)));
typedef unsigned u32x4 __attribute__((ext_vector_type(4)));
typedef unsigned u32x2 __attribute__((ext_vector_type(2)));
typedef __amdgpu_buffer_rsrc_t rsrc_t;
typedef __attribute__((address_space(1))) unsigned gu32;
#define LAS __attribute__((address_space(3)))
#define DI __device__ __forceinline__
#define SBAR() __builtin_amdgcn_sched_barrier(0)
#define PIN(x) asm volatile("" : "+v"(x))
#define MFMA16(a, b, c) __builtin_amdgcn_mfma_f32_16x16x32_bf16(a, b, c, 0, 0, 0)
#define MFMA16K16(a, b, c) __builtin_amdgcn_mfma_f32_16x16x16bf16_1k(a, b, c, 0, 0, 0)
#define LGKM0() __builtin_amdgcn_s_waitcnt(0xC07F)
#define VMW(N) asm volatile("s_waitcnt vmcnt(" #N ")" ::: "memory")
#define BAR() do { LGKM0(); asm volatile("s_barrier" ::: "memory"); } while (0)
#define MFMA_DONE() do { asm volatile("s_nop 7\n\ts_nop 3" ::: "memory"); SBAR(); } while (0)
template <int... Is> using iseq = std::integer_sequence<int, Is...>;
template <int N> using mkseq = std::make_integer_sequence<int, N>;
template <int N> using ic = std::integral_constant<int, N>;

constexpr int NB = 8, SQ = 208, DM = 640, H = 32, SK = 1024, DH = 128, HD = H * DH, NRT = 13;
constexpr unsigned SLOT = 16384, XST = 26624, XOFF = 5 * SLOT, Q2OFF = 6 * SLOT, MISC = Q2OFF + NRT * 4096, LDS_TOTAL = 163840;
static_assert(XOFF + 3 * XST <= LDS_TOTAL && MISC + 3 * 4096 == LDS_TOTAL, "lds map");
constexpr unsigned OBLK = NRT * 4096;
constexpr unsigned PST = 47104, POFF = 26624;
constexpr unsigned PS0 = LDS_TOTAL - PST, PS1 = 0, PS2 = PST, SCR2 = PST;
static_assert(SCR2 + 32768 + 512 <= 5 * SLOT && PS2 + PST <= PS0, "lds map 2");
constexpr size_t WS_CNT = 0, CNT_BYTES = 16384, WS_O = 1u << 20, WS_PART = 32u << 20, WS_STAMP = 64u << 20;
constexpr float QSCALE = 0.08838834764831845f * 1.4426950408889634f;
constexpr float L_LIMIT = 1.0e24f;
__host__ __device__ constexpr unsigned q1addr(int rt) { return rt < 8 ? 4 * SLOT + 4096 * rt : rt < 11 ? MISC + 4096 * (rt - 8) : SLOT + 4096 * (rt - 11); }

DI unsigned cvtpk(float lo, float hi) { f32x2 v = {lo, hi}; return __builtin_bit_cast(unsigned, __builtin_convertvector(v, bf16x2)); }
DI float fmul_s(float a, float b) { float r; asm("v_mul_f32_e32 %0, %1, %2" : "=v"(r) : "s"(b), "v"(a)); return r; }
DI float ex2(float x) { return __builtin_amdgcn_exp2f(x); }
DI bf16x8 rd128(unsigned base, int imm) { return *(const LAS bf16x8*)((const LAS char*)base + imm); }
DI s16x4 rdtr(unsigned base, int imm) { return __builtin_amdgcn_ds_read_tr16_b64_v4i16((LAS s16x4*)((LAS char*)base + imm)); }
DI bf16x8 cat(s16x4 lo, s16x4 hi) { return __builtin_shufflevector(lo, hi, 0, 1, 2, 3, 4, 5, 6, 7); }
DI void wr128f(unsigned base, int imm, f32x4 v) { *(LAS f32x4*)((LAS char*)base + imm) = v; }
DI f32x4 rd128f(unsigned base, int imm) { return *(const LAS f32x4*)((const LAS char*)base + imm); }
DI float red_add(float x) {
  auto a = __builtin_amdgcn_permlane16_swap(__float_as_uint(x), __float_as_uint(x), false, false); x = __uint_as_float(a[0]) + __uint_as_float(a[1]);
  auto b = __builtin_amdgcn_permlane32_swap(__float_as_uint(x), __float_as_uint(x), false, false); return __uint_as_float(b[0]) + __uint_as_float(b[1]); }
DI float red_max(float x) {
  auto a = __builtin_amdgcn_permlane16_swap(__float_as_uint(x), __float_as_uint(x), false, false); x = fmaxf(__uint_as_float(a[0]), __uint_as_float(a[1]));
  auto b = __builtin_amdgcn_permlane32_swap(__float_as_uint(x), __float_as_uint(x), false, false); return fmaxf(__uint_as_float(b[0]), __uint_as_float(b[1])); }
DI int lane_id() { int l; asm volatile("v_mbcnt_lo_u32_b32 %0, -1, 0\n\tv_mbcnt_hi_u32_b32 %0, -1, %0" : "=v"(l)); return l; }
DI rsrc_t mkrs(const void* p, unsigned bytes) { return __builtin_amdgcn_make_buffer_rsrc((void*)p, 0, bytes, 0x00020000); }

template <int IMM> DI void dma_nt(unsigned m0v, unsigned voff, rsrc_t rs, unsigned soff) {
  asm volatile("s_mov_b32 m0, %0\n\ts_nop 0\n\tbuffer_load_dwordx4 %1, %2, %3 offen offset:%4 nt lds" ::"s"(m0v), "v"(voff), "s"(rs), "s"(soff), "i"(IMM) : "memory");
}
DI void dma_df(unsigned m0v, unsigned voff, rsrc_t rs, unsigned soff) {
  asm volatile("s_mov_b32 m0, %0\n\ts_nop 0\n\tbuffer_load_dwordx4 %1, %2, %3 offen lds" ::"s"(m0v), "v"(voff), "s"(rs), "s"(soff) : "memory");
}
DI void touch80(unsigned& w0, unsigned& w1, const char* p) {
  const unsigned vo = 128 * lane_id();
  asm volatile("s_nop 4\n\tglobal_load_dword %0, %2, %3\n\tglobal_load_dword %1, %2, %3 offset:2048" : "=&v"(w0), "=&v"(w1) : "v"(vo), "s"(p) : "memory");
}
#define OAUX 16
#define XDMA dma_df
DI void mfma2(f32x4& c, const bf16x8& a0, const bf16x8& b0, const bf16x8& a1, const bf16x8& b1) {
  asm volatile("v_mfma_f32_16x16x32_bf16 %0, %1, %2, %0\n\tv_mfma_f32_16x16x32_bf16 %0, %3, %4, %0" : "+v"(c) : "v"(a0), "v"(b0), "v"(a1), "v"(b1));
}
DI void poll_issue(unsigned& v, const unsigned* cnt) { const unsigned z = 0; asm volatile("s_nop 4\n\tglobal_load_dword %0, %1, %2 sc1" : "=v"(v) : "v"(z), "s"(cnt) : "memory"); }
DI void poll_acquire(unsigned v, const unsigned* cnt) {
  asm volatile("" : "+v"(v)::"memory");
  if (__builtin_amdgcn_readfirstlane(v) < 4u) {
#pragma nounroll
    for (int spin = 0; spin < (1 << 22); ++spin) {
      const unsigned u = __hip_atomic_load((gu32*)cnt, __ATOMIC_RELAXED, __HIP_MEMORY_SCOPE_AGENT);
      if (__builtin_amdgcn_readfirstlane(u) >= 4u) break;
      __builtin_amdgcn_s_sleep(2);
    }
  }
  asm volatile("buffer_inv sc1" ::: "memory");
}
DI void signal_quad(unsigned* cnt) { if (lane_id() == 0) __hip_atomic_fetch_add((gu32*)cnt, 1u, __ATOMIC_RELAXED, __HIP_MEMORY_SCOPE_AGENT); }

#define STAMP(i)
#define FST(i)
#define PST_(i)
struct Args { const u16 *x0, *k0, *v0, *wq0, *wo0, *x1, *k1, *v1, *wq1, *wo1; char* ws; };
struct Ctx {
  const u16 *x, *k, *v, *wq; const char *wo, *warm; char *oq, *part; unsigned* cnt; unsigned long long* stp; unsigned ldsb; int own, nt;
};

DI void slow16(const u16* Kg, const u16* Vg, int key0, int kstride, int nstep, const bf16x8 (&q)[4], f32x4 (&o)[8], float& m, float& l) {
  const int lane = lane_id(), r = lane & 15, g = lane >> 4;
  m = -3.0e38f; l = 0.f;
#pragma unroll
  for (int d = 0; d < 8; ++d) o[d] = f32x4{};
#pragma nounroll
  for (int st = 0; st < nstep; ++st) {
    const int kb = key0 + st * kstride;
    f32x4 S = {};
#pragma unroll
    for (int ks = 0; ks < 4; ++ks) S = MFMA16(*(const bf16x8*)(Kg + (size_t)(kb + r) * DH + 32 * ks + 8 * g), q[ks], S);
    const float mx = red_max(fmaxf(fmaxf(S[0], S[1]), fmaxf(S[2], S[3])));
    const float mn = fmaxf(m, mx), al = ex2(m - mn); m = mn;
    float ps = 0.f;
#pragma unroll
    for (int i = 0; i < 4; ++i) { S[i] = ex2(S[i] - mn); ps += S[i]; }
    l = l * al + ps;
    u32x2 pw = {cvtpk(S[0], S[1]), cvtpk(S[2], S[3])};
#pragma unroll
    for (int d = 0; d < 8; ++d) {
      s16x4 vf;
#pragma unroll
      for (int j = 0; j < 4; ++j) vf[j] = (short)Vg[(size_t)(kb + 4 * g + j) * DH + 16 * d + r];
      o[d] *= al;
      o[d] = MFMA16K16(vf, __builtin_bit_cast(s16x4, pw), o[d]);
    }
  }
  __builtin_amdgcn_s_waitcnt(0);
}
DI float nanprobe(const f32x4 (&o)[8]) {
  f32x4 t = {};
#pragma unroll
  for (int d = 0; d < 8; ++d) t += o[d] * 0.f;
  return (t[0] + t[1]) + (t[2] + t[3]);
}

template <int NR, class FD, class FW> DI void qproj(const Ctx& c, int cg, FD&& dma, FW&& wait) {
  f32x4 acc[NR][4]; bf16x8 w[4][2], x[NR][2];
  const unsigned lane = lane_id(), r16 = lane & 15, g = lane >> 4, q4 = (lane & 15) >> 2, p = lane & 3;
  const unsigned wb0 = c.ldsb + 1024 * (cg & 1) + 2048 * g + 64 * q4 + 16 * ((p >> 1) ^ (2 * (g & 1))) + 8 * (p & 1);
  const unsigned xb0 = c.ldsb + XOFF + (NR == 7 ? 0 : 7 * 2048) + 128 * r16 + 16 * (g ^ ((r16 >> 1) & 7));
#pragma unroll
  for (int r = 0; r < NR; ++r)
#pragma unroll
    for (int i = 0; i < 4; ++i) acc[r][i] = f32x4{};
  unsigned xs = 0, ws = SLOT * (cg >> 1), xp0, xp1, wp0, wp1;
  auto ldw = [&](int i) { const unsigned b = (i & 1) ? wp1 : wp0; const int im = 512 * (i >> 1);
    w[i][0] = cat(rdtr(b, im), rdtr(b, im + 256)); w[i][1] = cat(rdtr(b, im + 8192), rdtr(b, im + 8448)); };
  auto ldx = [&](int r) { x[r][0] = rd128(xp0, 2048 * r); x[r][1] = rd128(xp1, 2048 * r); };
  auto row = [&](int r) {
#pragma unroll
    for (int i = 0; i < 4; ++i) mfma2(acc[r][i], w[i][0], x[r][0], w[i][1], x[r][1]);
  };
  auto step = [&](auto K, auto F, int s) {
    constexpr bool FIRST = decltype(F)::value;
    wait(s); BAR();
    FST(s);
    if constexpr (FIRST) { STAMP(1); }
    xp0 = xb0 + xs; xp1 = xp0 ^ 64; wp0 = wb0 + ws; wp1 = wp0 ^ 32;
    SBAR();
    if constexpr (NR == 7) {
#pragma unroll
      for (int r = 0; r < 7; ++r) { if constexpr (!FIRST) row(r); SBAR(); ldx(r); if (r & 1) dma(K, s, r >> 1); SBAR(); }
#pragma unroll
      for (int i = 0; i < 4; ++i) { ldw(i); dma(K, s, 3 + i); SBAR(); }
    } else {
#pragma unroll
      for (int i = 0; i < 4; ++i) { dma(K, s, i); ldw(i); SBAR(); }
#pragma unroll
      for (int r = 0; r < 6; ++r) { if (r == 2) dma(K, s, 4); ldx(r); SBAR(); }
#pragma unroll
      for (int r = 0; r < 6; ++r) { row(r); SBAR(); if ((r & 1) == 0) dma(K, s, 5 + (r >> 1)); SBAR(); }
    }
    xs = xs == 2 * XST ? 0 : xs + XST; ws += 2 * SLOT; ws = ws >= 5 * SLOT ? ws - 5 * SLOT : ws;
  };
  asm volatile("s_nop 1" ::: "memory");
  step(ic<NR == 7 ? 0 : 3>{}, ic<1>{}, 0);
#pragma nounroll
  for (int s = 1; s < 8; ++s) step(ic<0>{}, ic<0>{}, s);
  step(ic<1>{}, ic<0>{}, 8);
  step(ic<2>{}, ic<0>{}, 9);
  BAR();
  FST(10);
  dma(ic<4>{}, 10, 0);
  const unsigned ln = lane_id(), rr = ln & 15, gg = ln >> 4;
  const unsigned qw = c.ldsb + 2048 * (cg & 1) + 128 * rr + 16 * ((gg >> 1) ^ ((rr >> 1) & 7)) + 8 * (gg & 1);
  auto put = [&](int r) {
    const int rt = (NR == 7 ? 0 : 7) + r;
    const unsigned blk = (cg >> 1) ? Q2OFF + 4096 * rt : q1addr(rt);
#pragma unroll
    for (int i = 0; i < 4; ++i) {
      const f32x4 v = acc[r][i];
      *(LAS u32x2*)((qw ^ (32 * i)) + blk) = u32x2{cvtpk(fmul_s(v[0], QSCALE), fmul_s(v[1], QSCALE)), cvtpk(fmul_s(v[2], QSCALE), fmul_s(v[3], QSCALE))};
    }
  };
  SBAR();
  if constexpr (NR == 7) {
#pragma unroll
    for (int r = 0; r < 7; ++r) { row(r); SBAR(); if (r) put(r - 1); SBAR(); }
    MFMA_DONE();
    put(6);
  } else {
    MFMA_DONE();
#pragma unroll
    for (int r = 0; r < 6; ++r) put(r);
  }
}

struct OD { rsrc_t rw, ro; unsigned vwp, vwo, vo, mw, mo, lo; int rg; };
DI OD od_make(const Ctx& c, int rg) {
  OD d; const unsigned lane = lane_id(), xr = 16 * ((lane & 3) ^ (((lane >> 4) & 1) << 1));
  d.rw = mkrs(c.wo, 1024 * DM * 2); d.ro = mkrs(c.oq, 8 * OBLK);
  d.vwp = 1280 * (16 * rg + ((lane >> 2) & 7)) + 64 * (lane >> 5) + xr + 320 * c.nt + ((c.nt & 1) ? 64 : 0);
  d.vwo = 1280 * (16 * rg + (lane >> 2)) + xr + 320 * c.nt + ((c.nt & 1) ? 0 : 256);
  d.vo = 16 * lane; d.mw = c.ldsb + POFF + 5120 * rg; d.mo = c.ldsb + 1024 * rg; d.lo = 1024 * (24 + (rg & 1)); d.rg = rg;
  return d;
}
DI unsigned od_blk(const Ctx& c, int s) { return (unsigned)(s < 16 ? 4 * (((s >> 1) + c.own) & 3) + 2 * (s >> 3) + (s & 1) : 64); }
DI void od_piece(const Ctx& c, const OD& d, unsigned u, unsigned st, int n) {
  const unsigned sw = u * (64 * 1280), so = u * (OBLK / 2), m = d.mw + st;
  if (n == 0) dma_df(m, d.vwp, d.rw, sw);
  else if (n == 1) dma_df(m + 1024, d.vwp, d.rw, sw + 128);
  else if (n == 2) dma_df(m + 3072, d.vwp, d.rw, sw + 10240);
  else if (n == 3) dma_df(m + 4096, d.vwp, d.rw, sw + 10240 + 128);
  else if (n == 4) dma_df(m + 2048, d.vwo, d.rw, sw);
  else if (n < 11) dma_df(d.mo + st + 4096 * (n - 5), d.vo, d.ro, so + 1024 * d.rg + 4096 * (n - 5));
  else dma_df(c.ldsb + d.lo + st, d.vo, d.ro, so + d.lo);
}
template <int NC, int NR> DI void oproj(const Ctx& c, int rg) {
  const OD d = od_make(c, rg);
  f32x4 acc[NR][NC]; bf16x8 w[NC][2], x[NR][2];
  const int c0 = NC == 3 ? 3 * (rg >> 1) : 6 + 2 * (rg >> 1), r0 = NR == 7 ? 0 : 7;
  unsigned wb[NC], xb;
  {
    const unsigned lane = lane_id(), g = lane >> 4, q4 = (lane & 15) >> 2, p = lane & 3;
    const unsigned in = 64 * (4 * (g & 1) + q4) + 16 * ((p >> 1) ^ (2 * (g & 1))) + 8 * (p & 1);
#pragma unroll
    for (int i = 0; i < NC; ++i) {
      const int ct = c0 + i;
      const unsigned off = ct < 8 ? 512 * (ct >> 1) + 3072 * (g >> 1) : 2048 + 512 * (g >> 1);
      wb[i] = c.ldsb + POFF + off + (in ^ (32 * (ct & 1)));
    }
    xb = c.ldsb + 16 * lane + 2048 * r0;
  }
#pragma unroll
  for (int r = 0; r < NR; ++r)
#pragma unroll
    for (int i = 0; i < NC; ++i) acc[r][i] = f32x4{};
  unsigned st = PS0, nx = PS1, fr = PS2;
  auto ldw = [&](int i) { const unsigned wp = wb[i] + st; w[i][0] = cat(rdtr(wp, 0), rdtr(wp, 5120)); w[i][1] = cat(rdtr(wp, 10240), rdtr(wp, 15360)); };
  auto ldx = [&](int r) { const unsigned xp = xb + st; x[r][0] = rd128(xp, 2048 * r); x[r][1] = rd128(xp, 2048 * r + 1024); };
  auto row = [&](int r) {
#pragma unroll
    for (int i = 0; i < NC; ++i) mfma2(acc[r][i], w[i][0], x[r][0], w[i][1], x[r][1]);
  };
  auto step = [&](auto F, int s) {
    constexpr bool FIRST = decltype(F)::value;
    const unsigned u = od_blk(c, s + 2);
    SBAR();
    if constexpr (NC == 3) {
#pragma unroll
      for (int r = 0; r < NR; ++r) { if constexpr (!FIRST) row(r); SBAR(); ldx(r); if (r == 1 || r == 4) od_piece(c, d, u, fr, r / 3); SBAR(); }
#pragma unroll
      for (int i = 0; i < 3; ++i) { ldw(i); if (i) od_piece(c, d, u, fr, 1 + i); SBAR(); }
    } else {
#pragma unroll
      for (int i = 0; i < 2; ++i) { od_piece(c, d, u, fr, 4 + i); ldw(i); SBAR(); }
#pragma unroll
      for (int r = 0; r < NR; ++r) { if ((r & 1) == 0 && r < 6) od_piece(c, d, u, fr, 6 + (r >> 1)); ldx(r); SBAR(); }
#pragma unroll
      for (int r = 0; r < NR; ++r) { row(r); SBAR(); if ((r & 1) == 0 && r < 6) od_piece(c, d, u, fr, 9 + (r >> 1)); SBAR(); }
    }
    const unsigned t = st; st = nx; nx = fr; fr = t;
  };
  int fi = 14;
  auto sync = [&]() { if constexpr (NC == 3) VMW(4); else VMW(8); BAR(); FST(fi); ++fi; };
  asm volatile("s_nop 1" ::: "memory");
  if constexpr (NC == 2) VMW(13);
  BAR();
  STAMP(8); FST(13);
  step(ic<1>{}, 0);
  sync();
  if (NC == 2 && rg == 0) signal_quad(c.cnt + 32);
  if constexpr (NC == 3 && NR == 6) {
#pragma nounroll
    for (int s = 1; s < 5; ++s) { step(ic<0>{}, s); sync(); }
    unsigned pv = 4;
    if (rg == 1) poll_issue(pv, c.cnt + 32);
    step(ic<0>{}, 5);
    VMW(4);
    if (rg == 1) poll_acquire(pv, c.cnt + 32);
    asm volatile("" ::"v"(pv));
    BAR();
    step(ic<0>{}, 6);
    if (rg == 1) VMW(5); else VMW(4);
    BAR();
    step(ic<0>{}, 7);
    if (rg == 1) VMW(0); else VMW(4);
    BAR();
#pragma nounroll
    for (int s = 8; s < 15; ++s) { step(ic<0>{}, s); sync(); }
  } else {
#pragma nounroll
    for (int s = 1; s < 15; ++s) { step(ic<0>{}, s); sync(); }
  }
  STAMP(9);
  step(ic<0>{}, 15);
  if constexpr (NC == 3) {
    LGKM0(); SBAR();
#pragma unroll
    for (int r = 0; r < NR; ++r) row(r);
  }
  MFMA_DONE();
  STAMP(10);
  char* pb = c.part + 8 * lane_id();
#pragma unroll
  for (int r = 0; r < NR; ++r)
#pragma unroll
    for (int i = 0; i < NC; ++i) { const f32x4 v = acc[r][i]; *(u32x2*)(pb + ((r0 + r) * 10 + c0 + i) * 512) = u32x2{cvtpk(v[0], v[1]), cvtpk(v[2], v[3])}; }
  STAMP(11);
}

struct AS {
  f32x4 o[3][8], S[2][3], negm[3]; bf16x8 q[3][4], F[2][4]; u32x4 P[2][3]; float l[3];
  unsigned kp[2], vp[2], kn[2];
};
template <int N, int I> DI void a_frag(AS& a) {
  constexpr int sub = (N >> 2) & 1, b = N & 3;
  if constexpr ((b & 1) == 0) {
    constexpr int kt = 2 * sub + (b >> 1);
    a.F[N & 1][I] = rd128(N == 8 ? a.kn[I & 1] : a.kp[I & 1], 8192 * (I >> 1) + 2048 * kt);
  } else {
    constexpr int cc = 4 * (b >> 1) + I, imm = 8192 * sub + 512 * (cc >> 1);
    a.F[N & 1][I] = cat(rdtr(a.vp[cc & 1], imm), rdtr(a.vp[cc & 1], imm + 4096));
  }
}
template <int N, bool MM, bool SM, int NX, bool Z, int G> DI void a_gap(AS& a) {
  constexpr int sub = N >> 2, b = N & 3;
  if constexpr (MM) {
    if constexpr ((b & 1) == 0) {
      constexpr int rt = G >> 2, ks = G & 3, sb = b >> 1;
      if constexpr (ks == 0) { if constexpr (Z) a.S[sb][rt] = MFMA16(a.F[N & 1][0], a.q[rt][0], f32x4{}); else a.S[sb][rt] = MFMA16(a.F[N & 1][0], a.q[rt][0], a.negm[rt]); }
      else a.S[sb][rt] = MFMA16(a.F[N & 1][ks], a.q[rt][ks], a.S[sb][rt]);
    } else {
      constexpr int i = G / 3, rt = G % 3, cc = 4 * (b >> 1) + i;
      if constexpr (Z) a.o[rt][cc] = MFMA16(a.F[N & 1][i], __builtin_bit_cast(bf16x8, a.P[sub ^ 1][rt]), f32x4{});
      else a.o[rt][cc] = MFMA16(a.F[N & 1][i], __builtin_bit_cast(bf16x8, a.P[sub ^ 1][rt]), a.o[rt][cc]);
    }
    SBAR();
  }
  if constexpr (NX >= 0 && G < 4) a_frag<NX, G>(a);
  if constexpr (SM) {
    constexpr int sb = (b == 1 || b == 2) ? 0 : 1, e0 = (b & 1) ? 0 : 6, pb = b == 0 ? sub ^ 1 : sub;
    if constexpr ((G & 1) == 0) { constexpr int e = e0 + (G >> 1); a.S[sb][e >> 2][e & 3] = ex2(a.S[sb][e >> 2][e & 3]); PIN(a.S[sb][e >> 2]); }
    else {
      constexpr int e = e0 + (G >> 1), rt = e >> 2;
      a.l[rt] = a.l[rt] + a.S[sb][rt][e & 3]; PIN(a.l[rt]);
      if constexpr (e & 1) { a.P[pb][rt][2 * sb + ((e & 3) >> 1)] = cvtpk(a.S[sb][rt][(e & 3) - 1], a.S[sb][rt][e & 3]); PIN(a.P[pb][rt]); }
    }
  }
  SBAR();
}
template <int N, bool MM, bool SM, int NX, bool Z, int... Gs> DI void a_block_(AS& a, iseq<Gs...>) { LGKM0(); SBAR(); (a_gap<N, MM, SM, NX, Z, Gs>(a), ...); }
template <int N, bool MM, bool SM, int NX, bool Z = false> DI void a_block(AS& a) { a_block_<N, MM, SM, NX, Z>(a, mkseq<12>{}); }
template <bool FIRST> DI void a_period(AS& a) {
  if constexpr (FIRST) {
    a_block<0, true, false, -1, true>(a);
#pragma unroll
    for (int rt = 0; rt < 3; ++rt) {
      const float mx = red_max(fmaxf(fmaxf(a.S[0][rt][0], a.S[0][rt][1]), fmaxf(a.S[0][rt][2], a.S[0][rt][3])));
      a.negm[rt] = f32x4{-mx, -mx, -mx, -mx}; a.S[0][rt] -= mx; PIN(a.negm[rt]);
    }
    SBAR();
    a_block<1, false, true, 2>(a); a_block<2, true, true, -1>(a); a_block<3, false, true, 4>(a);
  } else {
    a_block<0, true, true, 1>(a); a_block<1, true, true, 2>(a); a_block<2, true, true, 3>(a); a_block<3, true, true, 4>(a);
  }
  a_block<4, true, true, 5>(a); a_block<5, true, true, 6, FIRST>(a); a_block<6, true, true, 7>(a);
  if constexpr (FIRST) VMW(0);
  BAR();
  a_block<7, true, true, 8, FIRST>(a);
}

DI void path_a(const Ctx& c, int wi) {
  STAMP(0);
  unsigned long long p0 = __builtin_amdgcn_s_memrealtime(), p1, p2; __builtin_amdgcn_s_waitcnt(0xC07F);
  {
    const rsrc_t rx = mkrs(c.x, SQ * DM * 2);
    const unsigned lane = lane_id(), vox = 1280u * (lane >> 3) + 16u * ((lane & 7) ^ ((4 * (wi & 1) + (lane >> 4)) & 7));
    auto xp = [&](int s, unsigned stage, int n) { const int pc = n < 6 ? wi + 4 * n : 24 + (wi & 1); XDMA(c.ldsb + XOFF + stage + 1024 * pc, vox, rx, 128u * s + 10240u * pc); };
#pragma unroll
    for (int n = 0; n < 7; ++n) xp(0, 0, n);
#pragma unroll
    for (int n = 0; n < 7; ++n) xp(1, XST, n);
    unsigned xf = XST;
    qproj<7>(c, wi,
      [&](auto K, int s, int n) { if constexpr (decltype(K)::value == 0) xp(s + 2, xf, n); },
      [&](int s) { if (s == 9) VMW(0); else VMW(7); xf = xf == 2 * XST ? 0 : xf + XST; });
  }
  FST(11);
  BAR();
  FST(12);
  AS a;
  unsigned kb0, vb0;
  { const unsigned lane = lane_id(), r16 = lane & 15, g = lane >> 4, q4 = (lane & 15) >> 2, p = lane & 3;
    kb0 = c.ldsb + 128 * r16 + 16 * (g ^ ((r16 >> 1) & 7));
    vb0 = c.ldsb + SLOT + 2048 * (g >> 1) + 64 * (4 * (g & 1) + q4) + 16 * ((p >> 1) ^ (2 * (g & 1))) + 8 * (p & 1); }
  unsigned sl = 0;
  p1 = __builtin_amdgcn_s_memrealtime(); __builtin_amdgcn_s_waitcnt(0xC07F);
#pragma nounroll
  for (int hd = 0; hd < 2; ++hd) {
#pragma unroll
    for (int rt = 0; rt < 3; ++rt) {
      const int t = 3 * wi + rt;
      const unsigned blk = hd ? Q2OFF + 4096 * t : (t < 8 ? 4 * SLOT + 4096 * t : t < 11 ? MISC + 4096 * (t - 8) : SLOT + 4096 * (t - 11));
      const unsigned q0 = kb0 + blk, q1 = q0 ^ 64;
      a.q[rt][0] = rd128(q0, 0); a.q[rt][1] = rd128(q1, 0); a.q[rt][2] = rd128(q0, 2048); a.q[rt][3] = rd128(q1, 2048);
      a.l[rt] = 0.f; a.P[0][rt] = u32x4{}; a.P[1][rt] = u32x4{};
    }
    a.kn[0] = kb0 + sl; a.kn[1] = a.kn[0] ^ 64;
    a_frag<8, 0>(a); a_frag<8, 1>(a); a_frag<8, 2>(a); a_frag<8, 3>(a);
    if (hd == 0) BAR();
    STAMP(2 + 3 * hd);
    auto setp = [&]() {
      a.kp[0] = a.kn[0]; a.kp[1] = a.kn[1]; a.vp[0] = vb0 + sl; a.vp[1] = a.vp[0] ^ 32;
      sl = sl == 4 * SLOT ? 0 : sl + 2 * SLOT;
      a.kn[0] = kb0 + sl; a.kn[1] = a.kn[0] ^ 64;
      SBAR();
    };
    setp();
    a_period<true>(a);
#pragma nounroll
    for (int t = 1; t < 16; ++t) { PST_(16 * hd + t); setp(); a_period<false>(a); }
    a.vp[0] = vb0 + sl; a.vp[1] = a.vp[0] ^ 32;
    SBAR();
    a_block<0, false, true, 1>(a); a_block<1, true, false, -1>(a);
    a_frag<3, 0>(a); a_frag<3, 1>(a); a_frag<3, 2>(a); a_frag<3, 3>(a);
    a_block<3, true, false, -1>(a);
    if (hd) BAR();
    STAMP(3 + 3 * hd);
    const rsrc_t ro = mkrs(c.oq + (size_t)(2 * c.own + hd) * OBLK, OBLK);
    const unsigned ov = 16 * lane_id() + 6144 * wi;
#pragma unroll
    for (int rt = 0; rt < 3; ++rt) {
      float l = red_add(a.l[rt]);
      if (__builtin_expect(__any(!(l + nanprobe(a.o[rt]) < L_LIMIT)), 0)) {
        float m;
        slow16(c.k + (size_t)hd * SK * DH, c.v + (size_t)hd * SK * DH, 0, 16, 64, a.q[rt], a.o[rt], m, l);
        l = red_add(l);
      }
      const float il = __builtin_amdgcn_rcpf(l);
#pragma unroll
      for (int kk = 0; kk < 4; ++kk) {
        const f32x4 u = a.o[rt][2 * kk] * il, v = a.o[rt][2 * kk + 1] * il;
        u32x4 w = {cvtpk(u[0], u[1]), cvtpk(u[2], u[3]), cvtpk(v[0], v[1]), cvtpk(v[2], v[3])};
        __builtin_amdgcn_raw_buffer_store_b128(w, ro, ov + 2048 * rt + 1024 * (kk & 1) + (OBLK / 2) * (kk >> 1), 0, OAUX);
      }
    }
    STAMP(4 + 3 * hd);
  }
  p2 = __builtin_amdgcn_s_memrealtime(); __builtin_amdgcn_s_waitcnt(0xC07F);
  if (wi & 1) oproj<3, 6>(c, wi); else oproj<3, 7>(c, wi);
  { const unsigned long long p3 = __builtin_amdgcn_s_memrealtime();
    const unsigned long long d = PROBE == 1 ? p1 - p0 : PROBE == 2 ? p2 - p1 : PROBE == 3 ? p3 - p2 : PROBE == 4 ? p3 - p0 : 0;
    while (__builtin_amdgcn_s_memrealtime() - p3 < d) __builtin_amdgcn_s_sleep(4); }
}

template <bool LATE> DI void path_b(const Ctx& c, int j) {
  STAMP(0);
  const rsrc_t rk = mkrs(c.k, 2 * SK * DH * 2), rv = mkrs(c.v, 2 * SK * DH * 2);
  unsigned vk0, vk1, vv, wm0, wm1;
  { const unsigned i = lane_id();
    vk0 = 256 * (i >> 3) + 16 * ((i & 7) ^ (i >> 4)); vk1 = (vk0 ^ 64) + 2048;
    vv = 256 * ((i >> 2) & 7) + 64 * (i >> 5) + 16 * ((i & 3) ^ (((i >> 4) & 1) << 1)); }
  auto kpc = [&](int t, unsigned slot, int n) {
    const unsigned m = c.ldsb + slot + 2048 * j, so = (unsigned)(64 * t + 16 * j) * 256u;
    if (n == 0) dma_nt<0>(m, vk0, rk, so); if (n == 1) dma_nt<128>(m + 8064, vk0, rk, so); if (n == 2) dma_nt<0>(m + 1024, vk1, rk, so); if (n == 3) dma_nt<128>(m + 9088, vk1, rk, so);
  };
  auto vpc = [&](int t, unsigned slot, int n) {
    const unsigned m = c.ldsb + slot + 4096 * j, so = (unsigned)(64 * t - 32 + 16 * j) * 256u;
    if (n == 0) dma_nt<0>(m, vv, rv, so); if (n == 1) dma_nt<128>(m + 896, vv, rv, so); if (n == 2) dma_nt<2048>(m, vv, rv, so); if (n == 3) dma_nt<2176>(m + 896, vv, rv, so);
  };
  auto kdma = [&](int t, unsigned slot) { kpc(t, slot, 0); kpc(t, slot, 1); kpc(t, slot, 2); kpc(t, slot, 3); };
  auto vdma = [&](int t, unsigned slot) { vpc(t, slot, 0); vpc(t, slot, 1); vpc(t, slot, 2); vpc(t, slot, 3); };
  {
    const rsrc_t rw = mkrs(c.wq, (DM - 1) * HD * 2 + 512);
    const unsigned i = lane_id(), vw0 = 8192 * ((i >> 2) & 7) + 64 * (i >> 5) + 16 * (i & 3), vw1 = (vw0 ^ 32) + 65536;
    auto wpc = [&](int it, unsigned slot, int n) {
      const unsigned m = c.ldsb + slot + 4096 * j, so = (unsigned)(64 * (it >> 1) + 16 * j) * 8192u + 256 * (it & 1);
      if (n == 0) dma_nt<0>(m, vw0, rw, so); if (n == 1) dma_nt<128>(m + 896, vw0, rw, so); if (n == 2) dma_nt<0>(m + 2048, vw1, rw, so); if (n == 3) dma_nt<128>(m + 2944, vw1, rw, so);
    };
#pragma unroll
    for (int it = 0; it < 5; ++it) { wpc(it, it * SLOT, 0); wpc(it, it * SLOT, 1); wpc(it, it * SLOT, 2); wpc(it, it * SLOT, 3); }
    unsigned f0 = SLOT, f1 = 2 * SLOT;
    auto adv = [](unsigned s_) { s_ += 2 * SLOT; return s_ >= 5 * SLOT ? s_ - 5 * SLOT : s_; };
    qproj<6>(c, j,
      [&](auto K, int s, int n) {
        constexpr int k = decltype(K)::value;
        if constexpr (k == 0) { if (n < 4) wpc(2 * s + 3, f0, n); else wpc(2 * s + 4, f1, n - 4); }
        if constexpr (k == 1) { if (n < 4) wpc(19, 4 * SLOT, n); else kpc(0, 0, n - 4);
          if (n == 7) touch80(wm0, wm1, c.warm + 10240 * j); }
        if constexpr (k == 2) { if (n < 4) { if (LATE) vpc(0, SLOT, n); } else kpc(1, 2 * SLOT, n - 4); }
        if constexpr (k == 4) vdma(1, 3 * SLOT);
      },
      [&](int s) { if (s == 0) VMW(12); else if (s == 9) VMW(6); else VMW(4); f0 = adv(f0); f1 = adv(f1); });
  }
  VMW(8);
  asm volatile("" ::"v"(wm0), "v"(wm1));
  BAR();
  unsigned qb, kb0, vb0;
  { const unsigned lane = lane_id(), r16 = lane & 15, g = lane >> 4, q4 = (lane & 15) >> 2, p = lane & 3;
    qb = c.ldsb + 128 * r16 + 16 * (g ^ ((r16 >> 1) & 7));
    kb0 = qb + 2048 * j;
    vb0 = c.ldsb + SLOT + 4096 * ((j + 2) & 3) + 2048 * (g >> 1) + 64 * (4 * (g & 1) + q4) + 16 * ((p >> 1) ^ (2 * (g & 1))) + 8 * (p & 1); }
  bf16x8 q[4]; f32x4 o[8], os[8], negm = {}; float l = 0.f, ms = 0.f, ls = 0.f; u32x2 P = {};
  unsigned sl = 0, fr = 4 * SLOT;
  auto rdq = [&](unsigned blk) { const unsigned q0 = qb + blk, q1 = q0 ^ 64; q[0] = rd128(q0, 0); q[1] = rd128(q1, 0); q[2] = rd128(q0, 2048); q[3] = rd128(q1, 2048); };
  auto pv = [&]() {
    const unsigned v0 = vb0 + sl, v1 = v0 ^ 32;
    s16x4 vf[8];
#pragma unroll
    for (int cc = 0; cc < 8; ++cc) vf[cc] = rdtr((cc & 1) ? v1 : v0, 512 * (cc >> 1));
#pragma unroll
    for (int cc = 0; cc < 8; ++cc) o[cc] = MFMA16K16(vf[cc], __builtin_bit_cast(s16x4, P), o[cc]);
  };
  auto qk = [&](auto F) {
    constexpr bool FIRST = decltype(F)::value;
    const unsigned k0 = kb0 + sl, k1 = k0 ^ 64;
    const bf16x8 f0 = rd128(k0, 0), f1 = rd128(k1, 0), f2 = rd128(k0, 8192), f3 = rd128(k1, 8192);
    f32x4 S;
    if constexpr (FIRST) S = MFMA16(f0, q[0], f32x4{}); else S = MFMA16(f0, q[0], negm);
    S = MFMA16(f1, q[1], S); S = MFMA16(f2, q[2], S); S = MFMA16(f3, q[3], S);
    if constexpr (FIRST) { const float mx = red_max(fmaxf(fmaxf(S[0], S[1]), fmaxf(S[2], S[3]))); negm = f32x4{-mx, -mx, -mx, -mx}; S -= mx; }
#pragma unroll
    for (int i = 0; i < 4; ++i) { S[i] = ex2(S[i]); l += S[i]; PIN(l); }
    P = u32x2{cvtpk(S[0], S[1]), cvtpk(S[2], S[3])};
  };
  auto finish = [&](int hd) {
    ls = red_add(l); ms = -negm[0];
    if (__builtin_expect(__any(!(ls + nanprobe(o) < L_LIMIT)), 0)) {
      slow16(c.k + (size_t)hd * SK * DH, c.v + (size_t)hd * SK * DH, 16 * j, 64, 16, q, o, ms, ls);
      ls = red_add(ls);
    }
#pragma unroll
    for (int cc = 0; cc < 8; ++cc) { os[cc] = o[cc]; o[cc] = f32x4{}; }
    l = 0.f;
  };
  auto put = [&](unsigned scr) {
    const unsigned lane = lane_id(), sc = c.ldsb + scr + 16 * lane + 8192 * j;
#pragma unroll
    for (int cc = 0; cc < 8; ++cc) wr128f(sc, 1024 * cc, os[cc]);
    if (lane < 16) { *(LAS float*)(c.ldsb + scr + 32768 + 128 * j + 4 * lane) = ms; *(LAS float*)(c.ldsb + scr + 32768 + 128 * j + 64 + 4 * lane) = ls; }
  };
  auto merge = [&](int hd, unsigned scr) {
    const unsigned lane = lane_id(), sc = c.ldsb + scr + 16 * lane + 2048 * j, ml = c.ldsb + scr + 32768 + 4 * (lane & 15);
    float mw[4], lw[4];
#pragma unroll
    for (int w = 0; w < 4; ++w) { mw[w] = *(const LAS float*)(ml + 128 * w); lw[w] = *(const LAS float*)(ml + 128 * w + 64); }
    const float mt = fmaxf(fmaxf(mw[0], mw[1]), fmaxf(mw[2], mw[3]));
    float lt = 0.f;
#pragma unroll
    for (int w = 0; w < 4; ++w) { mw[w] = ex2(mw[w] - mt); lt += mw[w] * lw[w]; }
    const float ir = __builtin_amdgcn_rcpf(lt);
    f32x4 u = {}, v = {};
#pragma unroll
    for (int w = 0; w < 4; ++w) { u += rd128f(sc, 8192 * w) * (mw[w] * ir); v += rd128f(sc, 8192 * w + 1024) * (mw[w] * ir); }
    u32x4 w4 = {cvtpk(u[0], u[1]), cvtpk(u[2], u[3]), cvtpk(v[0], v[1]), cvtpk(v[2], v[3])};
    __builtin_amdgcn_raw_buffer_store_b128(w4, mkrs(c.oq + (size_t)(2 * c.own + hd) * OBLK, OBLK), 16 * lane + 12 * 2048 + 1024 * (j & 1) + (OBLK / 2) * (j >> 1), 0, OAUX);
  };
  auto rot = [&]() { fr = sl; sl = sl == 4 * SLOT ? 0 : sl + 2 * SLOT; };
  auto period = [&](int t, auto&& extra) {
    const unsigned long long ts = __builtin_amdgcn_s_memrealtime();
    kdma(t + 2, fr); vdma(t + 2, fr + SLOT);
    if constexpr (LATE) pv();
    extra();
    qk(ic<0>{});
    if constexpr (!LATE) pv();
    if constexpr (LATE) { while (__builtin_amdgcn_s_memrealtime() - ts < PACE) __builtin_amdgcn_s_sleep(1); }
    VMW(8); BAR(); rot();
  };
  auto none = []() {};
#pragma unroll
  for (int cc = 0; cc < 8; ++cc) o[cc] = f32x4{};
  rdq(q1addr(12));
  __builtin_amdgcn_s_setprio(3);
  BAR();
  kdma(2, fr); vdma(2, fr + SLOT); qk(ic<1>{}); if constexpr (!LATE) pv(); VMW(8); BAR(); rot();
#pragma nounroll
  for (int t = 1; t < 16; ++t) period(t, none);
  kdma(18, fr); vdma(18, fr + SLOT);
  if constexpr (LATE) pv();
  finish(0); rdq(Q2OFF + 12 * 4096);
  qk(ic<1>{}); if constexpr (!LATE) pv(); VMW(8); BAR(); rot();
  period(17, [&]() { put(Q2OFF); });
  period(18, [&]() { merge(0, Q2OFF); });
  period(19, none);
  if (j == 0) signal_quad(c.cnt);
#pragma nounroll
  for (int t = 20; t < 28; ++t) period(t, none);
  const OD d = od_make(c, j);
#pragma unroll
  for (int n = 0; n < 12; ++n) od_piece(c, d, od_blk(c, 0), PS0, n);
  period(28, none);
  unsigned pq = 4;
  if (!LATE && j == 0) poll_issue(pq, c.cnt);
  kdma(31, fr); vdma(31, fr + SLOT);
  if constexpr (LATE) pv(); qk(ic<0>{}); if constexpr (!LATE) pv();
  VMW(8);
  if (!LATE && j == 0) poll_acquire(pq, c.cnt);
  asm volatile("" ::"v"(pq));
  BAR(); rot();
  if constexpr (!LATE) vdma(32, fr + SLOT);
  if constexpr (LATE) pv(); qk(ic<0>{}); if constexpr (!LATE) pv();
  if constexpr (LATE) VMW(0); else { if (j == 0) VMW(5); else VMW(4); }
  BAR(); rot();
  if constexpr (LATE) pv(); qk(ic<0>{}); if constexpr (!LATE) pv();
  VMW(0); BAR(); rot();
#pragma unroll
  for (int n = 0; n < 12; ++n) od_piece(c, d, od_blk(c, 1), PS1, n);
  if constexpr (LATE) pv();
  finish(1);
  put(SCR2);
  BAR();
  merge(1, SCR2);
  __builtin_amdgcn_s_setprio(0);
  if (j & 1) oproj<2, 7>(c, j); else oproj<2, 6>(c, j);
}

__global__ __launch_bounds__(512, 2) void fused_k(Args ar) {
  extern __shared__ __attribute__((aligned(16))) char lds[];
  const int wid = __builtin_amdgcn_readfirstlane(threadIdx.x >> 6);
  const int xcd = blockIdx.x & 7, idx = blockIdx.x >> 3, br = xcd >> 2, b = 2 * (xcd & 3) + (idx >> 4), hp = idx & 15, kq = hp >> 2;
  Ctx c;
  c.ldsb = (unsigned)(size_t)(LAS char*)lds;
  c.x = (br ? ar.x1 : ar.x0) + (size_t)b * SQ * DM;
  c.k = (br ? ar.k1 : ar.k0) + ((size_t)b * H + 2 * hp) * SK * DH;
  c.v = (br ? ar.v1 : ar.v0) + ((size_t)b * H + 2 * hp) * SK * DH;
  c.wq = (br ? ar.wq1 : ar.wq0) + 2 * hp * DH;
  const char* wo = (const char*)(br ? ar.wo1 : ar.wo0);
  c.wo = wo + (size_t)kq * 1024 * DM * 2;
  c.warm = wo + (size_t)((xcd & 3) * 32 + idx) * 40960;
  c.oq = ar.ws + WS_O + (size_t)((br * NB + b) * H + 8 * kq) * OBLK;
  c.part = ar.ws + WS_PART + (size_t)blockIdx.x * (130 * 512);
  c.cnt = (unsigned*)(ar.ws + WS_CNT) + ((br * NB + b) * 4 + kq) * 64;
  c.stp = (unsigned long long*)(ar.ws + WS_STAMP) + (blockIdx.x * 8 + wid) * 32;
  c.own = hp & 3; c.nt = hp & 3;
  if (wid < 4) path_a(c, wid);
  else if (wid < 6) path_b<false>(c, wid - 4);
  else path_b<true>(c, wid - 4);
}

__global__ __launch_bounds__(512) void reduce_k(const char* __restrict__ part, u16* __restrict__ out) {
  const int xcd = blockIdx.x & 7, idx = blockIdx.x >> 3, br = xcd >> 2, b = 2 * (xcd & 3) + (idx >> 4), nt = idx & 3, kq = (idx >> 2) & 3;
  const int lane = threadIdx.x & 63, w = threadIdx.x >> 6;
  const char* pb = part + (size_t)(xcd + 8 * (idx & ~12)) * (130 * 512) + 8 * lane;
  u32x2 v[5][4];
#pragma unroll
  for (int n = 0; n < 5; ++n) {
    const int t = kq + 4 * (w + 8 * n), tt = t < 130 ? t : 0;
#pragma unroll
    for (int q = 0; q < 4; ++q) v[n][q] = *(const u32x2*)(pb + (size_t)q * 32 * (130 * 512) + tt * 512);
  }
#pragma unroll
  for (int n = 0; n < 5; ++n) {
    const int t = kq + 4 * (w + 8 * n);
    if (t < 130) {
      float s0 = 0.f, s1 = 0.f, s2 = 0.f, s3 = 0.f;
#pragma unroll
      for (int q = 0; q < 4; ++q) {
        s0 += __uint_as_float(v[n][q][0] << 16); s1 += __uint_as_float(v[n][q][0] & 0xffff0000u);
        s2 += __uint_as_float(v[n][q][1] << 16); s3 += __uint_as_float(v[n][q][1] & 0xffff0000u);
      }
      const int rt = t / 10, ct = t % 10;
      const int col = 160 * nt + (ct < 8 ? ((nt & 1) ? 32 : 0) + 16 * ct : ((nt & 1) ? 0 : 128) + 16 * (ct - 8)) + 4 * (lane >> 4);
      *(u32x2*)(out + (size_t)(b * SQ + 16 * rt + (lane & 15)) * (2 * DM) + br * DM + col) = u32x2{cvtpk(s0, s1), cvtpk(s2, s3)};
    }
  }
}
}

extern "C" void kernel_launch(void* const* d_in, const int* in_sizes, int n_in, void* d_out, int out_size, void* d_ws, size_t ws_size, hipStream_t stream) {
  using namespace kk;
  Args a;
  a.x0 = (const u16*)d_in[0]; a.k0 = (const u16*)d_in[1]; a.v0 = (const u16*)d_in[2]; a.wq0 = (const u16*)d_in[3]; a.wo0 = (const u16*)d_in[4];
  a.x1 = (const u16*)d_in[5]; a.k1 = (const u16*)d_in[6]; a.v1 = (const u16*)d_in[7]; a.wq1 = (const u16*)d_in[8]; a.wo1 = (const u16*)d_in[9];
  a.ws = (char*)d_ws;
  (void)hipFuncSetAttribute((const void*)fused_k, hipFuncAttributeMaxDynamicSharedMemorySize, (int)LDS_TOTAL);
  (void)hipMemsetAsync((char*)d_ws + WS_CNT, 0, CNT_BYTES, stream);
  fused_k<<<256, 512, LDS_TOTAL, stream>>>(a);
  reduce_k<<<256, 512, 0, stream>>>((const char*)d_ws + WS_PART, (u16*)d_out);
}
```

```cpp
#define PACE 124
#define MAP 2
#include <hip/hip_runtime.h>
#include <hip/hip_bf16.h>
#include <utility>

namespace kk {
typedef unsigned short u16;
typedef short bf16x8 __attribute__((ext_vector_type(8)));
typedef short s16x4 __attribute__((ext_vector_type(4)));
typedef float f32x4 __attribute__((ext_vector_type(4)));
typedef float f32x2 __attribute__((ext_vector_type(2)));
typedef __bf16 bf16x2 __attribute__((ext_vector_type(2)));
typedef unsigned u32x4 __attribute__((ext_vector_type(4)));
typedef unsigned u32x2 __attribute__((ext_vector_type(2)));
typedef __amdgpu_buffer_rsrc_t rsrc_t;
typedef __attribute__((address_space(1))) unsigned gu32;
#define LAS __attribute__((address_space(3)))
#define DI __device__ __forceinline__
#define SBAR() __builtin_amdgcn_sched_barrier(0)
#define PIN(x) asm volatile("" : "+v"(x))
#define MFMA16(a, b, c) __builtin_amdgcn_mfma_f32_16x16x32_bf16(a, b, c, 0, 0, 0)
#define MFMA16K16(a, b, c) __builtin_amdgcn_mfma_f32_16x16x16bf16_1k(a, b, c, 0, 0, 0)
#define LGKM0() __builtin_amdgcn_s_waitcnt(0xC07F)
#define VMW(N) asm volatile("s_waitcnt vmcnt(" #N ")" ::: "memory")
#define BAR() do { LGKM0(); asm volatile("s_barrier" ::: "memory"); } while (0)
#define MFMA_DONE() do { asm volatile("s_nop 7\n\ts_nop 3" ::: "memory"); SBAR(); } while (0)
template <int... Is> using iseq = std::integer_sequence<int, Is...>;
template <int N> using mkseq = std::make_integer_sequence<int, N>;
template <int N> using ic = std::integral_constant<int, N>;

constexpr int NB = 8, SQ = 208, DM = 640, H = 32, SK = 1024, DH = 128, HD = H * DH, NRT = 13;
constexpr unsigned SLOT = 16384, XST = 26624, XOFF = 5 * SLOT, Q2OFF = 6 * SLOT, MISC = Q2OFF + NRT * 4096, LDS_TOTAL = 163840;
static_assert(XOFF + 3 * XST <= LDS_TOTAL && MISC + 3 * 4096 == LDS_TOTAL, "lds map");
constexpr unsigned OBLK = NRT * 4096;
constexpr unsigned PST = 47104, POFF = 26624;
constexpr unsigned PS0 = LDS_TOTAL - PST, PS1 = 0, PS2 = PST, SCR2 = PST;
static_assert(SCR2 + 32768 + 512 <= 5 * SLOT && PS2 + PST <= PS0, "lds map 2");
constexpr size_t WS_CNT = 0, CNT_BYTES = 16384, WS_O = 1u << 20, WS_PART = 32u << 20, WS_STAMP = 64u << 20;
constexpr unsigned PARTB = 8 * 11264;
constexpr float QSCALE = 0.08838834764831845f * 1.4426950408889634f;
constexpr float L_LIMIT = 1.0e24f;
__host__ __device__ constexpr unsigned q1addr(int rt) { return rt < 8 ? 4 * SLOT + 4096 * rt : rt < 11 ? MISC + 4096 * (rt - 8) : SLOT + 4096 * (rt - 11); }

DI unsigned cvtpk(float lo, float hi) { f32x2 v = {lo, hi}; return __builtin_bit_cast(unsigned, __builtin_convertvector(v, bf16x2)); }
DI float fmul_s(float a, float b) { float r; asm("v_mul_f32_e32 %0, %1, %2" : "=v"(r) : "s"(b), "v"(a)); return r; }
DI float ex2(float x) { return __builtin_amdgcn_exp2f(x); }
DI bf16x8 rd128(unsigned base, int imm) { return *(const LAS bf16x8*)((const LAS char*)base + imm); }
DI s16x4 rdtr(unsigned base, int imm) { return __builtin_amdgcn_ds_read_tr16_b64_v4i16((LAS s16x4*)((LAS char*)base + imm)); }
DI bf16x8 cat(s16x4 lo, s16x4 hi) { return __builtin_shufflevector(lo, hi, 0, 1, 2, 3, 4, 5, 6, 7); }
DI void wr128f(unsigned base, int imm, f32x4 v) { *(LAS f32x4*)((LAS char*)base + imm) = v; }
DI f32x4 rd128f(unsigned base, int imm) { return *(const LAS f32x4*)((const LAS char*)base + imm); }
DI float red_add(float x) {
  auto a = __builtin_amdgcn_permlane16_swap(__float_as_uint(x), __float_as_uint(x), false, false); x = __uint_as_float(a[0]) + __uint_as_float(a[1]);
  auto b = __builtin_amdgcn_permlane32_swap(__float_as_uint(x), __float_as_uint(x), false, false); return __uint_as_float(b[0]) + __uint_as_float(b[1]); }
DI float red_max(float x) {
  auto a = __builtin_amdgcn_permlane16_swap(__float_as_uint(x), __float_as_uint(x), false, false); x = fmaxf(__uint_as_float(a[0]), __uint_as_float(a[1]));
  auto b = __builtin_amdgcn_permlane32_swap(__float_as_uint(x), __float_as_uint(x), false, false); return fmaxf(__uint_as_float(b[0]), __uint_as_float(b[1])); }
DI int lane_id() { int l; asm volatile("v_mbcnt_lo_u32_b32 %0, -1, 0\n\tv_mbcnt_hi_u32_b32 %0, -1, %0" : "=v"(l)); return l; }
DI rsrc_t mkrs(const void* p, unsigned bytes) { return __builtin_amdgcn_make_buffer_rsrc((void*)p, 0, bytes, 0x00020000); }

template <int IMM> DI void dma_nt(unsigned m0v, unsigned voff, rsrc_t rs, unsigned soff) {
  asm volatile("s_mov_b32 m0, %0\n\ts_nop 0\n\tbuffer_load_dwordx4 %1, %2, %3 offen offset:%4 nt lds" ::"s"(m0v), "v"(voff), "s"(rs), "s"(soff), "i"(IMM) : "memory");
}
DI void dma_df(unsigned m0v, unsigned voff, rsrc_t rs, unsigned soff) {
  asm volatile("s_mov_b32 m0, %0\n\ts_nop 0\n\tbuffer_load_dwordx4 %1, %2, %3 offen lds" ::"s"(m0v), "v"(voff), "s"(rs), "s"(soff) : "memory");
}
constexpr unsigned CODE_BYTES = 61440;
DI void touch_code(unsigned& w, unsigned long long pc, int idx) {
  const rsrc_t rs = __builtin_amdgcn_make_buffer_rsrc((void*)pc, 0, CODE_BYTES, 0x00020000);
  const unsigned l = lane_id(), vo = l < 15 ? 128 * (l + 15 * idx) : 0x80000000u;
  asm volatile("s_nop 4\n\tbuffer_load_dword %0, %1, %2, 0 offen" : "=v"(w) : "v"(vo), "s"(rs) : "memory");
}
DI void dma_sc1(unsigned m0v, unsigned voff, rsrc_t rs, unsigned soff) {
  asm volatile("s_mov_b32 m0, %0\n\ts_nop 0\n\tbuffer_load_dwordx4 %1, %2, %3 offen sc1 lds" ::"s"(m0v), "v"(voff), "s"(rs), "s"(soff) : "memory");
}
DI void touch80(unsigned& w0, unsigned& w1, const char* p) {
  const unsigned vo = 128 * lane_id();
  asm volatile("s_nop 4\n\tglobal_load_dword %0, %2, %3\n\tglobal_load_dword %1, %2, %3 offset:2048" : "=&v"(w0), "=&v"(w1) : "v"(vo), "s"(p) : "memory");
}
#define QLATE 0
#define OLATE 0
#define OAUX 16
#define XDMA dma_df
DI void mfma2(f32x4& c, const bf16x8& a0, const bf16x8& b0, const bf16x8& a1, const bf16x8& b1) {
  asm volatile("v_mfma_f32_16x16x32_bf16 %0, %1, %2, %0\n\tv_mfma_f32_16x16x32_bf16 %0, %3, %4, %0" : "+v"(c) : "v"(a0), "v"(b0), "v"(a1), "v"(b1));
}
DI void mfma1(f32x4& c, const bf16x8& a, const bf16x8& b) { asm volatile("v_mfma_f32_16x16x32_bf16 %0, %1, %2, %0" : "+v"(c) : "v"(a), "v"(b)); }
DI void poll_issue(unsigned& v, const unsigned* cnt) { const unsigned z = 0; asm volatile("s_nop 4\n\tglobal_load_dword %0, %1, %2 sc1" : "=v"(v) : "v"(z), "s"(cnt) : "memory"); }
DI void poll_acquire(unsigned v, const unsigned* cnt) {
  asm volatile("" : "+v"(v)::"memory");
  if (__builtin_amdgcn_readfirstlane(v) < 4u) {
#pragma nounroll
    for (int spin = 0; spin < (1 << 22); ++spin) {
      const unsigned u = __hip_atomic_load((gu32*)cnt, __ATOMIC_RELAXED, __HIP_MEMORY_SCOPE_AGENT);
      if (__builtin_amdgcn_readfirstlane(u) >= 4u) break;
      __builtin_amdgcn_s_sleep(2);
    }
  }
}
DI void signal_quad(unsigned* cnt) { if (lane_id() == 0) __hip_atomic_fetch_add((gu32*)cnt, 1u, __ATOMIC_RELAXED, __HIP_MEMORY_SCOPE_AGENT); }

#define STAMP(i)
#define FST(i)
#define PST_(i)
struct Args { const u16 *x0, *k0, *v0, *wq0, *wo0, *x1, *k1, *v1, *wq1, *wo1; char* ws; u16* out; };
struct Ctx {
  const u16 *x, *k, *v, *wq; const char *wo, *warm; char *oq, *part; u16* out; unsigned *cnt, *cnt2; unsigned long long* stp; unsigned ldsb; int own, nt, kq, idx; unsigned long long pc;
};

DI void slow16(const u16* Kg, const u16* Vg, int key0, int nstep, const bf16x8 (&q)[4], f32x4 (&o)[8], float& m, float& l) {
  const int lane = lane_id(), r = lane & 15, g = lane >> 4;
  m = -3.0e38f; l = 0.f;
#pragma unroll
  for (int d = 0; d < 8; ++d) o[d] = f32x4{};
#pragma nounroll
  for (int st = 0; st < nstep; ++st) {
    const int kb = nstep == 64 ? 16 * st : key0 + 128 * (st >> 1) + 16 * (st & 1);
    f32x4 S = {};
#pragma unroll
    for (int ks = 0; ks < 4; ++ks) S = MFMA16(*(const bf16x8*)(Kg + (size_t)(kb + r) * DH + 32 * ks + 8 * g), q[ks], S);
    const float mx = red_max(fmaxf(fmaxf(S[0], S[1]), fmaxf(S[2], S[3])));
    const float mn = fmaxf(m, mx), al = ex2(m - mn); m = mn;
    float ps = 0.f;
#pragma unroll
    for (int i = 0; i < 4; ++i) { S[i] = ex2(S[i] - mn); ps += S[i]; }
    l = l * al + ps;
    u32x2 pw = {cvtpk(S[0], S[1]), cvtpk(S[2], S[3])};
#pragma unroll
    for (int d = 0; d < 8; ++d) {
      s16x4 vf;
#pragma unroll
      for (int j = 0; j < 4; ++j) vf[j] = (short)Vg[(size_t)(kb + 4 * g + j) * DH + 16 * d + r];
      o[d] *= al;
      o[d] = MFMA16K16(vf, __builtin_bit_cast(s16x4, pw), o[d]);
    }
  }
  __builtin_amdgcn_s_waitcnt(0);
}
DI float nanprobe(const f32x4 (&o)[8]) {
  f32x4 t = {};
#pragma unroll
  for (int d = 0; d < 8; ++d) t += o[d] * 0.f;
  return (t[0] + t[1]) + (t[2] + t[3]);
}

template <int NR, class FD, class FW> DI void qproj(const Ctx& c, int cg, FD&& dma, FW&& wait) {
  f32x4 acc[NR][4]; bf16x8 w[4][2], x[NR][2];
  const unsigned lane = lane_id(), r16 = lane & 15, g = lane >> 4, q4 = (lane & 15) >> 2, p = lane & 3;
  const unsigned wb0 = c.ldsb + 1024 * (cg & 1) + 2048 * g + 64 * q4 + 16 * ((p >> 1) ^ (2 * (g & 1))) + 8 * (p & 1);
  const unsigned xb0 = c.ldsb + XOFF + (NR == 7 ? 0 : 7 * 2048) + 128 * r16 + 16 * (g ^ ((r16 >> 1) & 7));
#pragma unroll
  for (int r = 0; r < NR; ++r)
#pragma unroll
    for (int i = 0; i < 4; ++i) acc[r][i] = f32x4{};
  unsigned xs = 0, ws = SLOT * (cg >> 1), xp0, xp1, wp0, wp1;
  auto ldw = [&](int i) { const unsigned b = (i & 1) ? wp1 : wp0; const int im = 512 * (i >> 1);
    w[i][0] = cat(rdtr(b, im), rdtr(b, im + 256)); w[i][1] = cat(rdtr(b, im + 8192), rdtr(b, im + 8448)); };
  auto ldx = [&](int r) { x[r][0] = rd128(xp0, 2048 * r); x[r][1] = rd128(xp1, 2048 * r); };
  auto row = [&](int r) {
#pragma unroll
    for (int i = 0; i < 4; ++i) mfma2(acc[r][i], w[i][0], x[r][0], w[i][1], x[r][1]);
  };
  constexpr int LATE_R = QLATE;
  auto half = [&](int r, int h) { mfma2(acc[r][2 * h], w[2 * h][0], x[r][0], w[2 * h][1], x[r][1]); mfma2(acc[r][2 * h + 1], w[2 * h + 1][0], x[r][0], w[2 * h + 1][1], x[r][1]); };
  auto step = [&](auto K, auto F, int s) {
    constexpr bool FIRST = decltype(F)::value;
    xp0 = xb0 + xs; xp1 = xp0 ^ 64; wp0 = wb0 + ws; wp1 = wp0 ^ 32;
    xs = xs == 2 * XST ? 0 : xs + XST; ws += 2 * SLOT; ws = ws >= 5 * SLOT ? ws - 5 * SLOT : ws;
    wait(s);
    SBAR();
    BAR();
    if constexpr (FIRST) { STAMP(1); }
    SBAR();
    if constexpr (NR == 7) {
#pragma unroll
      for (int r = 0; r < 7; ++r) {
        if constexpr (!FIRST) half(r, 0);
        SBAR(); if (r) x[r - 1][0] = rd128(xp0, 2048 * (r - 1)); SBAR();
        if constexpr (!FIRST) half(r, 1);
        SBAR(); if (r) x[r - 1][1] = rd128(xp1, 2048 * (r - 1)); SBAR();
      }
      ldx(6);
#pragma unroll
      for (int i = 0; i < 4; ++i) { ldw(i); dma(K, s, 2 * i); if (i < 3) dma(K, s, 2 * i + 1); SBAR(); }
    } else {
      if constexpr (!FIRST) {
#pragma unroll
        for (int r = 6 - LATE_R; r < 6; ++r) row(r);
        __builtin_amdgcn_s_setprio(0);
        SBAR();
      }
#pragma unroll
      for (int i = 0; i < 4; ++i) { dma(K, s, i); ldw(i); SBAR(); }
#pragma unroll
      for (int r = 0; r < 6; ++r) { if (r < 4) dma(K, s, 4 + r); ldx(r); SBAR(); }
#pragma unroll
      for (int r = 0; r < 6 - LATE_R; ++r) row(r);
      __builtin_amdgcn_s_setprio(1);
      SBAR();
    }
  };
  asm volatile("s_nop 1" ::: "memory");
  step(ic<NR == 7 ? 0 : 3>{}, ic<1>{}, 0);
#pragma nounroll
  for (int s = 1; s < 8; ++s) step(ic<0>{}, ic<0>{}, s);
  step(ic<1>{}, ic<0>{}, 8);
  step(ic<2>{}, ic<0>{}, 9);
  BAR();
  if constexpr (NR == 6) {
#pragma unroll
    for (int r = 6 - LATE_R; r < 6; ++r) row(r);
    __builtin_amdgcn_s_setprio(0);
    SBAR();
  }
  dma(ic<4>{}, 10, 0);
  const unsigned ln = lane_id(), rr = ln & 15, gg = ln >> 4;
  const unsigned qw = c.ldsb + 2048 * (cg & 1) + 128 * rr + 16 * ((gg >> 1) ^ ((rr >> 1) & 7)) + 8 * (gg & 1);
  auto put = [&](int r) {
    const int rt = (NR == 7 ? 0 : 7) + r;
    const unsigned blk = (cg >> 1) ? Q2OFF + 4096 * rt : q1addr(rt);
#pragma unroll
    for (int i = 0; i < 4; ++i) {
      const f32x4 v = acc[r][i];
      *(LAS u32x2*)((qw ^ (32 * i)) + blk) = u32x2{cvtpk(fmul_s(v[0], QSCALE), fmul_s(v[1], QSCALE)), cvtpk(fmul_s(v[2], QSCALE), fmul_s(v[3], QSCALE))};
    }
  };
  SBAR();
  if constexpr (NR == 7) {
#pragma unroll
    for (int r = 0; r < 7; ++r) { row(r); SBAR(); if (r) put(r - 1); SBAR(); }
    MFMA_DONE();
    put(6);
  } else {
    MFMA_DONE();
#pragma unroll
    for (int r = 0; r < 6; ++r) put(r);
  }
}

struct OD { rsrc_t rw, ro; unsigned vwp, vwo, vo, mw, mo, lo; int rg; };
DI OD od_make(const Ctx& c, int rg) {
  OD d; const unsigned lane = lane_id(), xr = 16 * ((lane & 3) ^ (((lane >> 4) & 1) << 1));
  d.rw = mkrs(c.wo, 1024 * DM * 2); d.ro = mkrs(c.oq, 8 * OBLK);
  d.vwp = 1280 * (16 * rg + ((lane >> 2) & 7)) + 64 * (lane >> 5) + xr + 320 * c.nt + ((c.nt & 1) ? 64 : 0);
  d.vwo = 1280 * (16 * rg + (lane >> 2)) + xr + 320 * c.nt + ((c.nt & 1) ? 0 : 256);
  d.vo = 16 * lane; d.mw = c.ldsb + POFF + 5120 * rg; d.mo = c.ldsb + 1024 * rg; d.lo = 1024 * (24 + (rg & 1)); d.rg = rg;
  return d;
}
DI unsigned od_blk(const Ctx& c, int s) { return (unsigned)(s < 16 ? 4 * ((s >> 1) & 3) + 2 * (s >> 3) + (s & 1) : 64); }
DI void od_piece(const Ctx& c, const OD& d, unsigned u, unsigned st, int n) {
  const unsigned sw = u * (64 * 1280), so = u * (OBLK / 2), m = d.mw + st;
  if (n == 0) dma_df(m, d.vwp, d.rw, sw);
  else if (n == 1) dma_df(m + 1024, d.vwp, d.rw, sw + 128);
  else if (n == 2) dma_df(m + 3072, d.vwp, d.rw, sw + 10240);
  else if (n == 3) dma_df(m + 4096, d.vwp, d.rw, sw + 10240 + 128);
  else if (n == 4) dma_df(m + 2048, d.vwo, d.rw, sw);
  else if (n < 11) dma_sc1(d.mo + st + 4096 * (n - 5), d.vo, d.ro, so + 1024 * d.rg + 4096 * (n - 5));
  else dma_sc1(c.ldsb + d.lo + st, d.vo, d.ro, so + d.lo);
}
template <int NC, int NR> DI void oproj(const Ctx& c, int rg) {
  const OD d = od_make(c, rg);
  f32x4 acc[NR][NC]; bf16x8 w[NC][2], x[NR][2];
  const int c0 = NC == 3 ? 3 * (rg >> 1) : 6 + 2 * (rg >> 1), r0 = NR == 7 ? 0 : 7;
  unsigned wb[NC], xb;
  {
    const unsigned lane = lane_id(), g = lane >> 4, q4 = (lane & 15) >> 2, p = lane & 3;
    const unsigned in = 64 * (4 * (g & 1) + q4) + 16 * ((p >> 1) ^ (2 * (g & 1))) + 8 * (p & 1);
#pragma unroll
    for (int i = 0; i < NC; ++i) {
      const int ct = c0 + i;
      const unsigned off = ct < 8 ? 512 * (ct >> 1) + 3072 * (g >> 1) : 2048 + 512 * (g >> 1);
      wb[i] = c.ldsb + POFF + off + (in ^ (32 * (ct & 1)));
    }
    xb = c.ldsb + 16 * lane + 2048 * r0;
  }
#pragma unroll
  for (int r = 0; r < NR; ++r)
#pragma unroll
    for (int i = 0; i < NC; ++i) acc[r][i] = f32x4{};
  unsigned st = PS0, nx = PS1, fr = PS2;
  auto ldw = [&](int i) { const unsigned wp = wb[i] + st; w[i][0] = cat(rdtr(wp, 0), rdtr(wp, 5120)); w[i][1] = cat(rdtr(wp, 10240), rdtr(wp, 15360)); };
  auto ldx = [&](int r) { const unsigned xp = xb + st; x[r][0] = rd128(xp, 2048 * r); x[r][1] = rd128(xp, 2048 * r + 1024); };
  auto row = [&](int r) {
#pragma unroll
    for (int i = 0; i < NC; ++i) mfma2(acc[r][i], w[i][0], x[r][0], w[i][1], x[r][1]);
  };
#pragma unroll
  for (int r = 0; r < NR; ++r) { x[r][0] = bf16x8{}; x[r][1] = bf16x8{}; }
#pragma unroll
  for (int i = 0; i < NC; ++i) { w[i][0] = bf16x8{}; w[i][1] = bf16x8{}; }
  unsigned pv = 0;
  const bool me = NC == 3 && rg == 1;
  STAMP(8);
#pragma nounroll
  for (int s = 0; s < 16; ++s) {
    const unsigned u = od_blk(c, s + 2), xp = xb + st;
    if constexpr (NC == 2) { if (s == 0) VMW(13); else VMW(8); }
    else if constexpr (NR == 7) { if (s) VMW(4); }
    else {
      if (s) VMW(4);
      if (me && s == 6) poll_acquire(pv, c.cnt + 32);
    }
    SBAR();
    BAR();
    if (NC == 3 && NR == 7 && rg == 2) PST_(s);
    SBAR();
    if constexpr (NC == 3) {
#pragma unroll
      for (int r = 0; r < NR; ++r) {
        mfma2(acc[r][0], w[0][0], x[r][0], w[0][1], x[r][1]);
        SBAR(); if (r) x[r - 1][0] = rd128(xp, 2048 * (r - 1)); SBAR();
        mfma2(acc[r][1], w[1][0], x[r][0], w[1][1], x[r][1]);
        SBAR(); if (r) x[r - 1][1] = rd128(xp, 2048 * (r - 1) + 1024); SBAR();
        mfma2(acc[r][2], w[2][0], x[r][0], w[2][1], x[r][1]);
        SBAR();
      }
      if constexpr (NR == 6) { if (me && s == 5) poll_issue(pv, c.cnt + 32); PIN(pv); }
      ldx(NR - 1); od_piece(c, d, u, fr, 0);
#pragma unroll
      for (int i = 0; i < 3; ++i) { ldw(i); od_piece(c, d, u, fr, 1 + i); SBAR(); }
    } else {
      if (NR == 6 && rg == 0 && s == 1) signal_quad(c.cnt + 32);
#pragma unroll
      for (int i = 0; i < 2; ++i) { od_piece(c, d, u, fr, 4 + i); ldw(i); SBAR(); }
#pragma unroll
      for (int r = 0; r < NR; ++r) { if (r < 6) od_piece(c, d, u, fr, 6 + r); ldx(r); SBAR(); }
#pragma unroll
      for (int r = 0; r < NR; ++r) row(r);
      SBAR();
    }
    const unsigned t = st; st = nx; nx = fr; fr = t;
  }
  STAMP(9);
  if constexpr (NC == 3) {
    LGKM0(); SBAR();
#pragma unroll
    for (int r = 0; r < NR; ++r) row(r);
  }
  MFMA_DONE();
  STAMP(10);
  constexpr int NT = NR * NC, NU = (NT + 1) / 2;
  const int wv = NC == 3 ? rg : 4 + rg;
  const unsigned ln = lane_id(), pv0 = 16 * ln + 11264 * wv;
  const rsrc_t rp = mkrs(c.part, 16 * PARTB);
  f32x4 own[3][2];
  auto scatter = [&](auto K) {
    constexpr int k = decltype(K)::value;
#pragma unroll
    for (int m = 0; m < NU; ++m) {
      const f32x4 u = acc[(2 * m) / NC][(2 * m) % NC], v = 2 * m + 1 < NT ? acc[(2 * m + 1) / NC][(2 * m + 1) % NC] : f32x4{};
      if ((m & 3) == k) { own[m >> 2][0] = u; own[m >> 2][1] = v; }
      else __builtin_amdgcn_raw_buffer_store_b128(u32x4{cvtpk(u[0], u[1]), cvtpk(u[2], u[3]), cvtpk(v[0], v[1]), cvtpk(v[2], v[3])}, rp, pv0 + (4 * k + c.nt) * PARTB + 1024 * m, 0, 16);
    }
#pragma unroll
    for (int e = 0; e < 3; ++e) if (4 * e + k >= NU) { own[e][0] = f32x4{}; own[e][1] = f32x4{}; }
  };
  if (c.kq == 0) scatter(ic<0>{}); else if (c.kq == 1) scatter(ic<1>{}); else if (c.kq == 2) scatter(ic<2>{}); else scatter(ic<3>{});
  VMW(0);
  BAR();
  if (wv == 0) {
    signal_quad(c.cnt2);
#pragma nounroll
    for (int spin = 0; spin < (1 << 22); ++spin) {
      const unsigned u = __hip_atomic_load((gu32*)c.cnt2, __ATOMIC_RELAXED, __HIP_MEMORY_SCOPE_AGENT);
      if (__builtin_amdgcn_readfirstlane(u) >= 4u) break;
      __builtin_amdgcn_s_sleep(1);
    }
  }
  BAR();
  u32x4 pp[3][3];
#pragma unroll
  for (int e = 0; e < 3; ++e)
#pragma unroll
    for (int q = 1; q < 4; ++q) {
      const int m = 4 * e + c.kq;
      pp[e][q - 1] = __builtin_amdgcn_raw_buffer_load_b128(rp, m < NU ? pv0 + (4 * ((c.kq + q) & 3) + c.nt) * PARTB + 1024 * m : 0x80000000u, 0, 16);
    }
#pragma unroll
  for (int e = 0; e < 3; ++e)
#pragma unroll
    for (int h = 0; h < 2; ++h) {
      const int n = 2 * (4 * e + c.kq) + h;
      f32x4 v = own[e][h];
#pragma unroll
      for (int q = 0; q < 3; ++q) {
        v[0] += __uint_as_float(pp[e][q][2 * h] << 16); v[1] += __uint_as_float(pp[e][q][2 * h] & 0xffff0000u);
        v[2] += __uint_as_float(pp[e][q][2 * h + 1] << 16); v[3] += __uint_as_float(pp[e][q][2 * h + 1] & 0xffff0000u);
      }
      if (n < NT) {
        const int rt = r0 + n / NC, ct = c0 + n % NC;
        const int col = 160 * c.nt + (ct < 8 ? ((c.nt & 1) ? 32 : 0) + 16 * ct : ((c.nt & 1) ? 0 : 128) + 16 * (ct - 8));
        *(u32x2*)(c.out + (size_t)(16 * rt + (ln & 15)) * (2 * DM) + col + 4 * (ln >> 4)) = u32x2{cvtpk(v[0], v[1]), cvtpk(v[2], v[3])};
      }
    }
  STAMP(11);
}

struct AS {
  f32x4 o[3][8], S[2][3], negm[3]; bf16x8 q[3][4], F[2][4]; u32x4 P[2][3]; float l[3];
  unsigned kp[2], vp[2], kn[2];
};
template <int N, int I> DI void a_frag(AS& a) {
  constexpr int sub = (N >> 2) & 1, b = N & 3;
  if constexpr ((b & 1) == 0) {
    constexpr int kt = 2 * sub + (b >> 1);
    a.F[N & 1][I] = rd128(N == 8 ? a.kn[I & 1] : a.kp[I & 1], 8192 * (I >> 1) + 2048 * kt);
  } else {
    constexpr int cc = 4 * (b >> 1) + I, imm = 8192 * sub + 512 * (cc >> 1);
    a.F[N & 1][I] = cat(rdtr(a.vp[cc & 1], imm), rdtr(a.vp[cc & 1], imm + 4096));
  }
}
template <int N, bool MM, bool SM, int NX, bool Z, int G> DI void a_gap(AS& a) {
  constexpr int sub = N >> 2, b = N & 3;
  if constexpr (MM) {
    if constexpr ((b & 1) == 0) {
      constexpr int rt = G >> 2, ks = G & 3, sb = b >> 1;
      if constexpr (ks == 0) { if constexpr (Z) a.S[sb][rt] = MFMA16(a.F[N & 1][0], a.q[rt][0], f32x4{}); else a.S[sb][rt] = MFMA16(a.F[N & 1][0], a.q[rt][0], a.negm[rt]); }
      else a.S[sb][rt] = MFMA16(a.F[N & 1][ks], a.q[rt][ks], a.S[sb][rt]);
    } else {
      constexpr int i = G / 3, rt = G % 3, cc = 4 * (b >> 1) + i;
      if constexpr (Z) a.o[rt][cc] = MFMA16(a.F[N & 1][i], __builtin_bit_cast(bf16x8, a.P[sub ^ 1][rt]), f32x4{});
      else a.o[rt][cc] = MFMA16(a.F[N & 1][i], __builtin_bit_cast(bf16x8, a.P[sub ^ 1][rt]), a.o[rt][cc]);
    }
    SBAR();
  }
  if constexpr (NX >= 0 && G < 4) a_frag<NX, G>(a);
  if constexpr (SM) {
    constexpr int sb = (b == 1 || b == 2) ? 0 : 1, e0 = (b & 1) ? 0 : 6, pb = b == 0 ? sub ^ 1 : sub;
    if constexpr ((G & 1) == 0) { constexpr int e = e0 + (G >> 1); a.S[sb][e >> 2][e & 3] = ex2(a.S[sb][e >> 2][e & 3]); PIN(a.S[sb][e >> 2]); }
    else {
      constexpr int e = e0 + (G >> 1), rt = e >> 2;
      a.l[rt] = a.l[rt] + a.S[sb][rt][e & 3]; PIN(a.l[rt]);
      if constexpr (e & 1) { a.P[pb][rt][2 * sb + ((e & 3) >> 1)] = cvtpk(a.S[sb][rt][(e & 3) - 1], a.S[sb][rt][e & 3]); PIN(a.P[pb][rt]); }
    }
  }
  SBAR();
}
template <int N, bool MM, bool SM, int NX, bool Z, int... Gs> DI void a_block_(AS& a, iseq<Gs...>) { LGKM0(); SBAR(); (a_gap<N, MM, SM, NX, Z, Gs>(a), ...); }
template <int N, bool MM, bool SM, int NX, bool Z = false> DI void a_block(AS& a) { a_block_<N, MM, SM, NX, Z>(a, mkseq<12>{}); }
template <bool FIRST> DI void a_period(AS& a) {
  if constexpr (FIRST) {
    a_block<0, true, false, -1, true>(a);
#pragma unroll
    for (int rt = 0; rt < 3; ++rt) {
      const float mx = red_max(fmaxf(fmaxf(a.S[0][rt][0], a.S[0][rt][1]), fmaxf(a.S[0][rt][2], a.S[0][rt][3])));
      a.negm[rt] = f32x4{-mx, -mx, -mx, -mx}; a.S[0][rt] -= mx; PIN(a.negm[rt]);
    }
    SBAR();
    a_block<1, false, true, 2>(a); a_block<2, true, true, -1>(a); a_block<3, false, true, 4>(a);
  } else {
    a_block<0, true, true, 1>(a); a_block<1, true, true, 2>(a); a_block<2, true, true, 3>(a); a_block<3, true, true, 4>(a);
  }
  a_block<4, true, true, 5>(a); a_block<5, true, true, 6, FIRST>(a); a_block<6, true, true, 7>(a);
  if constexpr (FIRST) VMW(0);
  BAR();
  a_block<7, true, true, 8, FIRST>(a);
}

DI void path_a(const Ctx& c, int wi) {
  STAMP(0);
  {
    const rsrc_t rx = mkrs(c.x, SQ * DM * 2);
    const unsigned lane = lane_id(), vox = 1280u * (lane >> 3) + 16u * ((lane & 7) ^ ((4 * (wi & 1) + (lane >> 4)) & 7));
    auto xp = [&](int s, unsigned stage, int n) { const int pc = n < 6 ? wi + 4 * n : 24 + (wi & 1); XDMA(c.ldsb + XOFF + stage + 1024 * pc, vox, rx, 128u * s + 10240u * pc); };
#pragma unroll
    for (int n = 0; n < 7; ++n) xp(0, 0, n);
#pragma unroll
    for (int n = 0; n < 7; ++n) xp(1, XST, n);
    unsigned xf = XST;
    qproj<7>(c, wi,
      [&](auto K, int s, int n) { if constexpr (decltype(K)::value == 0) xp(s + 2, xf, n); },
      [&](int s) { if (s == 9) VMW(0); else VMW(7); xf = xf == 2 * XST ? 0 : xf + XST; });
  }
  FST(11);
  BAR();
  FST(12);
  AS a;
  unsigned kb0, vb0;
  { const unsigned lane = lane_id(), r16 = lane & 15, g = lane >> 4, q4 = (lane & 15) >> 2, p = lane & 3;
    kb0 = c.ldsb + 128 * r16 + 16 * (g ^ ((r16 >> 1) & 7));
    vb0 = c.ldsb + SLOT + 2048 * (g >> 1) + 64 * (4 * (g & 1) + q4) + 16 * ((p >> 1) ^ (2 * (g & 1))) + 8 * (p & 1); }
  unsigned sl = 0;
#pragma nounroll
  for (int hd = 0; hd < 2; ++hd) {
#pragma unroll
    for (int rt = 0; rt < 3; ++rt) {
      const int t = 3 * wi + rt;
      const unsigned blk = hd ? Q2OFF + 4096 * t : (t < 8 ? 4 * SLOT + 4096 * t : t < 11 ? MISC + 4096 * (t - 8) : SLOT + 4096 * (t - 11));
      const unsigned q0 = kb0 + blk, q1 = q0 ^ 64;
      a.q[rt][0] = rd128(q0, 0); a.q[rt][1] = rd128(q1, 0); a.q[rt][2] = rd128(q0, 2048); a.q[rt][3] = rd128(q1, 2048);
      a.l[rt] = 0.f; a.P[0][rt] = u32x4{}; a.P[1][rt] = u32x4{};
    }
    a.kn[0] = kb0 + sl; a.kn[1] = a.kn[0] ^ 64;
    a_frag<8, 0>(a); a_frag<8, 1>(a); a_frag<8, 2>(a); a_frag<8, 3>(a);
    if (hd == 0) BAR();
    STAMP(2 + 3 * hd);
    auto setp = [&]() {
      a.kp[0] = a.kn[0]; a.kp[1] = a.kn[1]; a.vp[0] = vb0 + sl; a.vp[1] = a.vp[0] ^ 32;
      sl = sl == 4 * SLOT ? 0 : sl + 2 * SLOT;
      a.kn[0] = kb0 + sl; a.kn[1] = a.kn[0] ^ 64;
      SBAR();
    };
    setp();
    a_period<true>(a);
#pragma nounroll
    for (int t = 1; t < 16; ++t) { if (wi == 0) PST_(16 * hd + t); setp(); a_period<false>(a); }
    a.vp[0] = vb0 + sl; a.vp[1] = a.vp[0] ^ 32;
    SBAR();
    a_block<0, false, true, 1>(a); a_block<1, true, false, -1>(a);
    a_frag<3, 0>(a); a_frag<3, 1>(a); a_frag<3, 2>(a); a_frag<3, 3>(a);
    a_block<3, true, false, -1>(a);
    if (hd) BAR();
    STAMP(3 + 3 * hd);
    const rsrc_t ro = mkrs(c.oq + (size_t)(2 * c.own + hd) * OBLK, OBLK);
    const unsigned ov = 16 * lane_id() + 6144 * wi;
#pragma unroll
    for (int rt = 0; rt < 3; ++rt) {
      float l = red_add(a.l[rt]);
      if (__builtin_expect(__any(!(l + nanprobe(a.o[rt]) < L_LIMIT)), 0)) {
        float m;
        slow16(c.k + (size_t)hd * SK * DH, c.v + (size_t)hd * SK * DH, 0, 64, a.q[rt], a.o[rt], m, l);
        l = red_add(l);
      }
      const float il = __builtin_amdgcn_rcpf(l);
#pragma unroll
      for (int kk = 0; kk < 4; ++kk) {
        const f32x4 u = a.o[rt][2 * kk] * il, v = a.o[rt][2 * kk + 1] * il;
        u32x4 w = {cvtpk(u[0], u[1]), cvtpk(u[2], u[3]), cvtpk(v[0], v[1]), cvtpk(v[2], v[3])};
        __builtin_amdgcn_raw_buffer_store_b128(w, ro, ov + 2048 * rt + 1024 * (kk & 1) + (OBLK / 2) * (kk >> 1), 0, OAUX);
      }
    }
    STAMP(4 + 3 * hd);
  }
}

DI void path_b(const Ctx& c, int j) {
  STAMP(0);
  const rsrc_t rk = mkrs(c.k, 2 * SK * DH * 2), rv = mkrs(c.v, 2 * SK * DH * 2);
  unsigned vk0, vk1, vv, wm0 = 0, wm1 = 0;
  { const unsigned i = lane_id();
    vk0 = 256 * (i >> 3) + 16 * ((i & 7) ^ (i >> 4)); vk1 = (vk0 ^ 64) + 2048;
    vv = 256 * ((i >> 2) & 7) + 64 * (i >> 5) + 16 * ((i & 3) ^ (((i >> 4) & 1) << 1)); }
  auto kpc = [&](int t, unsigned slot, int n) {
    const unsigned m = c.ldsb + slot + 2048 * j, so = (unsigned)(64 * t + 16 * j) * 256u;
    if (n == 0) dma_nt<0>(m, vk0, rk, so); if (n == 1) dma_nt<128>(m + 8064, vk0, rk, so); if (n == 2) dma_nt<0>(m + 1024, vk1, rk, so); if (n == 3) dma_nt<128>(m + 9088, vk1, rk, so);
  };
  auto vpc = [&](int t, unsigned slot, int n) {
    const unsigned m = c.ldsb + slot + 4096 * j, so = (unsigned)(64 * t - 32 + 16 * j) * 256u;
    if (n == 0) dma_nt<0>(m, vv, rv, so); if (n == 1) dma_nt<128>(m + 896, vv, rv, so); if (n == 2) dma_nt<2048>(m, vv, rv, so); if (n == 3) dma_nt<2176>(m + 896, vv, rv, so);
  };
  auto kdma = [&](int t, unsigned slot) { kpc(t, slot, 0); kpc(t, slot, 1); kpc(t, slot, 2); kpc(t, slot, 3); };
  auto vdma = [&](int t, unsigned slot) { vpc(t, slot, 0); vpc(t, slot, 1); vpc(t, slot, 2); vpc(t, slot, 3); };
  {
    const rsrc_t rw = mkrs(c.wq, (DM - 1) * HD * 2 + 512);
    const unsigned i = lane_id(), vw0 = 8192 * ((i >> 2) & 7) + 64 * (i >> 5) + 16 * (i & 3), vw1 = (vw0 ^ 32) + 65536;
    auto wpc = [&](int it, unsigned slot, int n) {
      dma_df(c.ldsb + slot + 4096 * j + 1024 * n, n < 2 ? vw0 : vw1, rw, (unsigned)(64 * (it >> 1) + 16 * j) * 8192u + 256 * (it & 1) + 128 * (n & 1));
    };
    if (j == 3) touch_code(wm0, c.pc, c.idx);
#pragma nounroll
    for (int it = 0; it < 5; ++it) { wpc(it, it * SLOT, 0); wpc(it, it * SLOT, 1); wpc(it, it * SLOT, 2); wpc(it, it * SLOT, 3); }
    unsigned f0 = SLOT, f1 = 2 * SLOT;
    auto adv = [](unsigned s_) { s_ += 2 * SLOT; return s_ >= 5 * SLOT ? s_ - 5 * SLOT : s_; };
    qproj<6>(c, j,
      [&](auto K, int s, int n) {
        constexpr int k = decltype(K)::value;
        if constexpr (k == 0) { if (n < 4) wpc(2 * s + 3, f0, n); else wpc(2 * s + 4, f1, n - 4); }
        if constexpr (k == 1) { if (n < 4) wpc(19, 4 * SLOT, n); else kpc(0, 0, n - 4);
          if (n == 7) { PIN(wm0); touch80(wm0, wm1, c.warm + 10240 * j); } }
        if constexpr (k == 2) { if (n == 0) { if (j >= 2) vdma(0, SLOT); } else if (n >= 4) kpc(1, 2 * SLOT, n - 4); }
        if constexpr (k == 4) vdma(1, 3 * SLOT);
      },
      [&](int s) { if (s == 0) VMW(12); else if (s == 9) VMW(6); else VMW(4); f0 = adv(f0); f1 = adv(f1); });
  }
  VMW(8);
  asm volatile("" ::"v"(wm0), "v"(wm1));
  BAR();
  unsigned qb, vb0;
  { const unsigned lane = lane_id(), r16 = lane & 15, g = lane >> 4, q4 = (lane & 15) >> 2, p = lane & 3;
    qb = c.ldsb + 128 * r16 + 16 * (g ^ ((r16 >> 1) & 7));
    vb0 = c.ldsb + SLOT + 8192 * ((j & 1) ^ 1) + 2048 * (g >> 1) + 64 * (4 * (g & 1) + q4) + 16 * ((p >> 1) ^ (2 * (g & 1))) + 8 * (p & 1); }
  bf16x8 q[4], vf[8]; f32x4 o[8], os[8], negm = {}; float l = 0.f, ms = 0.f, ls = 0.f; u32x4 P = {};
  unsigned sl = 0, fr = 4 * SLOT, pq = 0;
  auto rdq = [&](unsigned blk) { const unsigned q0 = qb + blk, q1 = q0 ^ 64; q[0] = rd128(q0, 0); q[1] = rd128(q1, 0); q[2] = rd128(q0, 2048); q[3] = rd128(q1, 2048); };
  auto rdv = [&]() {
    const unsigned v0 = vb0 + sl, v1 = v0 ^ 32;
#pragma unroll
    for (int cc = 0; cc < 8; ++cc) vf[cc] = cat(rdtr((cc & 1) ? v1 : v0, 512 * (cc >> 1)), rdtr((cc & 1) ? v1 : v0, 512 * (cc >> 1) + 4096));
  };
  auto put = [&](unsigned scr) {
    const unsigned lane = lane_id(), sc = c.ldsb + scr + 16 * lane + 8192 * j;
#pragma unroll
    for (int cc = 0; cc < 8; ++cc) wr128f(sc, 1024 * cc, os[cc]);
    if (lane < 16) { *(LAS float*)(c.ldsb + scr + 32768 + 128 * j + 4 * lane) = ms; *(LAS float*)(c.ldsb + scr + 32768 + 128 * j + 64 + 4 * lane) = ls; }
  };
  auto merge = [&](int hd, unsigned scr) {
    const unsigned lane = lane_id(), sc = c.ldsb + scr + 16 * lane + 2048 * j, ml = c.ldsb + scr + 32768 + 4 * (lane & 15);
    float mw[4], lw[4];
#pragma unroll
    for (int w = 0; w < 4; ++w) { mw[w] = *(const LAS float*)(ml + 128 * w); lw[w] = *(const LAS float*)(ml + 128 * w + 64); }
    const float mt = fmaxf(fmaxf(mw[0], mw[1]), fmaxf(mw[2], mw[3]));
    float lt = 0.f;
#pragma unroll
    for (int w = 0; w < 4; ++w) { mw[w] = ex2(mw[w] - mt); lt += mw[w] * lw[w]; }
    const float ir = __builtin_amdgcn_rcpf(lt);
    f32x4 u = {}, v = {};
#pragma unroll
    for (int w = 0; w < 4; ++w) { u += rd128f(sc, 8192 * w) * (mw[w] * ir); v += rd128f(sc, 8192 * w + 1024) * (mw[w] * ir); }
    u32x4 w4 = {cvtpk(u[0], u[1]), cvtpk(u[2], u[3]), cvtpk(v[0], v[1]), cvtpk(v[2], v[3])};
    __builtin_amdgcn_raw_buffer_store_b128(w4, mkrs(c.oq + (size_t)(2 * c.own + hd) * OBLK, OBLK), 16 * lane + 12 * 2048 + 1024 * (j & 1) + (OBLK / 2) * (j >> 1), 0, OAUX);
  };
#pragma unroll
  for (int cc = 0; cc < 8; ++cc) { os[cc] = f32x4{}; vf[cc] = bf16x8{}; }
  int t = 0;
  unsigned long long ts = 0;
  auto next = [&]() {
    if (t < 18) VMW(8);
    else {
      if (t == 18) put(Q2OFF);
      if (t == 19) merge(0, Q2OFF);
      if (t == 21 && j == 0) signal_quad(c.cnt);
      if (t == 19 || (t == 21 && j == 0)) VMW(9);
      else if (t == 30) { if (j < 2) VMW(16); else VMW(12); }
      else if (t == 31) VMW(12);
      else VMW(8);
      if (t == 29 && j == 0) poll_acquire(pq, c.cnt);
    }
    if (j == 3 && t < 29) { while (__builtin_amdgcn_s_memrealtime() - ts < PACE) __builtin_amdgcn_s_sleep(1); }
    BAR();
    ts = __builtin_amdgcn_s_memrealtime();
    fr = sl; sl = sl == 4 * SLOT ? 0 : sl + 2 * SLOT; ++t;
    if (t < 28) { kdma(t + 2, fr); vdma(t + 2, fr + SLOT); }
    else {
      if (t == 28 && j == 3) touch_code(pq, c.pc, c.idx);
      if (t == 29 && j == 0) poll_issue(pq, c.cnt);
      if (t < 30) kdma(t + 2, fr);
      if (t < 30 || (t == 30 && j < 2)) vdma(t + 2, fr + SLOT);
      if (t == 30 || t == 32) {
        const OD d = od_make(c, j);
#pragma unroll
        for (int n = 0; n < 12; ++n) od_piece(c, d, t == 30 ? 0 : 1, t == 30 ? PS0 : PS1, n);
      }
    }
    PIN(pq);
  };
  rdq(q1addr(12));
  __builtin_amdgcn_s_setprio(3);
  BAR();
  kdma(2, fr); vdma(2, fr + SLOT);
  if (j >= 2) { VMW(8); BAR(); fr = sl; sl = 2 * SLOT; t = 1; kdma(3, fr); vdma(3, fr + SLOT); }
#pragma nounroll
  for (int hd = 0; hd < 2; ++hd) {
    if (hd) rdq(Q2OFF + 12 * 4096);
#pragma unroll
    for (int cc = 0; cc < 8; ++cc) o[cc] = f32x4{};
    negm = f32x4{}; l = 0.f;
#pragma nounroll
    for (int u = 0; u < 8; ++u) {
      if ((j & 1) == 0) rdv();
      {
        const unsigned k0 = qb + sl + 4096 * (j & 1), k1 = k0 ^ 64;
        f32x4 S[2];
#pragma unroll
        for (int h = 0; h < 2; ++h) {
          S[h] = MFMA16(rd128(k0, 2048 * h), q[0], negm); S[h] = MFMA16(rd128(k1, 2048 * h), q[1], S[h]);
          S[h] = MFMA16(rd128(k0, 2048 * h + 8192), q[2], S[h]); S[h] = MFMA16(rd128(k1, 2048 * h + 8192), q[3], S[h]);
        }
        if (u == 0) {
          const float mx = red_max(fmaxf(fmaxf(fmaxf(S[0][0], S[0][1]), fmaxf(S[0][2], S[0][3])), fmaxf(fmaxf(S[1][0], S[1][1]), fmaxf(S[1][2], S[1][3]))));
          negm = f32x4{-mx, -mx, -mx, -mx}; S[0] -= mx; S[1] -= mx;
        }
#pragma unroll
        for (int i = 0; i < 8; ++i) { S[i >> 2][i & 3] = ex2(S[i >> 2][i & 3]); l += S[i >> 2][i & 3]; PIN(l); }
        P = u32x4{cvtpk(S[0][0], S[0][1]), cvtpk(S[0][2], S[0][3]), cvtpk(S[1][0], S[1][1]), cvtpk(S[1][2], S[1][3])};
      }
      next();
      if (j & 1) rdv();
#pragma unroll
      for (int cc = 0; cc < 8; ++cc) o[cc] = MFMA16(vf[cc], __builtin_bit_cast(bf16x8, P), o[cc]);
      if (t == 32) break;
      next();
    }
    ls = red_add(l); ms = -negm[0];
    if (__builtin_expect(__any(!(ls + nanprobe(o) < L_LIMIT)), 0)) {
      slow16(c.k + (size_t)hd * SK * DH, c.v + (size_t)hd * SK * DH, 32 * j, 16, q, o, ms, ls);
      ls = red_add(ls);
    }
#pragma unroll
    for (int cc = 0; cc < 8; ++cc) os[cc] = o[cc];
  }
  put(SCR2);
  BAR();
  merge(1, SCR2);
  __builtin_amdgcn_s_setprio(0);
}

__global__ __launch_bounds__(512, 2) void fused_k(Args ar) {
  extern __shared__ __attribute__((aligned(16))) char lds[];
  const int wid = __builtin_amdgcn_readfirstlane(threadIdx.x >> 6);
  const int xcd = blockIdx.x & 7, idx = blockIdx.x >> 3, br = xcd >> 2, kq = 2 * (xcd & 1) + ((idx >> 2) & 1), b = 4 * ((xcd >> 1) & 1) + (idx >> 3), hp = 4 * kq + (idx & 3);
  Ctx c;
  c.pc = __builtin_amdgcn_s_getpc() & ~255ull; c.idx = idx;
  c.ldsb = (unsigned)(size_t)(LAS char*)lds;
  c.x = (br ? ar.x1 : ar.x0) + (size_t)b * SQ * DM;
  c.k = (br ? ar.k1 : ar.k0) + ((size_t)b * H + 2 * hp) * SK * DH;
  c.v = (br ? ar.v1 : ar.v0) + ((size_t)b * H + 2 * hp) * SK * DH;
  c.wq = (br ? ar.wq1 : ar.wq0) + 2 * hp * DH;
  const char* wo = (const char*)(br ? ar.wo1 : ar.wo0);
  c.wo = wo + (size_t)kq * 1024 * DM * 2;
  c.warm = wo + (size_t)((xcd & 3) * 32 + idx) * 40960;
  c.oq = ar.ws + WS_O + (size_t)((br * NB + b) * H + 8 * kq) * OBLK;
  c.part = ar.ws + WS_PART + (size_t)((br * NB + b) * 16) * PARTB;
  c.out = ar.out + (size_t)b * SQ * (2 * DM) + br * DM;
  c.cnt2 = (unsigned*)(ar.ws + WS_CNT) + ((br * NB + b) * 4 + (hp & 3)) * 64 + 16; c.kq = kq;
  c.cnt = (unsigned*)(ar.ws + WS_CNT) + ((br * NB + b) * 4 + kq) * 64;
  c.stp = (unsigned long long*)(ar.ws + WS_STAMP) + (blockIdx.x * 8 + wid) * 32;
  c.own = hp & 3; c.nt = hp & 3;
  if (wid < 4) path_a(c, wid); else path_b(c, wid - 4);
  if (wid < 4) { if (wid & 1) oproj<3, 6>(c, wid); else oproj<3, 7>(c, wid); }
  else { if (wid & 1) oproj<2, 7>(c, wid - 4); else oproj<2, 6>(c, wid - 4); }
}

}

extern "C" void kernel_launch(void* const* d_in, const int* in_sizes, int n_in, void* d_out, int out_size, void* d_ws, size_t ws_size, hipStream_t stream) {
  using namespace kk;
  Args a;
  a.x0 = (const u16*)d_in[0]; a.k0 = (const u16*)d_in[1]; a.v0 = (const u16*)d_in[2]; a.wq0 = (const u16*)d_in[3]; a.wo0 = (const u16*)d_in[4];
  a.x1 = (const u16*)d_in[5]; a.k1 = (const u16*)d_in[6]; a.v1 = (const u16*)d_in[7]; a.wq1 = (const u16*)d_in[8]; a.wo1 = (const u16*)d_in[9];
  a.ws = (char*)d_ws; a.out = (u16*)d_out;
  (void)hipFuncSetAttribute((const void*)fused_k, hipFuncAttributeMaxDynamicSharedMemorySize, (int)LDS_TOTAL);
  (void)hipMemsetAsync((char*)d_ws + WS_CNT, 0, CNT_BYTES, stream);
  fused_k<<<256, 512, LDS_TOTAL, stream>>>(a);
}
```

```cpp
#define PACE 116
#define MAP 2
#include <hip/hip_runtime.h>
#include <hip/hip_bf16.h>
#include <utility>

namespace kk {
typedef unsigned short u16;
typedef short bf16x8 __attribute__((ext_vector_type(8)));
typedef short s16x4 __attribute__((ext_vector_type(4)));
typedef float f32x4 __attribute__((ext_vector_type(4)));
typedef float f32x2 __attribute__((ext_vector_type(2)));
typedef __bf16 bf16x2 __attribute__((ext_vector_type(2)));
typedef unsigned u32x4 __attribute__((ext_vector_type(4)));
typedef unsigned u32x2 __attribute__((ext_vector_type(2)));
typedef __amdgpu_buffer_rsrc_t rsrc_t;
typedef __attribute__((address_space(1))) unsigned gu32;
#define LAS __attribute__((address_space(3)))
#define DI __device__ __forceinline__
#define SBAR() __builtin_amdgcn_sched_barrier(0)
#define PIN(x) asm volatile("" : "+v"(x))
#define MFMA16(a, b, c) __builtin_amdgcn_mfma_f32_16x16x32_bf16(a, b, c, 0, 0, 0)
#define MFMA16K16(a, b, c) __builtin_amdgcn_mfma_f32_16x16x16bf16_1k(a, b, c, 0, 0, 0)
#define LGKM0() __builtin_amdgcn_s_waitcnt(0xC07F)
#define VMW(N) asm volatile("s_waitcnt vmcnt(" #N ")" ::: "memory")
#define BAR() do { LGKM0(); asm volatile("s_barrier" ::: "memory"); } while (0)
#define MFMA_DONE() do { asm volatile("s_nop 7\n\ts_nop 3" ::: "memory"); SBAR(); } while (0)
template <int... Is> using iseq = std::integer_sequence<int, Is...>;
template <int N> using mkseq = std::make_integer_sequence<int, N>;
template <int N> using ic = std::integral_constant<int, N>;

constexpr int NB = 8, SQ = 208, DM = 640, H = 32, SK = 1024, DH = 128, HD = H * DH, NRT = 13;
constexpr unsigned SLOT = 16384, XST = 26624, XOFF = 5 * SLOT, Q2OFF = 6 * SLOT, MISC = Q2OFF + NRT * 4096, LDS_TOTAL = 163840;
static_assert(XOFF + 3 * XST <= LDS_TOTAL && MISC + 3 * 4096 == LDS_TOTAL, "lds map");
constexpr unsigned OBLK = NRT * 4096;
constexpr unsigned PST = 47104, POFF = 26624;
constexpr unsigned PS0 = LDS_TOTAL - PST, PS1 = 0, PS2 = PST, SCR2 = PST;
static_assert(SCR2 + 32768 + 512 <= 5 * SLOT && PS2 + PST <= PS0, "lds map 2");
constexpr size_t WS_CNT = 0, CNT_BYTES = 16384, WS_O = 1u << 20, WS_PART = 32u << 20, WS_STAMP = 64u << 20;
constexpr unsigned PARTB = 8 * 11264;
constexpr float QSCALE = 0.08838834764831845f * 1.4426950408889634f;
constexpr float L_LIMIT = 1.0e24f;
__host__ __device__ constexpr unsigned q1addr(int rt) { return rt < 8 ? 4 * SLOT + 4096 * rt : rt < 11 ? MISC + 4096 * (rt - 8) : SLOT + 4096 * (rt - 11); }

DI unsigned cvtpk(float lo, float hi) { f32x2 v = {lo, hi}; return __builtin_bit_cast(unsigned, __builtin_convertvector(v, bf16x2)); }
DI float fmul_s(float a, float b) { float r; asm("v_mul_f32_e32 %0, %1, %2" : "=v"(r) : "s"(b), "v"(a)); return r; }
DI float ffma_s(float a, float b, float c) { float r; asm("v_fma_f32 %0, %1, %2, %3" : "=v"(r) : "v"(a), "v"(b), "v"(c)); return r; }
DI float ex2(float x) { return __builtin_amdgcn_exp2f(x); }
DI bf16x8 rd128(unsigned base, int imm) { return *(const LAS bf16x8*)((const LAS char*)base + imm); }
DI s16x4 rdtr(unsigned base, int imm) { return __builtin_amdgcn_ds_read_tr16_b64_v4i16((LAS s16x4*)((LAS char*)base + imm)); }
DI bf16x8 cat(s16x4 lo, s16x4 hi) { return __builtin_shufflevector(lo, hi, 0, 1, 2, 3, 4, 5, 6, 7); }
DI void wr128f(unsigned base, int imm, f32x4 v) { *(LAS f32x4*)((LAS char*)base + imm) = v; }
DI f32x4 rd128f(unsigned base, int imm) { return *(const LAS f32x4*)((const LAS char*)base + imm); }
DI float red_add(float x) {
  auto a = __builtin_amdgcn_permlane16_swap(__float_as_uint(x), __float_as_uint(x), false, false); x = __uint_as_float(a[0]) + __uint_as_float(a[1]);
  auto b = __builtin_amdgcn_permlane32_swap(__float_as_uint(x), __float_as_uint(x), false, false); return __uint_as_float(b[0]) + __uint_as_float(b[1]); }
DI float red_max(float x) {
  auto a = __builtin_amdgcn_permlane16_swap(__float_as_uint(x), __float_as_uint(x), false, false); x = fmaxf(__uint_as_float(a[0]), __uint_as_float(a[1]));
  auto b = __builtin_amdgcn_permlane32_swap(__float_as_uint(x), __float_as_uint(x), false, false); return fmaxf(__uint_as_float(b[0]), __uint_as_float(b[1])); }
DI int lane_id() { int l; asm volatile("v_mbcnt_lo_u32_b32 %0, -1, 0\n\tv_mbcnt_hi_u32_b32 %0, -1, %0" : "=v"(l)); return l; }
DI rsrc_t mkrs(const void* p, unsigned bytes) { return __builtin_amdgcn_make_buffer_rsrc((void*)p, 0, bytes, 0x00020000); }

template <int IMM> DI void dma_nt(unsigned m0v, unsigned voff, rsrc_t rs, unsigned soff) {
  asm volatile("s_mov_b32 m0, %0\n\ts_nop 0\n\tbuffer_load_dwordx4 %1, %2, %3 offen offset:%4 nt lds" ::"s"(m0v), "v"(voff), "s"(rs), "s"(soff), "i"(IMM) : "memory");
}
DI void dma_df(unsigned m0v, unsigned voff, rsrc_t rs, unsigned soff) {
  asm volatile("s_mov_b32 m0, %0\n\ts_nop 0\n\tbuffer_load_dwordx4 %1, %2, %3 offen lds" ::"s"(m0v), "v"(voff), "s"(rs), "s"(soff) : "memory");
}
#define CODE_LINES 14
constexpr unsigned CODE_BYTES = CODE_LINES * 4096;
DI void touch_code(unsigned& w, unsigned long long pc, int idx) {
  const rsrc_t rs = __builtin_amdgcn_make_buffer_rsrc((void*)pc, 0, CODE_BYTES, 0x00020000);
  const unsigned l = lane_id(), vo = l < CODE_LINES ? 128 * (l + CODE_LINES * idx) : 0x80000000u;
  asm volatile("s_nop 4\n\tbuffer_load_dword %0, %1, %2, 0 offen" : "=v"(w) : "v"(vo), "s"(rs) : "memory");
}
DI void dma_sc1(unsigned m0v, unsigned voff, rsrc_t rs, unsigned soff) {
  asm volatile("s_mov_b32 m0, %0\n\ts_nop 0\n\tbuffer_load_dwordx4 %1, %2, %3 offen sc1 lds" ::"s"(m0v), "v"(voff), "s"(rs), "s"(soff) : "memory");
}
DI void touch80(unsigned& w0, unsigned& w1, const char* p) {
  const unsigned vo = 128 * lane_id();
  asm volatile("s_nop 4\n\tglobal_load_dword %0, %2, %3\n\tglobal_load_dword %1, %2, %3 offset:2048" : "=&v"(w0), "=&v"(w1) : "v"(vo), "s"(p) : "memory");
}
#define BPRIO 0
#define QLATE 0
#define OLATE 0
#define OAUX 16
#define XDMA dma_df
DI void mfma2(f32x4& c, const bf16x8& a0, const bf16x8& b0, const bf16x8& a1, const bf16x8& b1) {
  asm volatile("v_mfma_f32_16x16x32_bf16 %0, %1, %2, %0\n\tv_mfma_f32_16x16x32_bf16 %0, %3, %4, %0" : "+v"(c) : "v"(a0), "v"(b0), "v"(a1), "v"(b1));
}
template <bool SC1> DI void mfma2d(f32x4& c, const bf16x8& a0, const bf16x8& b0, const bf16x8& a1, const bf16x8& b1, unsigned m0v, unsigned voff, rsrc_t rs, unsigned soff) {
  if constexpr (SC1)
    asm volatile("v_mfma_f32_16x16x32_bf16 %0, %1, %2, %0\n\ts_mov_b32 m0, %5\n\tv_mfma_f32_16x16x32_bf16 %0, %3, %4, %0\n\tbuffer_load_dwordx4 %6, %7, %8 offen sc1 lds"
                 : "+v"(c) : "v"(a0), "v"(b0), "v"(a1), "v"(b1), "s"(m0v), "v"(voff), "s"(rs), "s"(soff) : "memory");
  else
    asm volatile("v_mfma_f32_16x16x32_bf16 %0, %1, %2, %0\n\ts_mov_b32 m0, %5\n\tv_mfma_f32_16x16x32_bf16 %0, %3, %4, %0\n\tbuffer_load_dwordx4 %6, %7, %8 offen lds"
                 : "+v"(c) : "v"(a0), "v"(b0), "v"(a1), "v"(b1), "s"(m0v), "v"(voff), "s"(rs), "s"(soff) : "memory");
}
DI void mfma1(f32x4& c, const bf16x8& a, const bf16x8& b) { asm volatile("v_mfma_f32_16x16x32_bf16 %0, %1, %2, %0" : "+v"(c) : "v"(a), "v"(b)); }
DI void poll_issue(unsigned& v, const unsigned* cnt) { const unsigned z = 0; asm volatile("s_nop 4\n\tglobal_load_dword %0, %1, %2 sc1" : "=v"(v) : "v"(z), "s"(cnt) : "memory"); }
DI void poll_acquire(unsigned v, const unsigned* cnt) {
  asm volatile("" : "+v"(v)::"memory");
  if (__builtin_amdgcn_readfirstlane(v) < 4u) {
#pragma nounroll
    for (int spin = 0; spin < (1 << 22); ++spin) {
      const unsigned u = __hip_atomic_load((gu32*)cnt, __ATOMIC_RELAXED, __HIP_MEMORY_SCOPE_AGENT);
      if (__builtin_amdgcn_readfirstlane(u) >= 4u) break;
      __builtin_amdgcn_s_sleep(2);
    }
  }
}
DI void signal_quad(unsigned* cnt) { if (lane_id() == 0) __hip_atomic_fetch_add((gu32*)cnt, 1u, __ATOMIC_RELAXED, __HIP_MEMORY_SCOPE_AGENT); }

#define STAMP(i)
#define TS(x)
#define FST(i)
#define PST_(i)
struct Args { const u16 *x0, *k0, *v0, *wq0, *wo0, *x1, *k1, *v1, *wq1, *wo1; char* ws; u16* out; };
struct Ctx {
  const u16 *x, *k, *v, *wq; const char *wo, *warm; char *oq, *part; u16* out; unsigned *cnt, *cnt2; unsigned long long* stp; unsigned ldsb; int own, nt, kq, idx; unsigned long long pc;
};

DI void slow16(const u16* Kg, const u16* Vg, int key0, int nstep, const bf16x8 (&q)[4], f32x4 (&o)[8], float& m, float& l) {
  const int lane = lane_id(), r = lane & 15, g = lane >> 4;
  m = -3.0e38f; l = 0.f;
#pragma unroll
  for (int d = 0; d < 8; ++d) o[d] = f32x4{};
#pragma nounroll
  for (int st = 0; st < nstep; ++st) {
    const int kb = nstep == 64 ? 16 * st : key0 + 128 * (st >> 1) + 16 * (st & 1);
    f32x4 S = {};
#pragma unroll
    for (int ks = 0; ks < 4; ++ks) S = MFMA16(*(const bf16x8*)(Kg + (size_t)(kb + r) * DH + 32 * ks + 8 * g), q[ks], S);
    const float mx = red_max(fmaxf(fmaxf(S[0], S[1]), fmaxf(S[2], S[3])));
    const float mn = fmaxf(m, mx), al = ex2(m - mn); m = mn;
    float ps = 0.f;
#pragma unroll
    for (int i = 0; i < 4; ++i) { S[i] = ex2(S[i] - mn); ps += S[i]; }
    l = l * al + ps;
    u32x2 pw = {cvtpk(S[0], S[1]), cvtpk(S[2], S[3])};
#pragma unroll
    for (int d = 0; d < 8; ++d) {
      s16x4 vf;
#pragma unroll
      for (int j = 0; j < 4; ++j) vf[j] = (short)Vg[(size_t)(kb + 4 * g + j) * DH + 16 * d + r];
      o[d] *= al;
      o[d] = MFMA16K16(vf, __builtin_bit_cast(s16x4, pw), o[d]);
    }
  }
  __builtin_amdgcn_s_waitcnt(0);
}
DI float nanprobe_s(const f32x4 (&o)[8]) {
  float t = 0.f; const float z = 0.f;
#pragma unroll
  for (int d = 0; d < 8; ++d) { t = ffma_s(o[d][0], z, t); t = ffma_s(o[d][1], z, t); t = ffma_s(o[d][2], z, t); t = ffma_s(o[d][3], z, t); }
  return t;
}
DI float nanprobe(const f32x4 (&o)[8]) {
  f32x4 t = {};
#pragma unroll
  for (int d = 0; d < 8; ++d) t += o[d] * 0.f;
  return (t[0] + t[1]) + (t[2] + t[3]);
}

template <int NR, class FD, class FW> DI void qproj(const Ctx& c, int cg, FD&& dma, FW&& wait) {
  f32x4 acc[NR][4]; bf16x8 w[4][2], x[NR][2];
  const unsigned lane = lane_id(), r16 = lane & 15, g = lane >> 4, q4 = (lane & 15) >> 2, p = lane & 3;
  const unsigned wb0 = c.ldsb + 1024 * (cg & 1) + 2048 * g + 64 * q4 + 16 * ((p >> 1) ^ (2 * (g & 1))) + 8 * (p & 1);
  const unsigned xb0 = c.ldsb + XOFF + (NR == 7 ? 0 : 7 * 2048) + 128 * r16 + 16 * (g ^ ((r16 >> 1) & 7));
#pragma unroll
  for (int r = 0; r < NR; ++r)
#pragma unroll
    for (int i = 0; i < 4; ++i) acc[r][i] = f32x4{};
  unsigned xs = 0, ws = SLOT * (cg >> 1), xp0, xp1, wp0, wp1;
  auto ldw = [&](int i) { const unsigned b = (i & 1) ? wp1 : wp0; const int im = 512 * (i >> 1);
    w[i][0] = cat(rdtr(b, im), rdtr(b, im + 256)); w[i][1] = cat(rdtr(b, im + 8192), rdtr(b, im + 8448)); };
  auto ldx = [&](int r) { x[r][0] = rd128(xp0, 2048 * r); x[r][1] = rd128(xp1, 2048 * r); };
  auto row = [&](int r) {
#pragma unroll
    for (int i = 0; i < 4; ++i) mfma2(acc[r][i], w[i][0], x[r][0], w[i][1], x[r][1]);
  };
  constexpr int LATE_R = QLATE;
  auto half = [&](int r, int h) { mfma2(acc[r][2 * h], w[2 * h][0], x[r][0], w[2 * h][1], x[r][1]); mfma2(acc[r][2 * h + 1], w[2 * h + 1][0], x[r][0], w[2 * h + 1][1], x[r][1]); };
  auto step = [&](auto K, auto F, int s) {
    constexpr bool FIRST = decltype(F)::value;
    xp0 = xb0 + xs; xp1 = xp0 ^ 64; wp0 = wb0 + ws; wp1 = wp0 ^ 32;
    xs = xs == 2 * XST ? 0 : xs + XST; ws += 2 * SLOT; ws = ws >= 5 * SLOT ? ws - 5 * SLOT : ws;
    wait(s);
    SBAR();
    BAR();
    if constexpr (FIRST) { STAMP(1); }
    SBAR();
    if constexpr (NR == 7) {
#pragma unroll
      for (int r = 0; r < 7; ++r) {
        if constexpr (!FIRST) half(r, 0);
        SBAR(); if (r) x[r - 1][0] = rd128(xp0, 2048 * (r - 1)); SBAR();
        if constexpr (!FIRST) half(r, 1);
        SBAR(); if (r) x[r - 1][1] = rd128(xp1, 2048 * (r - 1)); SBAR();
      }
      ldx(6);
#pragma unroll
      for (int i = 0; i < 4; ++i) { ldw(i); dma(K, s, 2 * i); if (i < 3) dma(K, s, 2 * i + 1); SBAR(); }
    } else {
      if constexpr (!FIRST) {
#pragma unroll
        for (int r = 6 - LATE_R; r < 6; ++r) row(r);
        __builtin_amdgcn_s_setprio(0);
        SBAR();
      }
#pragma unroll
      for (int i = 0; i < 4; ++i) { dma(K, s, i); ldw(i); SBAR(); }
#pragma unroll
      for (int r = 0; r < 6; ++r) { if (r < 4) dma(K, s, 4 + r); ldx(r); SBAR(); }
#pragma unroll
      for (int r = 0; r < 6 - LATE_R; ++r) row(r);
      __builtin_amdgcn_s_setprio(1);
      SBAR();
    }
  };
  asm volatile("s_nop 1" ::: "memory");
  step(ic<NR == 7 ? 0 : 3>{}, ic<1>{}, 0);
#pragma nounroll
  for (int s = 1; s < 8; ++s) step(ic<0>{}, ic<0>{}, s);
  step(ic<1>{}, ic<0>{}, 8);
  step(ic<2>{}, ic<0>{}, 9);
  BAR();
  if constexpr (NR == 6) {
#pragma unroll
    for (int r = 6 - LATE_R; r < 6; ++r) row(r);
    __builtin_amdgcn_s_setprio(0);
    SBAR();
  }
  dma(ic<4>{}, 10, 0);
  const unsigned ln = lane_id(), rr = ln & 15, gg = ln >> 4;
  const unsigned qw = c.ldsb + 2048 * (cg & 1) + 128 * rr + 16 * ((gg >> 1) ^ ((rr >> 1) & 7)) + 8 * (gg & 1);
  auto put = [&](int r) {
    const int rt = (NR == 7 ? 0 : 7) + r;
    const unsigned blk = (cg >> 1) ? Q2OFF + 4096 * rt : q1addr(rt);
#pragma unroll
    for (int i = 0; i < 4; ++i) {
      const f32x4 v = acc[r][i];
      *(LAS u32x2*)((qw ^ (32 * i)) + blk) = u32x2{cvtpk(fmul_s(v[0], QSCALE), fmul_s(v[1], QSCALE)), cvtpk(fmul_s(v[2], QSCALE), fmul_s(v[3], QSCALE))};
    }
  };
  SBAR();
  if constexpr (NR == 7) {
#pragma unroll
    for (int r = 0; r < 7; ++r) { row(r); SBAR(); if (r) put(r - 1); SBAR(); }
    MFMA_DONE();
    put(6);
  } else {
    MFMA_DONE();
#pragma unroll
    for (int r = 0; r < 6; ++r) put(r);
  }
}

struct OD { rsrc_t rw, ro; unsigned vwp, vwo, vo, mw, mo, lo; int rg; };
DI OD od_make(const Ctx& c, int rg) {
  OD d; const unsigned lane = lane_id(), xr = 16 * ((lane & 3) ^ (((lane >> 4) & 1) << 1));
  d.rw = mkrs(c.wo, 1024 * DM * 2); d.ro = mkrs(c.oq, 8 * OBLK);
  d.vwp = 1280 * (16 * rg + ((lane >> 2) & 7)) + 64 * (lane >> 5) + xr + 320 * c.nt + ((c.nt & 1) ? 64 : 0);
  d.vwo = 1280 * (16 * rg + (lane >> 2)) + xr + 320 * c.nt + ((c.nt & 1) ? 0 : 256);
  d.vo = 16 * lane; d.mw = c.ldsb + POFF + 5120 * rg; d.mo = c.ldsb + 1024 * rg; d.lo = 1024 * (24 + (rg & 1)); d.rg = rg;
  return d;
}
DI unsigned od_blk(const Ctx& c, int s) { return (unsigned)(s < 16 ? 4 * ((s >> 1) & 3) + 2 * (s >> 3) + (s & 1) : 64); }
template <class F> DI void od_piece(const Ctx& c, const OD& d, unsigned u, unsigned st, int n, F&& f) {
  const unsigned sw = u * (64 * 1280), so = u * (OBLK / 2), m = d.mw + st;
  if (n == 0) f(ic<0>{}, m, d.vwp, d.rw, sw);
  else if (n == 1) f(ic<0>{}, m + 1024, d.vwp, d.rw, sw + 128);
  else if (n == 2) f(ic<0>{}, m + 3072, d.vwp, d.rw, sw + 10240);
  else if (n == 3) f(ic<0>{}, m + 4096, d.vwp, d.rw, sw + 10240 + 128);
  else if (n == 4) f(ic<0>{}, m + 2048, d.vwo, d.rw, sw);
  else if (n < 11) f(ic<1>{}, d.mo + st + 4096 * (n - 5), d.vo, d.ro, so + 1024 * d.rg + 4096 * (n - 5));
  else f(ic<1>{}, c.ldsb + d.lo + st, d.vo, d.ro, so + d.lo);
}
DI void od_piece(const Ctx& c, const OD& d, unsigned u, unsigned st, int n) {
  od_piece(c, d, u, st, n, [](auto S, unsigned m, unsigned v, rsrc_t r, unsigned so) { if constexpr (decltype(S)::value) dma_sc1(m, v, r, so); else dma_df(m, v, r, so); });
}
template <int NC, int NR> DI void oproj(const Ctx& c, int rg) {
  const OD d = od_make(c, rg);
  f32x4 acc[NR][NC]; bf16x8 w[NC][2], x[NR][2];
  const int c0 = NC == 3 ? 3 * (rg >> 1) : 6 + 2 * (rg >> 1), r0 = NR == 7 ? 0 : 7;
  unsigned wb[NC], xb;
  {
    const unsigned lane = lane_id(), g = lane >> 4, q4 = (lane & 15) >> 2, p = lane & 3;
    const unsigned in = 64 * (4 * (g & 1) + q4) + 16 * ((p >> 1) ^ (2 * (g & 1))) + 8 * (p & 1);
#pragma unroll
    for (int i = 0; i < NC; ++i) {
      const int ct = c0 + i;
      const unsigned off = ct < 8 ? 512 * (ct >> 1) + 3072 * (g >> 1) : 2048 + 512 * (g >> 1);
      wb[i] = c.ldsb + POFF + off + (in ^ (32 * (ct & 1)));
    }
    xb = c.ldsb + 16 * lane + 2048 * r0;
  }
#pragma unroll
  for (int r = 0; r < NR; ++r)
#pragma unroll
    for (int i = 0; i < NC; ++i) acc[r][i] = f32x4{};
  unsigned st = PS0, nx = PS1, fr = PS2;
  auto ldw = [&](int i) { const unsigned wp = wb[i] + st; w[i][0] = cat(rdtr(wp, 0), rdtr(wp, 5120)); w[i][1] = cat(rdtr(wp, 10240), rdtr(wp, 15360)); };
  auto ldx = [&](int r) { const unsigned xp = xb + st; x[r][0] = rd128(xp, 2048 * r); x[r][1] = rd128(xp, 2048 * r + 1024); };
  auto row = [&](int r) {
#pragma unroll
    for (int i = 0; i < NC; ++i) mfma2(acc[r][i], w[i][0], x[r][0], w[i][1], x[r][1]);
  };
#pragma unroll
  for (int r = 0; r < NR; ++r) { x[r][0] = bf16x8{}; x[r][1] = bf16x8{}; }
#pragma unroll
  for (int i = 0; i < NC; ++i) { w[i][0] = bf16x8{}; w[i][1] = bf16x8{}; }
  unsigned pv = 0;
  unsigned long long f0 = 0, f1 = 0, f2 = 0, f3 = 0, tp = 0, t2 = 0; (void)f0; (void)f1; (void)f2; (void)f3; (void)tp; (void)t2;
  const bool me = NC == 3 && rg == 1;
  STAMP(8);
  auto tile_d = [&](int r, int i, unsigned u, int n) {
    od_piece(c, d, u, fr, n, [&](auto S, unsigned m, unsigned v, rsrc_t rs, unsigned so) { mfma2d<decltype(S)::value != 0>(acc[r][i], w[i][0], x[r][0], w[i][1], x[r][1], m, v, rs, so); });
  };
  if constexpr (NC == 2) VMW(13);
#pragma nounroll
  for (int s = 0; s < 16; ++s) {
    const unsigned u = od_blk(c, s + 2), xp = xb + st;
    SBAR();
    BAR();
    if (NC == 3 && NR == 7 && rg == 2) PST_(s);
    SBAR();
    if constexpr (NC == 3) {
      if constexpr (NR == 6) { if (me && s == 5) poll_issue(pv, c.cnt + 32); PIN(pv); }
#pragma unroll
      for (int r = 0; r < NR; ++r) {
        if (NR == 6 && r == 0) tile_d(0, 0, u, 11); else mfma2(acc[r][0], w[0][0], x[r][0], w[0][1], x[r][1]);
        SBAR(); if (r) x[r - 1][0] = rd128(xp, 2048 * (r - 1)); SBAR();
        mfma2(acc[r][1], w[1][0], x[r][0], w[1][1], x[r][1]);
        SBAR(); if (r) x[r - 1][1] = rd128(xp, 2048 * (r - 1) + 1024); SBAR();
        tile_d(r, 2, u, r < 5 ? r : r + 5);
        SBAR();
      }
      ldx(NR - 1);
#pragma unroll
      for (int i = 0; i < 3; ++i) ldw(i);
      SBAR();
      VMW(7);
      if constexpr (NR == 6) { if (me && s == 5) poll_acquire(pv, c.cnt + 32); }
    } else {
#pragma unroll
      for (int r = NR - OLATE; r < NR; ++r) row(r);
      __builtin_amdgcn_s_setprio(0);
      SBAR();
      ldw(0); ldx(0); ldw(1);
#pragma unroll
      for (int r = 1; r < NR; ++r) ldx(r);
      SBAR();
      if (NR == 6 && rg == 0 && s == 1) signal_quad(c.cnt + 32);
#pragma unroll
      for (int r = 0; r < NR - OLATE; ++r) {
        if (r < 5) tile_d(r, 0, u, 5 + r); else mfma2(acc[r][0], w[0][0], x[r][0], w[0][1], x[r][1]);
        if (NR - OLATE < 5 && r == 0) tile_d(r, 1, u, 9); else mfma2(acc[r][1], w[1][0], x[r][0], w[1][1], x[r][1]);
        SBAR();
      }
      VMW(5);
      __builtin_amdgcn_s_setprio(1);
    }
    const unsigned t = st; st = nx; nx = fr; fr = t;
  }
  STAMP(9);
  LGKM0(); SBAR();
#pragma unroll
  for (int r = NC == 3 ? 0 : NR - OLATE; r < NR; ++r) row(r);
  __builtin_amdgcn_s_setprio(0);
  MFMA_DONE();
  STAMP(10);
  constexpr int NT = NR * NC, NU = (NT + 1) / 2;
  const int wv = NC == 3 ? rg : 4 + rg;
  const unsigned ln = lane_id(), pv0 = 16 * ln + 11264 * wv;
  const rsrc_t rp = mkrs(c.part, 16 * PARTB);
  f32x4 own[3][2];
  auto scatter = [&](auto K) {
    constexpr int k = decltype(K)::value;
#pragma unroll
    for (int m = 0; m < NU; ++m) {
      const f32x4 u = acc[(2 * m) / NC][(2 * m) % NC], v = 2 * m + 1 < NT ? acc[(2 * m + 1) / NC][(2 * m + 1) % NC] : f32x4{};
      if ((m & 3) == k) { own[m >> 2][0] = u; own[m >> 2][1] = v; }
      else __builtin_amdgcn_raw_buffer_store_b128(u32x4{cvtpk(u[0], u[1]), cvtpk(u[2], u[3]), cvtpk(v[0], v[1]), cvtpk(v[2], v[3])}, rp, pv0 + (4 * k + c.nt) * PARTB + 1024 * m, 0, 16);
    }
#pragma unroll
    for (int e = 0; e < 3; ++e) if (4 * e + k >= NU) { own[e][0] = f32x4{}; own[e][1] = f32x4{}; }
  };
  if (c.kq == 0) scatter(ic<0>{}); else if (c.kq == 1) scatter(ic<1>{}); else if (c.kq == 2) scatter(ic<2>{}); else scatter(ic<3>{});
  VMW(0);
  BAR();
  if (wv == 0) {
    signal_quad(c.cnt2);
#pragma nounroll
    for (int spin = 0; spin < (1 << 22); ++spin) {
      const unsigned u = __hip_atomic_load((gu32*)c.cnt2, __ATOMIC_RELAXED, __HIP_MEMORY_SCOPE_AGENT);
      if (__builtin_amdgcn_readfirstlane(u) >= 4u) break;
      __builtin_amdgcn_s_sleep(1);
    }
  }
  BAR();
  u32x4 pp[3][3];
#pragma unroll
  for (int e = 0; e < 3; ++e)
#pragma unroll
    for (int q = 1; q < 4; ++q) {
      const int m = 4 * e + c.kq;
      pp[e][q - 1] = __builtin_amdgcn_raw_buffer_load_b128(rp, m < NU ? pv0 + (4 * ((c.kq + q) & 3) + c.nt) * PARTB + 1024 * m : 0x80000000u, 0, 16);
    }
#pragma unroll
  for (int e = 0; e < 3; ++e)
#pragma unroll
    for (int h = 0; h < 2; ++h) {
      const int n = 2 * (4 * e + c.kq) + h;
      f32x4 v = own[e][h];
#pragma unroll
      for (int q = 0; q < 3; ++q) {
        v[0] += __uint_as_float(pp[e][q][2 * h] << 16); v[1] += __uint_as_float(pp[e][q][2 * h] & 0xffff0000u);
        v[2] += __uint_as_float(pp[e][q][2 * h + 1] << 16); v[3] += __uint_as_float(pp[e][q][2 * h + 1] & 0xffff0000u);
      }
      if (n < NT) {
        const int rt = r0 + n / NC, ct = c0 + n % NC;
        const int col = 160 * c.nt + (ct < 8 ? ((c.nt & 1) ? 32 : 0) + 16 * ct : ((c.nt & 1) ? 0 : 128) + 16 * (ct - 8));
        *(u32x2*)(c.out + (size_t)(16 * rt + (ln & 15)) * (2 * DM) + col + 4 * (ln >> 4)) = u32x2{cvtpk(v[0], v[1]), cvtpk(v[2], v[3])};
      }
    }
  STAMP(11);
}

struct AS {
  f32x4 o[3][8], S[2][3], negm, l[3]; bf16x8 q[3][4], F[2][4], ones; u32x4 P[2][3];
  unsigned kp[2], vp[2], kn[2];
};
template <int N, int I> DI void a_frag(AS& a) {
  constexpr int sub = (N >> 2) & 1, b = N & 3;
  if constexpr ((b & 1) == 0) {
    constexpr int kt = 2 * sub + (b >> 1);
    a.F[N & 1][I] = rd128(N == 8 ? a.kn[I & 1] : a.kp[I & 1], 8192 * (I >> 1) + 2048 * kt);
  } else {
    constexpr int cc = 4 * (b >> 1) + I, imm = 8192 * sub + 512 * (cc >> 1);
    a.F[N & 1][I] = cat(rdtr(a.vp[cc & 1], imm), rdtr(a.vp[cc & 1], imm + 4096));
  }
}
#define LGKM(n) __builtin_amdgcn_s_waitcnt(0xC07F | ((n) << 8))
template <int BLK, int K> DI void sm_exp(AS& a) { constexpr int b = BLK & 3, sb = (b == 1 || b == 2) ? 0 : 1, e = ((b & 1) ? 0 : 6) + K;
  a.S[sb][e >> 2][e & 3] = ex2(a.S[sb][e >> 2][e & 3]); PIN(a.S[sb][e >> 2]); }
template <int BLK, int K> DI void sm_cvt(AS& a) { constexpr int b = BLK & 3, sub = (BLK >> 2) & 1, sb = (b == 1 || b == 2) ? 0 : 1, e = ((b & 1) ? 0 : 6) + 2 * K, pb = b == 0 ? sub ^ 1 : sub, rt = e >> 2;
  a.P[pb][rt][2 * sb + ((e & 3) >> 1)] = cvtpk(a.S[sb][rt][e & 3], a.S[sb][rt][(e & 3) + 1]); PIN(a.P[pb][rt]); }
template <int N, bool MM, bool SM, bool CY, int NX, bool Z, int G> DI void a_gap(AS& a) {
  constexpr int sub = N >> 2, b = N & 3; constexpr bool QK = (b & 1) == 0;
  if constexpr (MM) {
    if constexpr (QK) {
      constexpr int rt = G >> 2, ks = G & 3, sb = b >> 1;
      if constexpr (ks == 0) { if constexpr (Z) a.S[sb][rt] = MFMA16(a.F[N & 1][0], a.q[rt][0], f32x4{}); else a.S[sb][rt] = MFMA16(a.F[N & 1][0], a.q[rt][0], a.negm); }
      else a.S[sb][rt] = MFMA16(a.F[N & 1][ks], a.q[rt][ks], a.S[sb][rt]);
    } else if constexpr (G < 12) {
      constexpr int i = G / 3, rt = G % 3, cc = 4 * (b >> 1) + i;
      if constexpr (Z) a.o[rt][cc] = MFMA16(a.F[N & 1][i], __builtin_bit_cast(bf16x8, a.P[sub ^ 1][rt]), f32x4{});
      else a.o[rt][cc] = MFMA16(a.F[N & 1][i], __builtin_bit_cast(bf16x8, a.P[sub ^ 1][rt]), a.o[rt][cc]);
    } else {
      constexpr int rt = b == 1 ? G - 12 : 2;
      if constexpr (Z) a.l[rt] = MFMA16(a.ones, __builtin_bit_cast(bf16x8, a.P[sub ^ 1][rt]), f32x4{});
      else a.l[rt] = MFMA16(a.ones, __builtin_bit_cast(bf16x8, a.P[sub ^ 1][rt]), a.l[rt]);
    }
    SBAR();
  }
  constexpr int LAST = QK ? 11 : b == 1 ? 13 : 12;
  if constexpr (G == LAST) { if constexpr (QK) LGKM(4); else LGKM(0); }
  if constexpr (G == 5 && !QK) { if constexpr (NX >= 0) LGKM(3); else LGKM(0); }
  SBAR();
  if constexpr (NX >= 0 && (G & 1) == 0 && G < 8) a_frag<NX, G / 2>(a);
  SBAR();
  if constexpr ((G & 1) && G < 12) { if constexpr (SM) sm_exp<N, G / 2>(a); }
  else if constexpr (QK) {
    if constexpr (SM && G >= 8) sm_cvt<N, G / 2 - 4>(a);
  } else {
    if constexpr (G == 0 && CY) sm_cvt<N - 1, 2>(a);
    if constexpr (SM && G >= 8 && G <= 12 && (G & 1) == 0) sm_cvt<N, G / 2 - 4>(a);
  }
  SBAR();
}
template <int N, bool MM, bool SM, bool CY, int NX, bool Z, int... Gs> DI void a_block_(AS& a, iseq<Gs...>) { (a_gap<N, MM, SM, CY, NX, Z, Gs>(a), ...); }
template <int N, bool MM, bool SM, bool CY, int NX, bool Z = false> DI void a_block(AS& a) { a_block_<N, MM, SM, CY, NX, Z>(a, mkseq<(N & 1) == 0 ? 12 : (N & 3) == 1 ? 14 : 13>{}); }
template <bool FIRST> DI void a_period(AS& a) {
  if constexpr (FIRST) {
    a_block<0, true, false, false, -1, true>(a);
    float mx = 3.0e38f;
#pragma unroll
    for (int rt = 0; rt < 3; ++rt) mx = fminf(mx, red_max(fmaxf(fmaxf(a.S[0][rt][0], a.S[0][rt][1]), fmaxf(a.S[0][rt][2], a.S[0][rt][3]))));
    a.negm = f32x4{-mx, -mx, -mx, -mx}; PIN(a.negm);
#pragma unroll
    for (int rt = 0; rt < 3; ++rt) a.S[0][rt] -= mx;
    SBAR();
    a_block<1, false, true, false, 2>(a); a_block<2, true, true, true, -1>(a); a_block<3, false, true, true, 4>(a);
  } else {
    a_block<0, true, true, true, 1>(a); a_block<1, true, true, true, 2>(a); a_block<2, true, true, true, 3>(a); a_block<3, true, true, true, 4>(a);
  }
  a_block<4, true, true, true, 5>(a); a_block<5, true, true, true, 6, FIRST>(a); a_block<6, true, true, true, 7>(a);
  if constexpr (FIRST) VMW(0);
  BAR();
  a_block<7, true, true, true, 8, FIRST>(a);
}

DI void path_a(const Ctx& c, int wi) {
  STAMP(0);
  {
    const rsrc_t rx = mkrs(c.x, SQ * DM * 2);
    const unsigned lane = lane_id(), vox = 1280u * (lane >> 3) + 16u * ((lane & 7) ^ ((4 * (wi & 1) + (lane >> 4)) & 7));
    auto xp = [&](int s, unsigned stage, int n) { const int pc = n < 6 ? wi + 4 * n : 24 + (wi & 1); XDMA(c.ldsb + XOFF + stage + 1024 * pc, vox, rx, 128u * s + 10240u * pc); };
#pragma unroll
    for (int n = 0; n < 7; ++n) xp(0, 0, n);
#pragma unroll
    for (int n = 0; n < 7; ++n) xp(1, XST, n);
    unsigned xf = XST;
    qproj<7>(c, wi,
      [&](auto K, int s, int n) { if constexpr (decltype(K)::value == 0) xp(s + 2, xf, n); },
      [&](int s) { if (s == 9) VMW(0); else VMW(7); xf = xf == 2 * XST ? 0 : xf + XST; });
  }
  FST(11);
  BAR();
  FST(12);
  AS a;
  unsigned kb0, vb0;
  { const unsigned lane = lane_id(), r16 = lane & 15, g = lane >> 4, q4 = (lane & 15) >> 2, p = lane & 3;
    kb0 = c.ldsb + 128 * r16 + 16 * (g ^ ((r16 >> 1) & 7));
    vb0 = c.ldsb + SLOT + 2048 * (g >> 1) + 64 * (4 * (g & 1) + q4) + 16 * ((p >> 1) ^ (2 * (g & 1))) + 8 * (p & 1); }
  unsigned sl = 0;
  a.ones = bf16x8{0x3f80, 0x3f80, 0x3f80, 0x3f80, 0x3f80, 0x3f80, 0x3f80, 0x3f80}; PIN(a.ones);
#pragma nounroll
  for (int hd = 0; hd < 2; ++hd) {
#pragma unroll
    for (int rt = 0; rt < 3; ++rt) {
      const int t = 3 * wi + rt;
      const unsigned blk = hd ? Q2OFF + 4096 * t : (t < 8 ? 4 * SLOT + 4096 * t : t < 11 ? MISC + 4096 * (t - 8) : SLOT + 4096 * (t - 11));
      const unsigned q0 = kb0 + blk, q1 = q0 ^ 64;
      a.q[rt][0] = rd128(q0, 0); a.q[rt][1] = rd128(q1, 0); a.q[rt][2] = rd128(q0, 2048); a.q[rt][3] = rd128(q1, 2048);
      a.P[0][rt] = u32x4{}; a.P[1][rt] = u32x4{};
    }
    a.kn[0] = kb0 + sl; a.kn[1] = a.kn[0] ^ 64;
    a_frag<8, 0>(a); a_frag<8, 1>(a); a_frag<8, 2>(a); a_frag<8, 3>(a);
    if (hd == 0) BAR();
    STAMP(2 + 3 * hd);
    auto setp = [&]() {
      a.kp[0] = a.kn[0]; a.kp[1] = a.kn[1]; a.vp[0] = vb0 + sl; a.vp[1] = a.vp[0] ^ 32;
      sl = sl == 4 * SLOT ? 0 : sl + 2 * SLOT;
      a.kn[0] = kb0 + sl; a.kn[1] = a.kn[0] ^ 64;
      SBAR();
    };
    setp();
    a_period<true>(a);
#pragma nounroll
    for (int t = 1; t < 16; ++t) {
 if (wi == 0) PST_(16 * hd + t);
 setp(); a_period<false>(a); }
    a.vp[0] = vb0 + sl; a.vp[1] = a.vp[0] ^ 32;
    SBAR();
    a_block<0, false, true, true, 1>(a); a_block<1, true, false, true, -1>(a);
    a_frag<3, 0>(a); a_frag<3, 1>(a); a_frag<3, 2>(a); a_frag<3, 3>(a);
    a_block<3, true, false, false, -1>(a);
    if (hd) BAR();
    STAMP(3 + 3 * hd);
    const rsrc_t ro = mkrs(c.oq + (size_t)(2 * c.own + hd) * OBLK, OBLK);
    const unsigned ov = 16 * lane_id() + 6144 * wi;
#pragma unroll
    for (int rt = 0; rt < 3; ++rt) {
      float l = a.l[rt][0];
      if (__builtin_expect(__any(!(l + nanprobe(a.o[rt]) < L_LIMIT)), 0)) {
        float m;
        slow16(c.k + (size_t)hd * SK * DH, c.v + (size_t)hd * SK * DH, 0, 64, a.q[rt], a.o[rt], m, l);
        l = red_add(l);
      }
      const float il = __builtin_amdgcn_rcpf(l);
#pragma unroll
      for (int kk = 0; kk < 4; ++kk) {
        const f32x4 u = a.o[rt][2 * kk] * il, v = a.o[rt][2 * kk + 1] * il;
        u32x4 w = {cvtpk(u[0], u[1]), cvtpk(u[2], u[3]), cvtpk(v[0], v[1]), cvtpk(v[2], v[3])};
        __builtin_amdgcn_raw_buffer_store_b128(w, ro, ov + 2048 * rt + 1024 * (kk & 1) + (OBLK / 2) * (kk >> 1), 0, OAUX);
      }
    }
    STAMP(4 + 3 * hd);
  }
}

DI void path_b(const Ctx& c, int j) {
  STAMP(0);
  const rsrc_t rk = mkrs(c.k, 2 * SK * DH * 2), rv = mkrs(c.v, 2 * SK * DH * 2);
  unsigned vk0, vk1, vv, wm0 = 0, wm1 = 0;
  { const unsigned i = lane_id();
    vk0 = 256 * (i >> 3) + 16 * ((i & 7) ^ (i >> 4)); vk1 = (vk0 ^ 64) + 2048;
    vv = 256 * ((i >> 2) & 7) + 64 * (i >> 5) + 16 * ((i & 3) ^ (((i >> 4) & 1) << 1)); }
  auto kpc = [&](int t, unsigned slot, int n) {
    const unsigned m = c.ldsb + slot + 2048 * j, so = (unsigned)(64 * t + 16 * j) * 256u;
    if (n == 0) dma_nt<0>(m, vk0, rk, so); if (n == 1) dma_nt<128>(m + 8064, vk0, rk, so); if (n == 2) dma_nt<0>(m + 1024, vk1, rk, so); if (n == 3) dma_nt<128>(m + 9088, vk1, rk, so);
  };
  auto vpc = [&](int t, unsigned slot, int n) {
    const unsigned m = c.ldsb + slot + 4096 * j, so = (unsigned)(64 * t - 32 + 16 * j) * 256u;
    if (n == 0) dma_nt<0>(m, vv, rv, so); if (n == 1) dma_nt<128>(m + 896, vv, rv, so); if (n == 2) dma_nt<2048>(m, vv, rv, so); if (n == 3) dma_nt<2176>(m + 896, vv, rv, so);
  };
  auto kdma = [&](int t, unsigned slot) { kpc(t, slot, 0); kpc(t, slot, 1); kpc(t, slot, 2); kpc(t, slot, 3); };
  auto vdma = [&](int t, unsigned slot) { vpc(t, slot, 0); vpc(t, slot, 1); vpc(t, slot, 2); vpc(t, slot, 3); };
  {
    const rsrc_t rw = mkrs(c.wq, (DM - 1) * HD * 2 + 512);
    const unsigned i = lane_id(), vw0 = 8192 * ((i >> 2) & 7) + 64 * (i >> 5) + 16 * (i & 3), vw1 = (vw0 ^ 32) + 65536;
    auto wpc = [&](int it, unsigned slot, int n) {
      dma_df(c.ldsb + slot + 4096 * j + 1024 * n, n < 2 ? vw0 : vw1, rw, (unsigned)(64 * (it >> 1) + 16 * j) * 8192u + 256 * (it & 1) + 128 * (n & 1));
    };
    if (j == 3) touch_code(wm0, c.pc, c.idx);
#pragma nounroll
    for (int it = 0; it < 5; ++it) { wpc(it, it * SLOT, 0); wpc(it, it * SLOT, 1); wpc(it, it * SLOT, 2); wpc(it, it * SLOT, 3); }
    unsigned f0 = SLOT, f1 = 2 * SLOT;
    auto adv = [](unsigned s_) { s_ += 2 * SLOT; return s_ >= 5 * SLOT ? s_ - 5 * SLOT : s_; };
    qproj<6>(c, j,
      [&](auto K, int s, int n) {
        constexpr int k = decltype(K)::value;
        if constexpr (k == 0) { if (n < 4) wpc(2 * s + 3, f0, n); else wpc(2 * s + 4, f1, n - 4); }
        if constexpr (k == 1) { if (n < 4) wpc(19, 4 * SLOT, n); else kpc(0, 0, n - 4);
          if (n == 7) { PIN(wm0); touch80(wm0, wm1, c.warm + 10240 * j); } }
        if constexpr (k == 2) { if (n == 0) { if (j >= 2) vdma(0, SLOT); } else if (n >= 4) kpc(1, 2 * SLOT, n - 4); }
        if constexpr (k == 4) vdma(1, 3 * SLOT);
      },
      [&](int s) { if (s == 0) VMW(12); else if (s == 9) VMW(6); else VMW(4); f0 = adv(f0); f1 = adv(f1); });
  }
  VMW(8);
  asm volatile("" ::"v"(wm0), "v"(wm1));
  BAR();
  unsigned qb, vb0;
  { const unsigned lane = lane_id(), r16 = lane & 15, g = lane >> 4, q4 = (lane & 15) >> 2, p = lane & 3;
    qb = c.ldsb + 128 * r16 + 16 * (g ^ ((r16 >> 1) & 7));
    vb0 = c.ldsb + SLOT + 8192 * ((j & 1) ^ 1) + 2048 * (g >> 1) + 64 * (4 * (g & 1) + q4) + 16 * ((p >> 1) ^ (2 * (g & 1))) + 8 * (p & 1); }
  bf16x8 q[4], vf[8]; f32x4 o[8], os[8], negm = {}; float l = 0.f, ms = 0.f, ls = 0.f; u32x4 P = {};
  unsigned sl = 0, fr = 4 * SLOT, pq = 0;
  auto rdq = [&](unsigned blk) { const unsigned q0 = qb + blk, q1 = q0 ^ 64; q[0] = rd128(q0, 0); q[1] = rd128(q1, 0); q[2] = rd128(q0, 2048); q[3] = rd128(q1, 2048); };
  auto rdv = [&]() {
    const unsigned v0 = vb0 + sl, v1 = v0 ^ 32;
#pragma unroll
    for (int cc = 0; cc < 8; ++cc) vf[cc] = cat(rdtr((cc & 1) ? v1 : v0, 512 * (cc >> 1)), rdtr((cc & 1) ? v1 : v0, 512 * (cc >> 1) + 4096));
  };
  auto put = [&](unsigned scr) {
    const unsigned lane = lane_id(), sc = c.ldsb + scr + 16 * lane + 8192 * j;
#pragma unroll
    for (int cc = 0; cc < 8; ++cc) wr128f(sc, 1024 * cc, os[cc]);
    if (lane < 16) { *(LAS float*)(c.ldsb + scr + 32768 + 128 * j + 4 * lane) = ms; *(LAS float*)(c.ldsb + scr + 32768 + 128 * j + 64 + 4 * lane) = ls; }
  };
  auto merge = [&](int hd, unsigned scr) {
    const unsigned lane = lane_id(), sc = c.ldsb + scr + 16 * lane + 2048 * j, ml = c.ldsb + scr + 32768 + 4 * (lane & 15);
    float mw[4], lw[4];
#pragma unroll
    for (int w = 0; w < 4; ++w) { mw[w] = *(const LAS float*)(ml + 128 * w); lw[w] = *(const LAS float*)(ml + 128 * w + 64); }
    const float mt = fmaxf(fmaxf(mw[0], mw[1]), fmaxf(mw[2], mw[3]));
    float lt = 0.f;
#pragma unroll
    for (int w = 0; w < 4; ++w) { mw[w] = ex2(mw[w] - mt); PIN(mw[w]); lt = ffma_s(mw[w], lw[w], lt); }
    const float ir = __builtin_amdgcn_rcpf(lt);
    f32x4 u = {}, v = {};
#pragma unroll
    for (int w = 0; w < 4; ++w) {
      const f32x4 a = rd128f(sc, 8192 * w), b = rd128f(sc, 8192 * w + 1024); float f = mw[w] * ir; PIN(f);
#pragma unroll
      for (int i = 0; i < 4; ++i) { u[i] = ffma_s(a[i], f, u[i]); v[i] = ffma_s(b[i], f, v[i]); }
    }
    u32x4 w4 = {cvtpk(u[0], u[1]), cvtpk(u[2], u[3]), cvtpk(v[0], v[1]), cvtpk(v[2], v[3])};
    __builtin_amdgcn_raw_buffer_store_b128(w4, mkrs(c.oq + (size_t)(2 * c.own + hd) * OBLK, OBLK), 16 * lane + 12 * 2048 + 1024 * (j & 1) + (OBLK / 2) * (j >> 1), 0, OAUX);
  };
#pragma unroll
  for (int cc = 0; cc < 8; ++cc) { os[cc] = f32x4{}; vf[cc] = bf16x8{}; }
  int t = 0;
  unsigned long long ts = 0;
  auto next = [&]() {
    if (t < 18) VMW(8);
    else {
      if (t == 18) put(Q2OFF);
      if (t == 19) merge(0, Q2OFF);
      if (t == 21 && j == 0) signal_quad(c.cnt);
      if (t == 19 || (t == 21 && j == 0)) VMW(9);
      else if (t == 30) { if (j < 2) VMW(16); else VMW(12); }
      else if (t == 31) VMW(12);
      else VMW(8);
      if (t == 29 && j == 0) poll_acquire(pq, c.cnt);
    }
    if (j == 3 && t < 29) { while (__builtin_amdgcn_s_memrealtime() - ts < PACE) __builtin_amdgcn_s_sleep(1); }
    BAR();
    ts = __builtin_amdgcn_s_memrealtime();
    fr = sl; sl = sl == 4 * SLOT ? 0 : sl + 2 * SLOT; ++t;
    if (t < 28) { kdma(t + 2, fr); vdma(t + 2, fr + SLOT); }
    else {
      if (t == 28 && j == 3) touch_code(pq, c.pc, c.idx);
      if (t == 29 && j == 0) poll_issue(pq, c.cnt);
      if (t < 30) kdma(t + 2, fr);
      if (t < 30 || (t == 30 && j < 2)) vdma(t + 2, fr + SLOT);
      if (t == 30 || t == 32) {
        const OD d = od_make(c, j);
#pragma unroll
        for (int n = 0; n < 12; ++n) od_piece(c, d, t == 30 ? 0 : 1, t == 30 ? PS0 : PS1, n);
      }
    }
    PIN(pq);
  };
  rdq(q1addr(12));
  __builtin_amdgcn_s_setprio(BPRIO);
  BAR();
  kdma(2, fr); vdma(2, fr + SLOT);
  if (j >= 2) { VMW(8);
 BAR();
 fr = sl; sl = 2 * SLOT; t = 1; kdma(3, fr); vdma(3, fr + SLOT); }
#pragma nounroll
  for (int hd = 0; hd < 2; ++hd) {
    if (hd) rdq(Q2OFF + 12 * 4096);
#pragma unroll
    for (int cc = 0; cc < 8; ++cc) o[cc] = f32x4{};
    negm = f32x4{}; l = 0.f;
#pragma nounroll
    for (int u = 0; u < 8; ++u) {
      if ((j & 1) == 0) rdv();
      {
        const unsigned k0 = qb + sl + 4096 * (j & 1), k1 = k0 ^ 64;
        f32x4 S[2]; bf16x8 kf[2][4];
#pragma unroll
        for (int h = 0; h < 2; ++h) { kf[h][0] = rd128(k0, 2048 * h); kf[h][1] = rd128(k1, 2048 * h); kf[h][2] = rd128(k0, 2048 * h + 8192); kf[h][3] = rd128(k1, 2048 * h + 8192); }
        LGKM0(); SBAR();
        __builtin_amdgcn_s_setprio(3);
#pragma unroll
        for (int h = 0; h < 2; ++h) { S[h] = MFMA16(kf[h][0], q[0], negm); S[h] = MFMA16(kf[h][1], q[1], S[h]); S[h] = MFMA16(kf[h][2], q[2], S[h]); S[h] = MFMA16(kf[h][3], q[3], S[h]); }
        SBAR();
        __builtin_amdgcn_s_setprio(BPRIO);
        SBAR();
        if (u == 0) {
          const float mx = red_max(fmaxf(fmaxf(fmaxf(S[0][0], S[0][1]), fmaxf(S[0][2], S[0][3])), fmaxf(fmaxf(S[1][0], S[1][1]), fmaxf(S[1][2], S[1][3]))));
          negm = f32x4{-mx, -mx, -mx, -mx}; S[0] -= mx; S[1] -= mx;
        }
#pragma unroll
        for (int i = 0; i < 8; ++i) { S[i >> 2][i & 3] = ex2(S[i >> 2][i & 3]); l += S[i >> 2][i & 3]; PIN(l); }
        P = u32x4{cvtpk(S[0][0], S[0][1]), cvtpk(S[0][2], S[0][3]), cvtpk(S[1][0], S[1][1]), cvtpk(S[1][2], S[1][3])};
      }
      next();
      if (j & 1) rdv();
      LGKM0(); SBAR();
      __builtin_amdgcn_s_setprio(3);
#pragma unroll
      for (int cc = 0; cc < 8; ++cc) o[cc] = MFMA16(vf[cc], __builtin_bit_cast(bf16x8, P), o[cc]);
      SBAR();
      __builtin_amdgcn_s_setprio(BPRIO);
      SBAR();
      if (t == 32) break;
      next();
    }
    ls = red_add(l); ms = -negm[0];
    MFMA_DONE();
    if (__builtin_expect(__any(!(ls + nanprobe_s(o) < L_LIMIT)), 0)) {
      slow16(c.k + (size_t)hd * SK * DH, c.v + (size_t)hd * SK * DH, 32 * j, 16, q, o, ms, ls);
      ls = red_add(ls);
    }
#pragma unroll
    for (int cc = 0; cc < 8; ++cc) os[cc] = o[cc];
  }
  put(SCR2);
  BAR();
  merge(1, SCR2);
  __builtin_amdgcn_s_setprio(0);
}

__global__ __launch_bounds__(512, 2) void fused_k(Args ar) {
  extern __shared__ __attribute__((aligned(16))) char lds[];
  const int wid = __builtin_amdgcn_readfirstlane(threadIdx.x >> 6);
  const int xcd = blockIdx.x & 7, idx = blockIdx.x >> 3, br = xcd >> 2, kq = 2 * (xcd & 1) + ((idx >> 2) & 1), b = 4 * ((xcd >> 1) & 1) + (idx >> 3), hp = 4 * kq + (idx & 3);
  Ctx c;
  c.pc = __builtin_amdgcn_s_getpc() & ~255ull; c.idx = idx;
  c.ldsb = (unsigned)(size_t)(LAS char*)lds;
  c.x = (br ? ar.x1 : ar.x0) + (size_t)b * SQ * DM;
  c.k = (br ? ar.k1 : ar.k0) + ((size_t)b * H + 2 * hp) * SK * DH;
  c.v = (br ? ar.v1 : ar.v0) + ((size_t)b * H + 2 * hp) * SK * DH;
  c.wq = (br ? ar.wq1 : ar.wq0) + 2 * hp * DH;
  const char* wo = (const char*)(br ? ar.wo1 : ar.wo0);
  c.wo = wo + (size_t)kq * 1024 * DM * 2;
  c.warm = wo + (size_t)((xcd & 3) * 32 + idx) * 40960;
  c.oq = ar.ws + WS_O + (size_t)((br * NB + b) * H + 8 * kq) * OBLK;
  c.part = ar.ws + WS_PART + (size_t)((br * NB + b) * 16) * PARTB;
  c.out = ar.out + (size_t)b * SQ * (2 * DM) + br * DM;
  c.cnt2 = (unsigned*)(ar.ws + WS_CNT) + ((br * NB + b) * 4 + (hp & 3)) * 64 + 16; c.kq = kq;
  c.cnt = (unsigned*)(ar.ws + WS_CNT) + ((br * NB + b) * 4 + kq) * 64;
  c.stp = (unsigned long long*)(ar.ws + WS_STAMP) + (blockIdx.x * 8 + wid) * 32;
  c.own = hp & 3; c.nt = hp & 3;
  if (wid < 4) path_a(c, wid); else path_b(c, wid - 4);
  if (wid < 4) { if (wid & 1) oproj<3, 6>(c, wid); else oproj<3, 7>(c, wid); }
  else { if (wid & 1) oproj<2, 7>(c, wid - 4); else oproj<2, 6>(c, wid - 4); }
}

}

extern "C" void kernel_launch(void* const* d_in, const int* in_sizes, int n_in, void* d_out, int out_size, void* d_ws, size_t ws_size, hipStream_t stream) {
  using namespace kk;
  Args a;
  a.x0 = (const u16*)d_in[0]; a.k0 = (const u16*)d_in[1]; a.v0 = (const u16*)d_in[2]; a.wq0 = (const u16*)d_in[3]; a.wo0 = (const u16*)d_in[4];
  a.x1 = (const u16*)d_in[5]; a.k1 = (const u16*)d_in[6]; a.v1 = (const u16*)d_in[7]; a.wq1 = (const u16*)d_in[8]; a.wo1 = (const u16*)d_in[9];
  a.ws = (char*)d_ws; a.out = (u16*)d_out;
  (void)hipFuncSetAttribute((const void*)fused_k, hipFuncAttributeMaxDynamicSharedMemorySize, (int)LDS_TOTAL);
  (void)hipMemsetAsync((char*)d_ws + WS_CNT, 0, CNT_BYTES, stream);
  fused_k<<<256, 512, LDS_TOTAL, stream>>>(a);
}
```

```cpp
#define PACE 118
#define MAP 2
#define FIXT 100
#include <hip/hip_runtime.h>
#include <hip/hip_bf16.h>
#include <utility>

namespace kk {
typedef unsigned short u16;
typedef short bf16x8 __attribute__((ext_vector_type(8)));
typedef short s16x4 __attribute__((ext_vector_type(4)));
typedef float f32x4 __attribute__((ext_vector_type(4)));
typedef float f32x2 __attribute__((ext_vector_type(2)));
typedef __bf16 bf16x2 __attribute__((ext_vector_type(2)));
typedef unsigned u32x4 __attribute__((ext_vector_type(4)));
typedef unsigned u32x2 __attribute__((ext_vector_type(2)));
typedef __amdgpu_buffer_rsrc_t rsrc_t;
typedef __attribute__((address_space(1))) unsigned gu32;
#define LAS __attribute__((address_space(3)))
#define DI __device__ __forceinline__
#define SBAR() __builtin_amdgcn_sched_barrier(0)
#define PIN(x) asm volatile("" : "+v"(x))
#define MFMA16(a, b, c) __builtin_amdgcn_mfma_f32_16x16x32_bf16(a, b, c, 0, 0, 0)
#define MFMA16K16(a, b, c) __builtin_amdgcn_mfma_f32_16x16x16bf16_1k(a, b, c, 0, 0, 0)
#define LGKM0() __builtin_amdgcn_s_waitcnt(0xC07F)
#define VMW(N) asm volatile("s_waitcnt vmcnt(" #N ")" ::: "memory")
#define BAR() do { LGKM0(); asm volatile("s_barrier" ::: "memory"); } while (0)
#define MFMA_DONE() do { asm volatile("s_nop 7\n\ts_nop 3" ::: "memory"); SBAR(); } while (0)
template <int... Is> using iseq = std::integer_sequence<int, Is...>;
template <int N> using mkseq = std::make_integer_sequence<int, N>;
template <int N> using ic = std::integral_constant<int, N>;

constexpr int NB = 8, SQ = 208, DM = 640, H = 32, SK = 1024, DH = 128, HD = H * DH, NRT = 13;
constexpr unsigned SLOT = 16384, XST = 26624, XOFF = 5 * SLOT, Q2OFF = 6 * SLOT, MISC = Q2OFF + NRT * 4096, LDS_TOTAL = 163840;
static_assert(XOFF + 3 * XST <= LDS_TOTAL && MISC + 3 * 4096 == LDS_TOTAL, "lds map");
constexpr unsigned OBLK = NRT * 4096;
constexpr unsigned PST = 47104, POFF = 26624;
constexpr unsigned PS0 = LDS_TOTAL - PST, PS1 = 0, PS2 = PST, SCR2 = PST;
static_assert(SCR2 + 32768 + 512 <= 5 * SLOT && PS2 + PST <= PS0, "lds map 2");
constexpr size_t WS_CNT = 0, CNT_BYTES = 16384, WS_O = 1u << 20, WS_PART = 32u << 20, WS_STAMP = 64u << 20;
constexpr unsigned PARTB = 8 * 11264;
constexpr float QSCALE = 0.08838834764831845f * 1.4426950408889634f;
constexpr float L_LIMIT = 1.0e24f;
__host__ __device__ constexpr unsigned q1addr(int rt) { return rt < 8 ? 4 * SLOT + 4096 * rt : rt < 11 ? MISC + 4096 * (rt - 8) : SLOT + 4096 * (rt - 11); }

DI unsigned cvtpk(float lo, float hi) { f32x2 v = {lo, hi}; return __builtin_bit_cast(unsigned, __builtin_convertvector(v, bf16x2)); }
DI float fmul_s(float a, float b) { float r; asm("v_mul_f32_e32 %0, %1, %2" : "=v"(r) : "s"(b), "v"(a)); return r; }
DI float ffma_s(float a, float b, float c) { float r; asm("v_fma_f32 %0, %1, %2, %3" : "=v"(r) : "v"(a), "v"(b), "v"(c)); return r; }
DI float ex2(float x) { return __builtin_amdgcn_exp2f(x); }
DI bf16x8 rd128(unsigned base, int imm) { return *(const LAS bf16x8*)((const LAS char*)base + imm); }
DI s16x4 rdtr(unsigned base, int imm) { return __builtin_amdgcn_ds_read_tr16_b64_v4i16((LAS s16x4*)((LAS char*)base + imm)); }
DI bf16x8 cat(s16x4 lo, s16x4 hi) { return __builtin_shufflevector(lo, hi, 0, 1, 2, 3, 4, 5, 6, 7); }
DI void wr128f(unsigned base, int imm, f32x4 v) { *(LAS f32x4*)((LAS char*)base + imm) = v; }
DI f32x4 rd128f(unsigned base, int imm) { return *(const LAS f32x4*)((const LAS char*)base + imm); }
DI float red_add(float x) {
  auto a = __builtin_amdgcn_permlane16_swap(__float_as_uint(x), __float_as_uint(x), false, false); x = __uint_as_float(a[0]) + __uint_as_float(a[1]);
  auto b = __builtin_amdgcn_permlane32_swap(__float_as_uint(x), __float_as_uint(x), false, false); return __uint_as_float(b[0]) + __uint_as_float(b[1]); }
DI float red_max(float x) {
  auto a = __builtin_amdgcn_permlane16_swap(__float_as_uint(x), __float_as_uint(x), false, false); x = fmaxf(__uint_as_float(a[0]), __uint_as_float(a[1]));
  auto b = __builtin_amdgcn_permlane32_swap(__float_as_uint(x), __float_as_uint(x), false, false); return fmaxf(__uint_as_float(b[0]), __uint_as_float(b[1])); }
DI int lane_id() { int l; asm volatile("v_mbcnt_lo_u32_b32 %0, -1, 0\n\tv_mbcnt_hi_u32_b32 %0, -1, %0" : "=v"(l)); return l; }
DI rsrc_t mkrs(const void* p, unsigned bytes) { return __builtin_amdgcn_make_buffer_rsrc((void*)p, 0, bytes, 0x00020000); }

template <int IMM> DI void dma_nt(unsigned m0v, unsigned voff, rsrc_t rs, unsigned soff) {
  asm volatile("s_mov_b32 m0, %0\n\ts_nop 0\n\tbuffer_load_dwordx4 %1, %2, %3 offen offset:%4 nt lds" ::"s"(m0v), "v"(voff), "s"(rs), "s"(soff), "i"(IMM) : "memory");
}
DI void dma_df(unsigned m0v, unsigned voff, rsrc_t rs, unsigned soff) {
  asm volatile("s_mov_b32 m0, %0\n\ts_nop 0\n\tbuffer_load_dwordx4 %1, %2, %3 offen lds" ::"s"(m0v), "v"(voff), "s"(rs), "s"(soff) : "memory");
}
#define CODE_LINES 14
constexpr unsigned CODE_BYTES = CODE_LINES * 4096;
DI void touch_code(unsigned& w, unsigned long long pc, int idx) {
  const rsrc_t rs = __builtin_amdgcn_make_buffer_rsrc((void*)pc, 0, CODE_BYTES, 0x00020000);
  const unsigned l = lane_id(), vo = l < CODE_LINES ? 128 * (l + CODE_LINES * idx) : 0x80000000u;
  asm volatile("s_nop 4\n\tbuffer_load_dword %0, %1, %2, 0 offen" : "=v"(w) : "v"(vo), "s"(rs) : "memory");
}
DI void dma_sc1(unsigned m0v, unsigned voff, rsrc_t rs, unsigned soff) {
  asm volatile("s_mov_b32 m0, %0\n\ts_nop 0\n\tbuffer_load_dwordx4 %1, %2, %3 offen sc1 lds" ::"s"(m0v), "v"(voff), "s"(rs), "s"(soff) : "memory");
}
DI void touch80(unsigned& w0, unsigned& w1, const char* p) {
  const unsigned vo = 128 * lane_id();
  asm volatile("s_nop 4\n\tglobal_load_dword %0, %2, %3\n\tglobal_load_dword %1, %2, %3 offset:2048" : "=&v"(w0), "=&v"(w1) : "v"(vo), "s"(p) : "memory");
}
#define BPRIO 0
#define QLATE 0
#define OLATE 0
#define OAUX 16
#define XDMA dma_df
DI void mfma2(f32x4& c, const bf16x8& a0, const bf16x8& b0, const bf16x8& a1, const bf16x8& b1) {
  asm volatile("v_mfma_f32_16x16x32_bf16 %0, %1, %2, %0\n\tv_mfma_f32_16x16x32_bf16 %0, %3, %4, %0" : "+v"(c) : "v"(a0), "v"(b0), "v"(a1), "v"(b1));
}
template <bool SC1> DI void mfma2d(f32x4& c, const bf16x8& a0, const bf16x8& b0, const bf16x8& a1, const bf16x8& b1, unsigned m0v, unsigned voff, rsrc_t rs, unsigned soff) {
  if constexpr (SC1)
    asm volatile("v_mfma_f32_16x16x32_bf16 %0, %1, %2, %0\n\ts_mov_b32 m0, %5\n\tv_mfma_f32_16x16x32_bf16 %0, %3, %4, %0\n\tbuffer_load_dwordx4 %6, %7, %8 offen sc1 lds"
                 : "+v"(c) : "v"(a0), "v"(b0), "v"(a1), "v"(b1), "s"(m0v), "v"(voff), "s"(rs), "s"(soff) : "memory");
  else
    asm volatile("v_mfma_f32_16x16x32_bf16 %0, %1, %2, %0\n\ts_mov_b32 m0, %5\n\tv_mfma_f32_16x16x32_bf16 %0, %3, %4, %0\n\tbuffer_load_dwordx4 %6, %7, %8 offen lds"
                 : "+v"(c) : "v"(a0), "v"(b0), "v"(a1), "v"(b1), "s"(m0v), "v"(voff), "s"(rs), "s"(soff) : "memory");
}
DI void mfma1(f32x4& c, const bf16x8& a, const bf16x8& b) { asm volatile("v_mfma_f32_16x16x32_bf16 %0, %1, %2, %0" : "+v"(c) : "v"(a), "v"(b)); }
DI void poll_issue(unsigned& v, const unsigned* cnt) { const unsigned z = 0; asm volatile("s_nop 4\n\tglobal_load_dword %0, %1, %2 sc1" : "=v"(v) : "v"(z), "s"(cnt) : "memory"); }
DI void poll_acquire(unsigned v, const unsigned* cnt) {
  asm volatile("" : "+v"(v)::"memory");
  if (__builtin_amdgcn_readfirstlane(v) < 4u) {
#pragma nounroll
    for (int spin = 0; spin < (1 << 22); ++spin) {
      const unsigned u = __hip_atomic_load((gu32*)cnt, __ATOMIC_RELAXED, __HIP_MEMORY_SCOPE_AGENT);
      if (__builtin_amdgcn_readfirstlane(u) >= 4u) break;
      __builtin_amdgcn_s_sleep(2);
    }
  }
}
DI void signal_quad(unsigned* cnt) { if (lane_id() == 0) __hip_atomic_fetch_add((gu32*)cnt, 1u, __ATOMIC_RELAXED, __HIP_MEMORY_SCOPE_AGENT); }

#define STAMP(i)
#define TS(x)
#define FST(i)
#define PST_(i)
struct Args { const u16 *x0, *k0, *v0, *wq0, *wo0, *x1, *k1, *v1, *wq1, *wo1; char* ws; u16* out; };
struct Ctx {
  const u16 *x, *k, *v, *wq; const char *wo, *warm; char *oq, *part; u16* out; unsigned *cnt, *cnt2; unsigned long long* stp; unsigned ldsb; int own, nt, kq, idx; unsigned long long pc;
};

DI void slow16(const u16* Kg, const u16* Vg, int key0, int nstep, const bf16x8 (&q)[4], f32x4 (&o)[8], float& m, float& l) {
  const int lane = lane_id(), r = lane & 15, g = lane >> 4;
  m = -3.0e38f; l = 0.f;
#pragma unroll
  for (int d = 0; d < 8; ++d) o[d] = f32x4{};
#pragma nounroll
  for (int st = 0; st < nstep; ++st) {
    const int kb = nstep == 64 ? 16 * st : key0 + 128 * (st >> 1) + 16 * (st & 1);
    f32x4 S = {};
#pragma unroll
    for (int ks = 0; ks < 4; ++ks) S = MFMA16(*(const bf16x8*)(Kg + (size_t)(kb + r) * DH + 32 * ks + 8 * g), q[ks], S);
    const float mx = red_max(fmaxf(fmaxf(S[0], S[1]), fmaxf(S[2], S[3])));
    const float mn = fmaxf(m, mx), al = ex2(m - mn); m = mn;
    float ps = 0.f;
#pragma unroll
    for (int i = 0; i < 4; ++i) { S[i] = ex2(S[i] - mn); ps += S[i]; }
    l = l * al + ps;
    u32x2 pw = {cvtpk(S[0], S[1]), cvtpk(S[2], S[3])};
#pragma unroll
    for (int d = 0; d < 8; ++d) {
      s16x4 vf;
#pragma unroll
      for (int j = 0; j < 4; ++j) vf[j] = (short)Vg[(size_t)(kb + 4 * g + j) * DH + 16 * d + r];
      o[d] *= al;
      o[d] = MFMA16K16(vf, __builtin_bit_cast(s16x4, pw), o[d]);
    }
  }
  __builtin_amdgcn_s_waitcnt(0);
}
DI float nanprobe_s(const f32x4 (&o)[8]) {
  float t = 0.f; const float z = 0.f;
#pragma unroll
  for (int d = 0; d < 8; ++d) { t = ffma_s(o[d][0], z, t); t = ffma_s(o[d][1], z, t); t = ffma_s(o[d][2], z, t); t = ffma_s(o[d][3], z, t); }
  return t;
}
DI float nanprobe(const f32x4 (&o)[8]) {
  f32x4 t = {};
#pragma unroll
  for (int d = 0; d < 8; ++d) t += o[d] * 0.f;
  return (t[0] + t[1]) + (t[2] + t[3]);
}

template <int NR, class FD, class FW> DI void qproj(const Ctx& c, int cg, FD&& dma, FW&& wait) {
  f32x4 acc[NR][4]; bf16x8 w[4][2], x[NR][2];
  const unsigned lane = lane_id(), r16 = lane & 15, g = lane >> 4, q4 = (lane & 15) >> 2, p = lane & 3;
  const unsigned wb0 = c.ldsb + 1024 * (cg & 1) + 2048 * g + 64 * q4 + 16 * ((p >> 1) ^ (2 * (g & 1))) + 8 * (p & 1);
  const unsigned xb0 = c.ldsb + XOFF + (NR == 7 ? 0 : 7 * 2048) + 128 * r16 + 16 * (g ^ ((r16 >> 1) & 7));
#pragma unroll
  for (int r = 0; r < NR; ++r)
#pragma unroll
    for (int i = 0; i < 4; ++i) acc[r][i] = f32x4{};
  unsigned xs = 0, ws = SLOT * (cg >> 1), xp0, xp1, wp0, wp1;
  auto ldw = [&](int i) { const unsigned b = (i & 1) ? wp1 : wp0; const int im = 512 * (i >> 1);
    w[i][0] = cat(rdtr(b, im), rdtr(b, im + 256)); w[i][1] = cat(rdtr(b, im + 8192), rdtr(b, im + 8448)); };
  auto ldx = [&](int r) { x[r][0] = rd128(xp0, 2048 * r); x[r][1] = rd128(xp1, 2048 * r); };
  auto row = [&](int r) {
#pragma unroll
    for (int i = 0; i < 4; ++i) mfma2(acc[r][i], w[i][0], x[r][0], w[i][1], x[r][1]);
  };
  constexpr int LATE_R = QLATE;
  auto half = [&](int r, int h) { mfma2(acc[r][2 * h], w[2 * h][0], x[r][0], w[2 * h][1], x[r][1]); mfma2(acc[r][2 * h + 1], w[2 * h + 1][0], x[r][0], w[2 * h + 1][1], x[r][1]); };
  auto step = [&](auto K, auto F, int s) {
    constexpr bool FIRST = decltype(F)::value;
    xp0 = xb0 + xs; xp1 = xp0 ^ 64; wp0 = wb0 + ws; wp1 = wp0 ^ 32;
    xs = xs == 2 * XST ? 0 : xs + XST; ws += 2 * SLOT; ws = ws >= 5 * SLOT ? ws - 5 * SLOT : ws;
    wait(s);
    SBAR();
    BAR();
    if constexpr (FIRST) { STAMP(1); }
    SBAR();
    if constexpr (NR == 7) {
#pragma unroll
      for (int r = 0; r < 7; ++r) {
        if constexpr (!FIRST) half(r, 0);
        SBAR(); if (r) x[r - 1][0] = rd128(xp0, 2048 * (r - 1)); SBAR();
        if constexpr (!FIRST) half(r, 1);
        SBAR(); if (r) x[r - 1][1] = rd128(xp1, 2048 * (r - 1)); SBAR();
      }
      ldx(6);
#pragma unroll
      for (int i = 0; i < 4; ++i) { ldw(i); dma(K, s, 2 * i); if (i < 3) dma(K, s, 2 * i + 1); SBAR(); }
    } else {
      if constexpr (!FIRST) {
#pragma unroll
        for (int r = 6 - LATE_R; r < 6; ++r) row(r);
        __builtin_amdgcn_s_setprio(0);
        SBAR();
      }
#pragma unroll
      for (int i = 0; i < 4; ++i) { dma(K, s, i); ldw(i); SBAR(); }
#pragma unroll
      for (int r = 0; r < 6; ++r) { if (r < 4) dma(K, s, 4 + r); ldx(r); SBAR(); }
#pragma unroll
      for (int r = 0; r < 6 - LATE_R; ++r) row(r);
      __builtin_amdgcn_s_setprio(1);
      SBAR();
    }
  };
  asm volatile("s_nop 1" ::: "memory");
  step(ic<NR == 7 ? 0 : 3>{}, ic<1>{}, 0);
#pragma nounroll
  for (int s = 1; s < 8; ++s) step(ic<0>{}, ic<0>{}, s);
  step(ic<1>{}, ic<0>{}, 8);
  step(ic<2>{}, ic<0>{}, 9);
  BAR();
  if constexpr (NR == 6) {
#pragma unroll
    for (int r = 6 - LATE_R; r < 6; ++r) row(r);
    __builtin_amdgcn_s_setprio(0);
    SBAR();
  }
  dma(ic<4>{}, 10, 0);
  const unsigned ln = lane_id(), rr = ln & 15, gg = ln >> 4;
  const unsigned qw = c.ldsb + 2048 * (cg & 1) + 128 * rr + 16 * ((gg >> 1) ^ ((rr >> 1) & 7)) + 8 * (gg & 1);
  auto put = [&](int r) {
    const int rt = (NR == 7 ? 0 : 7) + r;
    const unsigned blk = (cg >> 1) ? Q2OFF + 4096 * rt : q1addr(rt);
#pragma unroll
    for (int i = 0; i < 4; ++i) {
      const f32x4 v = acc[r][i];
      *(LAS u32x2*)((qw ^ (32 * i)) + blk) = u32x2{cvtpk(fmul_s(v[0], QSCALE), fmul_s(v[1], QSCALE)), cvtpk(fmul_s(v[2], QSCALE), fmul_s(v[3], QSCALE))};
    }
  };
  SBAR();
  if constexpr (NR == 7) {
#pragma unroll
    for (int r = 0; r < 7; ++r) { row(r); SBAR(); if (r) put(r - 1); SBAR(); }
    MFMA_DONE();
    put(6);
  } else {
    MFMA_DONE();
#pragma unroll
    for (int r = 0; r < 6; ++r) put(r);
  }
}

struct OD { rsrc_t rw, ro; unsigned vwp, vwo, vo, mw, mo, lo; int rg; };
DI OD od_make(const Ctx& c, int rg) {
  OD d; const unsigned lane = lane_id(), xr = 16 * ((lane & 3) ^ (((lane >> 4) & 1) << 1));
  d.rw = mkrs(c.wo, 1024 * DM * 2); d.ro = mkrs(c.oq, 8 * OBLK);
  d.vwp = 1280 * (16 * rg + ((lane >> 2) & 7)) + 64 * (lane >> 5) + xr + 320 * c.nt + ((c.nt & 1) ? 64 : 0);
  d.vwo = 1280 * (16 * rg + (lane >> 2)) + xr + 320 * c.nt + ((c.nt & 1) ? 0 : 256);
  d.vo = 16 * lane; d.mw = c.ldsb + POFF + 5120 * rg; d.mo = c.ldsb + 1024 * rg; d.lo = 1024 * (24 + (rg & 1)); d.rg = rg;
  return d;
}
DI unsigned od_blk(const Ctx& c, int s) { return (unsigned)(s < 16 ? 4 * ((s >> 1) & 3) + 2 * (s >> 3) + (s & 1) : 64); }
template <class F> DI void od_piece(const Ctx& c, const OD& d, unsigned u, unsigned st, int n, F&& f) {
  const unsigned sw = u * (64 * 1280), so = u * (OBLK / 2), m = d.mw + st;
  if (n == 0) f(ic<0>{}, m, d.vwp, d.rw, sw);
  else if (n == 1) f(ic<0>{}, m + 1024, d.vwp, d.rw, sw + 128);
  else if (n == 2) f(ic<0>{}, m + 3072, d.vwp, d.rw, sw + 10240);
  else if (n == 3) f(ic<0>{}, m + 4096, d.vwp, d.rw, sw + 10240 + 128);
  else if (n == 4) f(ic<0>{}, m + 2048, d.vwo, d.rw, sw);
  else if (n < 11) f(ic<1>{}, d.mo + st + 4096 * (n - 5), d.vo, d.ro, so + 1024 * d.rg + 4096 * (n - 5));
  else f(ic<1>{}, c.ldsb + d.lo + st, d.vo, d.ro, so + d.lo);
}
DI void od_piece(const Ctx& c, const OD& d, unsigned u, unsigned st, int n) {
  od_piece(c, d, u, st, n, [](auto S, unsigned m, unsigned v, rsrc_t r, unsigned so) { if constexpr (decltype(S)::value) dma_sc1(m, v, r, so); else dma_df(m, v, r, so); });
}
template <int NC, int NR> DI void oproj(const Ctx& c, int rg) {
  const OD d = od_make(c, rg);
  f32x4 acc[NR][NC]; bf16x8 w[NC][2], x[NR][2];
  const int c0 = NC == 3 ? 3 * (rg >> 1) : 6 + 2 * (rg >> 1), r0 = NR == 7 ? 0 : 7;
  unsigned wb[NC], xb;
  {
    const unsigned lane = lane_id(), g = lane >> 4, q4 = (lane & 15) >> 2, p = lane & 3;
    const unsigned in = 64 * (4 * (g & 1) + q4) + 16 * ((p >> 1) ^ (2 * (g & 1))) + 8 * (p & 1);
#pragma unroll
    for (int i = 0; i < NC; ++i) {
      const int ct = c0 + i;
      const unsigned off = ct < 8 ? 512 * (ct >> 1) + 3072 * (g >> 1) : 2048 + 512 * (g >> 1);
      wb[i] = c.ldsb + POFF + off + (in ^ (32 * (ct & 1)));
    }
    xb = c.ldsb + 16 * lane + 2048 * r0;
  }
#pragma unroll
  for (int r = 0; r < NR; ++r)
#pragma unroll
    for (int i = 0; i < NC; ++i) acc[r][i] = f32x4{};
  unsigned st = PS0, nx = PS1, fr = PS2;
  auto ldw = [&](int i) { const unsigned wp = wb[i] + st; w[i][0] = cat(rdtr(wp, 0), rdtr(wp, 5120)); w[i][1] = cat(rdtr(wp, 10240), rdtr(wp, 15360)); };
  auto ldx = [&](int r) { const unsigned xp = xb + st; x[r][0] = rd128(xp, 2048 * r); x[r][1] = rd128(xp, 2048 * r + 1024); };
  auto row = [&](int r) {
#pragma unroll
    for (int i = 0; i < NC; ++i) mfma2(acc[r][i], w[i][0], x[r][0], w[i][1], x[r][1]);
  };
#pragma unroll
  for (int r = 0; r < NR; ++r) { x[r][0] = bf16x8{}; x[r][1] = bf16x8{}; }
#pragma unroll
  for (int i = 0; i < NC; ++i) { w[i][0] = bf16x8{}; w[i][1] = bf16x8{}; }
  unsigned pv = 0;
  unsigned long long f0 = 0, f1 = 0, f2 = 0, f3 = 0, tp = 0, t2 = 0; (void)f0; (void)f1; (void)f2; (void)f3; (void)tp; (void)t2;
  const bool me = NC == 3 && rg == 1;
  STAMP(8);
  auto tile_d = [&](int r, int i, unsigned u, int n) {
    od_piece(c, d, u, fr, n, [&](auto S, unsigned m, unsigned v, rsrc_t rs, unsigned so) { mfma2d<decltype(S)::value != 0>(acc[r][i], w[i][0], x[r][0], w[i][1], x[r][1], m, v, rs, so); });
  };
  if constexpr (NC == 2) VMW(13);
#pragma nounroll
  for (int s = 0; s < 16; ++s) {
    const unsigned u = od_blk(c, s + 2), xp = xb + st;
    SBAR();
    BAR();
    if (NC == 3 && NR == 7 && rg == 2) PST_(s);
    SBAR();
    if constexpr (NC == 3) {
      if constexpr (NR == 6) { if (me && s == 5) poll_issue(pv, c.cnt + 32); PIN(pv); }
#pragma unroll
      for (int r = 0; r < NR; ++r) {
        if (NR == 6 && r == 0) tile_d(0, 0, u, 11); else mfma2(acc[r][0], w[0][0], x[r][0], w[0][1], x[r][1]);
        SBAR(); if (r) x[r - 1][0] = rd128(xp, 2048 * (r - 1)); SBAR();
        mfma2(acc[r][1], w[1][0], x[r][0], w[1][1], x[r][1]);
        SBAR(); if (r) x[r - 1][1] = rd128(xp, 2048 * (r - 1) + 1024); SBAR();
        tile_d(r, 2, u, r < 5 ? r : r + 5);
        SBAR();
      }
      ldx(NR - 1);
#pragma unroll
      for (int i = 0; i < 3; ++i) ldw(i);
      SBAR();
      VMW(7);
      if constexpr (NR == 6) { if (me && s == 5) poll_acquire(pv, c.cnt + 32); }
    } else {
#pragma unroll
      for (int r = NR - OLATE; r < NR; ++r) row(r);
      __builtin_amdgcn_s_setprio(0);
      SBAR();
      ldw(0); ldx(0); ldw(1);
#pragma unroll
      for (int r = 1; r < NR; ++r) ldx(r);
      SBAR();
      if (NR == 6 && rg == 0 && s == 1) signal_quad(c.cnt + 32);
#pragma unroll
      for (int r = 0; r < NR - OLATE; ++r) {
        if (r < 5) tile_d(r, 0, u, 5 + r); else mfma2(acc[r][0], w[0][0], x[r][0], w[0][1], x[r][1]);
        if (NR - OLATE < 5 && r == 0) tile_d(r, 1, u, 9); else mfma2(acc[r][1], w[1][0], x[r][0], w[1][1], x[r][1]);
        SBAR();
      }
      VMW(5);
      __builtin_amdgcn_s_setprio(1);
    }
    const unsigned t = st; st = nx; nx = fr; fr = t;
  }
  STAMP(9);
  LGKM0(); SBAR();
#pragma unroll
  for (int r = NC == 3 ? 0 : NR - OLATE; r < NR; ++r) row(r);
  __builtin_amdgcn_s_setprio(0);
  MFMA_DONE();
  STAMP(10);
  constexpr int NT = NR * NC, NU = (NT + 1) / 2;
  const int wv = NC == 3 ? rg : 4 + rg;
  const unsigned ln = lane_id(), pv0 = 16 * ln + 11264 * wv;
  const rsrc_t rp = mkrs(c.part, 16 * PARTB);
  f32x4 own[3][2];
  auto scatter = [&](auto K) {
    constexpr int k = decltype(K)::value;
#pragma unroll
    for (int m = 0; m < NU; ++m) {
      const f32x4 u = acc[(2 * m) / NC][(2 * m) % NC], v = 2 * m + 1 < NT ? acc[(2 * m + 1) / NC][(2 * m + 1) % NC] : f32x4{};
      if ((m & 3) == k) { own[m >> 2][0] = u; own[m >> 2][1] = v; }
      else __builtin_amdgcn_raw_buffer_store_b128(u32x4{cvtpk(u[0], u[1]), cvtpk(u[2], u[3]), cvtpk(v[0], v[1]), cvtpk(v[2], v[3])}, rp, pv0 + (4 * k + c.nt) * PARTB + 1024 * m, 0, 16);
    }
#pragma unroll
    for (int e = 0; e < 3; ++e) if (4 * e + k >= NU) { own[e][0] = f32x4{}; own[e][1] = f32x4{}; }
  };
  if (c.kq == 0) scatter(ic<0>{}); else if (c.kq == 1) scatter(ic<1>{}); else if (c.kq == 2) scatter(ic<2>{}); else scatter(ic<3>{});
  VMW(0);
  BAR();
  if (wv == 0) {
    signal_quad(c.cnt2);
#pragma nounroll
    for (int spin = 0; spin < (1 << 22); ++spin) {
      const unsigned u = __hip_atomic_load((gu32*)c.cnt2, __ATOMIC_RELAXED, __HIP_MEMORY_SCOPE_AGENT);
      if (__builtin_amdgcn_readfirstlane(u) >= 4u) break;
      __builtin_amdgcn_s_sleep(1);
    }
  }
  BAR();
  u32x4 pp[3][3];
#pragma unroll
  for (int e = 0; e < 3; ++e)
#pragma unroll
    for (int q = 1; q < 4; ++q) {
      const int m = 4 * e + c.kq;
      pp[e][q - 1] = __builtin_amdgcn_raw_buffer_load_b128(rp, m < NU ? pv0 + (4 * ((c.kq + q) & 3) + c.nt) * PARTB + 1024 * m : 0x80000000u, 0, 16);
    }
#pragma unroll
  for (int e = 0; e < 3; ++e)
#pragma unroll
    for (int h = 0; h < 2; ++h) {
      const int n = 2 * (4 * e + c.kq) + h;
      f32x4 v = own[e][h];
#pragma unroll
      for (int q = 0; q < 3; ++q) {
        v[0] += __uint_as_float(pp[e][q][2 * h] << 16); v[1] += __uint_as_float(pp[e][q][2 * h] & 0xffff0000u);
        v[2] += __uint_as_float(pp[e][q][2 * h + 1] << 16); v[3] += __uint_as_float(pp[e][q][2 * h + 1] & 0xffff0000u);
      }
      if (n < NT) {
        const int rt = r0 + n / NC, ct = c0 + n % NC;
        const int col = 160 * c.nt + (ct < 8 ? ((c.nt & 1) ? 32 : 0) + 16 * ct : ((c.nt & 1) ? 0 : 128) + 16 * (ct - 8));
        *(u32x2*)(c.out + (size_t)(16 * rt + (ln & 15)) * (2 * DM) + col + 4 * (ln >> 4)) = u32x2{cvtpk(v[0], v[1]), cvtpk(v[2], v[3])};
      }
    }
  STAMP(11);
}

struct AS {
  f32x4 o[3][8], S[2][3], negm, l[3]; bf16x8 q[3][4], F[2][4], ones; u32x4 P[2][3];
  unsigned kp[2], vp[2], kn[2];
};
template <int N, int I> DI void a_frag(AS& a) {
  constexpr int sub = (N >> 2) & 1, b = N & 3;
  if constexpr ((b & 1) == 0) {
    constexpr int kt = 2 * sub + (b >> 1);
    a.F[N & 1][I] = rd128(N == 8 ? a.kn[I & 1] : a.kp[I & 1], 8192 * (I >> 1) + 2048 * kt);
  } else {
    constexpr int cc = 4 * (b >> 1) + I, imm = 8192 * sub + 512 * (cc >> 1);
    a.F[N & 1][I] = cat(rdtr(a.vp[cc & 1], imm), rdtr(a.vp[cc & 1], imm + 4096));
  }
}
#define LGKM(n) __builtin_amdgcn_s_waitcnt(0xC07F | ((n) << 8))
template <int BLK, int K> DI void sm_exp(AS& a) { constexpr int b = BLK & 3, sb = (b == 1 || b == 2) ? 0 : 1, e = ((b & 1) ? 0 : 6) + K;
  a.S[sb][e >> 2][e & 3] = ex2(a.S[sb][e >> 2][e & 3]); PIN(a.S[sb][e >> 2]); }
template <int BLK, int K> DI void sm_cvt(AS& a) { constexpr int b = BLK & 3, sub = (BLK >> 2) & 1, sb = (b == 1 || b == 2) ? 0 : 1, e = ((b & 1) ? 0 : 6) + 2 * K, pb = b == 0 ? sub ^ 1 : sub, rt = e >> 2;
  a.P[pb][rt][2 * sb + ((e & 3) >> 1)] = cvtpk(a.S[sb][rt][e & 3], a.S[sb][rt][(e & 3) + 1]); PIN(a.P[pb][rt]); }
template <int N, bool MM, bool SM, bool CY, int NX, bool Z, int G> DI void a_gap(AS& a) {
  constexpr int sub = N >> 2, b = N & 3; constexpr bool QK = (b & 1) == 0;
  if constexpr (MM) {
    if constexpr (QK) {
      constexpr int rt = G >> 2, ks = G & 3, sb = b >> 1;
      if constexpr (ks == 0) { if constexpr (Z) a.S[sb][rt] = MFMA16(a.F[N & 1][0], a.q[rt][0], f32x4{}); else a.S[sb][rt] = MFMA16(a.F[N & 1][0], a.q[rt][0], a.negm); }
      else a.S[sb][rt] = MFMA16(a.F[N & 1][ks], a.q[rt][ks], a.S[sb][rt]);
    } else if constexpr (G < 12) {
      constexpr int i = G / 3, rt = G % 3, cc = 4 * (b >> 1) + i;
      if constexpr (Z) a.o[rt][cc] = MFMA16(a.F[N & 1][i], __builtin_bit_cast(bf16x8, a.P[sub ^ 1][rt]), f32x4{});
      else a.o[rt][cc] = MFMA16(a.F[N & 1][i], __builtin_bit_cast(bf16x8, a.P[sub ^ 1][rt]), a.o[rt][cc]);
    } else {
      constexpr int rt = b == 1 ? G - 12 : 2;
      if constexpr (Z) a.l[rt] = MFMA16(a.ones, __builtin_bit_cast(bf16x8, a.P[sub ^ 1][rt]), f32x4{});
      else a.l[rt] = MFMA16(a.ones, __builtin_bit_cast(bf16x8, a.P[sub ^ 1][rt]), a.l[rt]);
    }
    SBAR();
  }
  constexpr int LAST = QK ? 11 : b == 1 ? 13 : 12;
  if constexpr (G == LAST) { if constexpr (QK) LGKM(4); else LGKM(0); }
  if constexpr (G == 5 && !QK) { if constexpr (NX >= 0) LGKM(3); else LGKM(0); }
  SBAR();
  if constexpr (NX >= 0 && (G & 1) == 0 && G < 8) a_frag<NX, G / 2>(a);
  SBAR();
  if constexpr ((G & 1) && G < 12) { if constexpr (SM) sm_exp<N, G / 2>(a); }
  else if constexpr (QK) {
    if constexpr (SM && G >= 8) sm_cvt<N, G / 2 - 4>(a);
  } else {
    if constexpr (G == 0 && CY) sm_cvt<N - 1, 2>(a);
    if constexpr (SM && G >= 8 && G <= 12 && (G & 1) == 0) sm_cvt<N, G / 2 - 4>(a);
  }
  SBAR();
}
template <int N, bool MM, bool SM, bool CY, int NX, bool Z, int... Gs> DI void a_block_(AS& a, iseq<Gs...>) { (a_gap<N, MM, SM, CY, NX, Z, Gs>(a), ...); }
template <int N, bool MM, bool SM, bool CY, int NX, bool Z = false> DI void a_block(AS& a) { a_block_<N, MM, SM, CY, NX, Z>(a, mkseq<(N & 1) == 0 ? 12 : (N & 3) == 1 ? 14 : 13>{}); }
template <bool FIRST> DI void a_period(AS& a) {
  if constexpr (FIRST) {
    a_block<0, true, false, false, -1, true>(a);
    float mx = 3.0e38f;
#pragma unroll
    for (int rt = 0; rt < 3; ++rt) mx = fminf(mx, red_max(fmaxf(fmaxf(a.S[0][rt][0], a.S[0][rt][1]), fmaxf(a.S[0][rt][2], a.S[0][rt][3]))));
    a.negm = f32x4{-mx, -mx, -mx, -mx}; PIN(a.negm);
#pragma unroll
    for (int rt = 0; rt < 3; ++rt) a.S[0][rt] -= mx;
    SBAR();
    a_block<1, false, true, false, 2>(a); a_block<2, true, true, true, -1>(a); a_block<3, false, true, true, 4>(a);
  } else {
    a_block<0, true, true, true, 1>(a); a_block<1, true, true, true, 2>(a); a_block<2, true, true, true, 3>(a); a_block<3, true, true, true, 4>(a);
  }
  a_block<4, true, true, true, 5>(a); a_block<5, true, true, true, 6, FIRST>(a); a_block<6, true, true, true, 7>(a);
  if constexpr (FIRST) VMW(0);
  BAR();
  a_block<7, true, true, true, 8, FIRST>(a);
}

DI void path_a(const Ctx& c, int wi) {
  STAMP(0);
  {
    const rsrc_t rx = mkrs(c.x, SQ * DM * 2);
    const unsigned lane = lane_id(), vox = 1280u * (lane >> 3) + 16u * ((lane & 7) ^ ((4 * (wi & 1) + (lane >> 4)) & 7));
    auto xp = [&](int s, unsigned stage, int n) { const int pc = n < 6 ? wi + 4 * n : 24 + (wi & 1); XDMA(c.ldsb + XOFF + stage + 1024 * pc, vox, rx, 128u * s + 10240u * pc); };
#pragma unroll
    for (int n = 0; n < 7; ++n) xp(0, 0, n);
#pragma unroll
    for (int n = 0; n < 7; ++n) xp(1, XST, n);
    unsigned xf = XST;
    qproj<7>(c, wi,
      [&](auto K, int s, int n) { if constexpr (decltype(K)::value == 0) xp(s + 2, xf, n); },
      [&](int s) { if (s == 9) VMW(0); else VMW(7); xf = xf == 2 * XST ? 0 : xf + XST; });
  }
  FST(11);
  BAR();
  FST(12);
  AS a;
  unsigned kb0, vb0;
  { const unsigned lane = lane_id(), r16 = lane & 15, g = lane >> 4, q4 = (lane & 15) >> 2, p = lane & 3;
    kb0 = c.ldsb + 128 * r16 + 16 * (g ^ ((r16 >> 1) & 7));
    vb0 = c.ldsb + SLOT + 2048 * (g >> 1) + 64 * (4 * (g & 1) + q4) + 16 * ((p >> 1) ^ (2 * (g & 1))) + 8 * (p & 1); }
  unsigned sl = 0;
  a.ones = bf16x8{0x3f80, 0x3f80, 0x3f80, 0x3f80, 0x3f80, 0x3f80, 0x3f80, 0x3f80}; PIN(a.ones);
#pragma nounroll
  for (int hd = 0; hd < 2; ++hd) {
#pragma unroll
    for (int rt = 0; rt < 3; ++rt) {
      const int t = 3 * wi + rt;
      const unsigned blk = hd ? Q2OFF + 4096 * t : (t < 8 ? 4 * SLOT + 4096 * t : t < 11 ? MISC + 4096 * (t - 8) : SLOT + 4096 * (t - 11));
      const unsigned q0 = kb0 + blk, q1 = q0 ^ 64;
      a.q[rt][0] = rd128(q0, 0); a.q[rt][1] = rd128(q1, 0); a.q[rt][2] = rd128(q0, 2048); a.q[rt][3] = rd128(q1, 2048);
      a.P[0][rt] = u32x4{}; a.P[1][rt] = u32x4{};
    }
    a.kn[0] = kb0 + sl; a.kn[1] = a.kn[0] ^ 64;
    a_frag<8, 0>(a); a_frag<8, 1>(a); a_frag<8, 2>(a); a_frag<8, 3>(a);
    if (hd == 0) BAR();
    STAMP(2 + 3 * hd);
    auto setp = [&]() {
      a.kp[0] = a.kn[0]; a.kp[1] = a.kn[1]; a.vp[0] = vb0 + sl; a.vp[1] = a.vp[0] ^ 32;
      sl = sl == 4 * SLOT ? 0 : sl + 2 * SLOT;
      a.kn[0] = kb0 + sl; a.kn[1] = a.kn[0] ^ 64;
      SBAR();
    };
    setp();
    a_period<true>(a);
#pragma nounroll
    for (int t = 1; t < 16; ++t) {
 if (wi == 0) PST_(16 * hd + t);
 setp(); a_period<false>(a); }
    a.vp[0] = vb0 + sl; a.vp[1] = a.vp[0] ^ 32;
    SBAR();
    a_block<0, false, true, true, 1>(a); a_block<1, true, false, true, -1>(a);
    a_frag<3, 0>(a); a_frag<3, 1>(a); a_frag<3, 2>(a); a_frag<3, 3>(a);
    a_block<3, true, false, false, -1>(a);
    if (hd) BAR();
    STAMP(3 + 3 * hd);
    const rsrc_t ro = mkrs(c.oq + (size_t)(2 * c.own + hd) * OBLK, OBLK);
    const unsigned ov = 16 * lane_id() + 6144 * wi;
#pragma unroll
    for (int rt = 0; rt < 3; ++rt) {
      float l = a.l[rt][0];
      if (__builtin_expect(__any(!(l + nanprobe(a.o[rt]) < L_LIMIT)), 0)) {
        float m;
        slow16(c.k + (size_t)hd * SK * DH, c.v + (size_t)hd * SK * DH, 0, 64, a.q[rt], a.o[rt], m, l);
        l = red_add(l);
      }
      const float il = __builtin_amdgcn_rcpf(l);
#pragma unroll
      for (int kk = 0; kk < 4; ++kk) {
        const f32x4 u = a.o[rt][2 * kk] * il, v = a.o[rt][2 * kk + 1] * il;
        u32x4 w = {cvtpk(u[0], u[1]), cvtpk(u[2], u[3]), cvtpk(v[0], v[1]), cvtpk(v[2], v[3])};
        __builtin_amdgcn_raw_buffer_store_b128(w, ro, ov + 2048 * rt + 1024 * (kk & 1) + (OBLK / 2) * (kk >> 1), 0, OAUX);
      }
    }
    STAMP(4 + 3 * hd);
  }
}

DI void path_b(const Ctx& c, int j) {
  STAMP(0);
  const rsrc_t rk = mkrs(c.k, 2 * SK * DH * 2), rv = mkrs(c.v, 2 * SK * DH * 2);
  unsigned vk0, vk1, vv, wm0 = 0, wm1 = 0;
  { const unsigned i = lane_id();
    vk0 = 256 * (i >> 3) + 16 * ((i & 7) ^ (i >> 4)); vk1 = (vk0 ^ 64) + 2048;
    vv = 256 * ((i >> 2) & 7) + 64 * (i >> 5) + 16 * ((i & 3) ^ (((i >> 4) & 1) << 1)); }
  auto kpc = [&](int t, unsigned slot, int n) {
    const unsigned m = c.ldsb + slot + 2048 * j, so = (unsigned)(64 * t + 16 * j) * 256u;
    if (n == 0) dma_nt<0>(m, vk0, rk, so); if (n == 1) dma_nt<128>(m + 8064, vk0, rk, so); if (n == 2) dma_nt<0>(m + 1024, vk1, rk, so); if (n == 3) dma_nt<128>(m + 9088, vk1, rk, so);
  };
  auto vpc = [&](int t, unsigned slot, int n) {
    const unsigned m = c.ldsb + slot + 4096 * j, so = (unsigned)(64 * t - 32 + 16 * j) * 256u;
    if (n == 0) dma_nt<0>(m, vv, rv, so); if (n == 1) dma_nt<128>(m + 896, vv, rv, so); if (n == 2) dma_nt<2048>(m, vv, rv, so); if (n == 3) dma_nt<2176>(m + 896, vv, rv, so);
  };
  auto kdma = [&](int t, unsigned slot) { kpc(t, slot, 0); kpc(t, slot, 1); kpc(t, slot, 2); kpc(t, slot, 3); };
  auto vdma = [&](int t, unsigned slot) { vpc(t, slot, 0); vpc(t, slot, 1); vpc(t, slot, 2); vpc(t, slot, 3); };
  {
    const rsrc_t rw = mkrs(c.wq, (DM - 1) * HD * 2 + 512);
    const unsigned i = lane_id(), vw0 = 8192 * ((i >> 2) & 7) + 64 * (i >> 5) + 16 * (i & 3), vw1 = (vw0 ^ 32) + 65536;
    auto wpc = [&](int it, unsigned slot, int n) {
      dma_df(c.ldsb + slot + 4096 * j + 1024 * n, n < 2 ? vw0 : vw1, rw, (unsigned)(64 * (it >> 1) + 16 * j) * 8192u + 256 * (it & 1) + 128 * (n & 1));
    };
    if (j == 3) touch_code(wm0, c.pc, c.idx);
#pragma nounroll
    for (int it = 0; it < 5; ++it) { wpc(it, it * SLOT, 0); wpc(it, it * SLOT, 1); wpc(it, it * SLOT, 2); wpc(it, it * SLOT, 3); }
    unsigned f0 = SLOT, f1 = 2 * SLOT;
    auto adv = [](unsigned s_) { s_ += 2 * SLOT; return s_ >= 5 * SLOT ? s_ - 5 * SLOT : s_; };
    qproj<6>(c, j,
      [&](auto K, int s, int n) {
        constexpr int k = decltype(K)::value;
        if constexpr (k == 0) { if (n < 4) wpc(2 * s + 3, f0, n); else wpc(2 * s + 4, f1, n - 4); }
        if constexpr (k == 1) { if (n < 4) wpc(19, 4 * SLOT, n); else kpc(0, 0, n - 4);
          if (n == 7) { PIN(wm0); touch80(wm0, wm1, c.warm + 10240 * j); } }
        if constexpr (k == 2) { if (n == 0) { if (j >= 2) vdma(0, SLOT); } else if (n >= 4) kpc(1, 2 * SLOT, n - 4); }
        if constexpr (k == 4) vdma(1, 3 * SLOT);
      },
      [&](int s) { if (s == 0) VMW(12); else if (s == 9) VMW(6); else VMW(4); f0 = adv(f0); f1 = adv(f1); });
  }
  VMW(8);
  asm volatile("" ::"v"(wm0), "v"(wm1));
  BAR();
  unsigned qb, vb0;
  { const unsigned lane = lane_id(), r16 = lane & 15, g = lane >> 4, q4 = (lane & 15) >> 2, p = lane & 3;
    qb = c.ldsb + 128 * r16 + 16 * (g ^ ((r16 >> 1) & 7));
    vb0 = c.ldsb + SLOT + 8192 * ((j & 1) ^ 1) + 2048 * (g >> 1) + 64 * (4 * (g & 1) + q4) + 16 * ((p >> 1) ^ (2 * (g & 1))) + 8 * (p & 1); }
  bf16x8 q[4], vf[8]; f32x4 o[8], os[8], negm = {}; float l = 0.f, ms = 0.f, ls = 0.f; u32x4 P = {};
  unsigned sl = 0, fr = 4 * SLOT, pq = 0;
  auto rdq = [&](unsigned blk) { const unsigned q0 = qb + blk, q1 = q0 ^ 64; q[0] = rd128(q0, 0); q[1] = rd128(q1, 0); q[2] = rd128(q0, 2048); q[3] = rd128(q1, 2048); };
  auto rdv = [&]() {
    const unsigned v0 = vb0 + sl, v1 = v0 ^ 32;
#pragma unroll
    for (int cc = 0; cc < 8; ++cc) vf[cc] = cat(rdtr((cc & 1) ? v1 : v0, 512 * (cc >> 1)), rdtr((cc & 1) ? v1 : v0, 512 * (cc >> 1) + 4096));
  };
  auto put = [&](unsigned scr) {
    const unsigned lane = lane_id(), sc = c.ldsb + scr + 16 * lane + 8192 * j;
#pragma unroll
    for (int cc = 0; cc < 8; ++cc) wr128f(sc, 1024 * cc, os[cc]);
    if (lane < 16) { *(LAS float*)(c.ldsb + scr + 32768 + 128 * j + 4 * lane) = ms; *(LAS float*)(c.ldsb + scr + 32768 + 128 * j + 64 + 4 * lane) = ls; }
  };
  auto merge = [&](int hd, unsigned scr) {
    const unsigned lane = lane_id(), sc = c.ldsb + scr + 16 * lane + 2048 * j, ml = c.ldsb + scr + 32768 + 4 * (lane & 15);
    float mw[4], lw[4];
#pragma unroll
    for (int w = 0; w < 4; ++w) { mw[w] = *(const LAS float*)(ml + 128 * w); lw[w] = *(const LAS float*)(ml + 128 * w + 64); }
    const float mt = fmaxf(fmaxf(mw[0], mw[1]), fmaxf(mw[2], mw[3]));
    float lt = 0.f;
#pragma unroll
    for (int w = 0; w < 4; ++w) { mw[w] = ex2(mw[w] - mt); PIN(mw[w]); lt = ffma_s(mw[w], lw[w], lt); }
    const float ir = __builtin_amdgcn_rcpf(lt);
    f32x4 u = {}, v = {};
#pragma unroll
    for (int w = 0; w < 4; ++w) {
      const f32x4 a = rd128f(sc, 8192 * w), b = rd128f(sc, 8192 * w + 1024); float f = mw[w] * ir; PIN(f);
#pragma unroll
      for (int i = 0; i < 4; ++i) { u[i] = ffma_s(a[i], f, u[i]); v[i] = ffma_s(b[i], f, v[i]); }
    }
    u32x4 w4 = {cvtpk(u[0], u[1]), cvtpk(u[2], u[3]), cvtpk(v[0], v[1]), cvtpk(v[2], v[3])};
    __builtin_amdgcn_raw_buffer_store_b128(w4, mkrs(c.oq + (size_t)(2 * c.own + hd) * OBLK, OBLK), 16 * lane + 12 * 2048 + 1024 * (j & 1) + (OBLK / 2) * (j >> 1), 0, OAUX);
  };
#pragma unroll
  for (int cc = 0; cc < 8; ++cc) { os[cc] = f32x4{}; vf[cc] = bf16x8{}; }
  int t = 0;
  unsigned long long ts = 0;
  auto next = [&]() {
    if (t < 18) VMW(8);
    else {
      if (t == 18) put(Q2OFF);
      if (t == 19) merge(0, Q2OFF);
      if (t == 21 && j == 0) signal_quad(c.cnt);
      if (t == 19 || (t == 21 && j == 0)) VMW(9);
      else if (t == 30) { if (j < 2) VMW(16); else VMW(12); }
      else if (t == 31) VMW(12);
      else VMW(8);
      if (t == 29 && j == 0) poll_acquire(pq, c.cnt);
    }
    if (j == 3 && t < 29) { while (__builtin_amdgcn_s_memrealtime() - ts < PACE) __builtin_amdgcn_s_sleep(1); }
    BAR();
    ts = __builtin_amdgcn_s_memrealtime();
    fr = sl; sl = sl == 4 * SLOT ? 0 : sl + 2 * SLOT; ++t;
    if (t < 28) { kdma(t + 2, fr); vdma(t + 2, fr + SLOT); }
    else {
      if (t == 28 && j == 3) touch_code(pq, c.pc, c.idx);
      if (t == 29 && j == 0) poll_issue(pq, c.cnt);
      if (t < 30) kdma(t + 2, fr);
      if (t < 30 || (t == 30 && j < 2)) vdma(t + 2, fr + SLOT);
      if (t == 30 || t == 32) {
        const OD d = od_make(c, j);
#pragma unroll
        for (int n = 0; n < 12; ++n) od_piece(c, d, t == 30 ? 0 : 1, t == 30 ? PS0 : PS1, n);
      }
    }
    PIN(pq);
  };
  rdq(q1addr(12));
  __builtin_amdgcn_s_setprio(BPRIO);
  BAR();
  kdma(2, fr); vdma(2, fr + SLOT);
  if (j >= 2) { VMW(8);
 BAR();
 fr = sl; sl = 2 * SLOT; t = 1; kdma(3, fr); vdma(3, fr + SLOT); }
#pragma nounroll
  for (int hd = 0; hd < 2; ++hd) {
    if (hd) rdq(Q2OFF + 12 * 4096);
#pragma unroll
    for (int cc = 0; cc < 8; ++cc) o[cc] = f32x4{};
    negm = f32x4{}; l = 0.f;
#pragma nounroll
    for (int u = 0; u < 8; ++u) {
      if ((j & 1) == 0) rdv();
      {
        const unsigned k0 = qb + sl + 4096 * (j & 1), k1 = k0 ^ 64;
        f32x4 S[2]; bf16x8 kf[2][4];
#pragma unroll
        for (int h = 0; h < 2; ++h) { kf[h][0] = rd128(k0, 2048 * h); kf[h][1] = rd128(k1, 2048 * h); kf[h][2] = rd128(k0, 2048 * h + 8192); kf[h][3] = rd128(k1, 2048 * h + 8192); }
        LGKM0(); SBAR();
        __builtin_amdgcn_s_setprio(3);
#pragma unroll
        for (int h = 0; h < 2; ++h) { S[h] = MFMA16(kf[h][0], q[0], negm); S[h] = MFMA16(kf[h][1], q[1], S[h]); S[h] = MFMA16(kf[h][2], q[2], S[h]); S[h] = MFMA16(kf[h][3], q[3], S[h]); }
        SBAR();
        __builtin_amdgcn_s_setprio(BPRIO);
        SBAR();
        if (u == 0) {
          const float mx = red_max(fmaxf(fmaxf(fmaxf(S[0][0], S[0][1]), fmaxf(S[0][2], S[0][3])), fmaxf(fmaxf(S[1][0], S[1][1]), fmaxf(S[1][2], S[1][3]))));
          negm = f32x4{-mx, -mx, -mx, -mx}; S[0] -= mx; S[1] -= mx;
        }
#pragma unroll
        for (int i = 0; i < 8; ++i) { S[i >> 2][i & 3] = ex2(S[i >> 2][i & 3]); l += S[i >> 2][i & 3]; PIN(l); }
        P = u32x4{cvtpk(S[0][0], S[0][1]), cvtpk(S[0][2], S[0][3]), cvtpk(S[1][0], S[1][1]), cvtpk(S[1][2], S[1][3])};
      }
      next();
      if (j & 1) rdv();
      LGKM0(); SBAR();
      __builtin_amdgcn_s_setprio(3);
#pragma unroll
      for (int cc = 0; cc < 8; ++cc) o[cc] = MFMA16(vf[cc], __builtin_bit_cast(bf16x8, P), o[cc]);
      SBAR();
      __builtin_amdgcn_s_setprio(BPRIO);
      SBAR();
      if (t == 32) break;
      next();
    }
    ls = red_add(l); ms = -negm[0];
    MFMA_DONE();
    if (__builtin_expect(__any(!(ls + nanprobe_s(o) < L_LIMIT)), 0)) {
      slow16(c.k + (size_t)hd * SK * DH, c.v + (size_t)hd * SK * DH, 32 * j, 16, q, o, ms, ls);
      ls = red_add(ls);
    }
#pragma unroll
    for (int cc = 0; cc < 8; ++cc) os[cc] = o[cc];
  }
  put(SCR2);
  BAR();
  merge(1, SCR2);
  __builtin_amdgcn_s_setprio(0);
}

__global__ __launch_bounds__(512, 2) void fused_k(Args ar) {
  extern __shared__ __attribute__((aligned(16))) char lds[];
  const int wid = __builtin_amdgcn_readfirstlane(threadIdx.x >> 6);
  const int xcd = blockIdx.x & 7, idx = blockIdx.x >> 3, br = xcd >> 2, kq = 2 * (xcd & 1) + ((idx >> 2) & 1), b = 4 * ((xcd >> 1) & 1) + (idx >> 3), hp = 4 * kq + (idx & 3);
  Ctx c;
  c.pc = __builtin_amdgcn_s_getpc() & ~255ull; c.idx = idx;
  c.ldsb = (unsigned)(size_t)(LAS char*)lds;
  c.x = (br ? ar.x1 : ar.x0) + (size_t)b * SQ * DM;
  c.k = (br ? ar.k1 : ar.k0) + ((size_t)b * H + 2 * hp) * SK * DH;
  c.v = (br ? ar.v1 : ar.v0) + ((size_t)b * H + 2 * hp) * SK * DH;
  c.wq = (br ? ar.wq1 : ar.wq0) + 2 * hp * DH;
  const char* wo = (const char*)(br ? ar.wo1 : ar.wo0);
  c.wo = wo + (size_t)kq * 1024 * DM * 2;
  c.warm = wo + (size_t)((xcd & 3) * 32 + idx) * 40960;
  c.oq = ar.ws + WS_O + (size_t)((br * NB + b) * H + 8 * kq) * OBLK;
  c.part = ar.ws + WS_PART + (size_t)((br * NB + b) * 16) * PARTB;
  c.out = ar.out + (size_t)b * SQ * (2 * DM) + br * DM;
  c.cnt2 = (unsigned*)(ar.ws + WS_CNT) + ((br * NB + b) * 4 + (hp & 3)) * 64 + 16; c.kq = kq;
  c.cnt = (unsigned*)(ar.ws + WS_CNT) + ((br * NB + b) * 4 + kq) * 64;
  c.stp = (unsigned long long*)(ar.ws + WS_STAMP) + (blockIdx.x * 8 + wid) * 32;
  c.own = hp & 3; c.nt = hp & 3;
  const unsigned long long tb = __builtin_amdgcn_s_memrealtime(); __builtin_amdgcn_s_waitcnt(0xC07F);
  if (wid < 4) path_a(c, wid); else path_b(c, wid - 4);
  if (wid < 4) { if (wid & 1) oproj<3, 6>(c, wid); else oproj<3, 7>(c, wid); }
  else { if (wid & 1) oproj<2, 7>(c, wid - 4); else oproj<2, 6>(c, wid - 4); }
  while (__builtin_amdgcn_s_memrealtime() - tb < FIXT) __builtin_amdgcn_s_sleep(2);
}

}

extern "C" void kernel_launch(void* const* d_in, const int* in_sizes, int n_in, void* d_out, int out_size, void* d_ws, size_t ws_size, hipStream_t stream) {
  using namespace kk;
  Args a;
  a.x0 = (const u16*)d_in[0]; a.k0 = (const u16*)d_in[1]; a.v0 = (const u16*)d_in[2]; a.wq0 = (const u16*)d_in[3]; a.wo0 = (const u16*)d_in[4];
  a.x1 = (const u16*)d_in[5]; a.k1 = (const u16*)d_in[6]; a.v1 = (const u16*)d_in[7]; a.wq1 = (const u16*)d_in[8]; a.wo1 = (const u16*)d_in[9];
  a.ws = (char*)d_ws; a.out = (u16*)d_out;
  (void)hipFuncSetAttribute((const void*)fused_k, hipFuncAttributeMaxDynamicSharedMemorySize, (int)LDS_TOTAL);
  (void)hipMemsetAsync((char*)d_ws + WS_CNT, 0, CNT_BYTES, stream);
  fused_k<<<256, 512, LDS_TOTAL, stream>>>(a);
}
```

```cpp
#define PACE 128
#define MAP 2
#define FIXT 15000
#include <hip/hip_runtime.h>
#include <hip/hip_bf16.h>
#include <utility>

namespace kk {
typedef unsigned short u16;
typedef short bf16x8 __attribute__((ext_vector_type(8)));
typedef short s16x4 __attribute__((ext_vector_type(4)));
typedef float f32x4 __attribute__((ext_vector_type(4)));
typedef float f32x2 __attribute__((ext_vector_type(2)));
typedef __bf16 bf16x2 __attribute__((ext_vector_type(2)));
typedef unsigned u32x4 __attribute__((ext_vector_type(4)));
typedef unsigned u32x2 __attribute__((ext_vector_type(2)));
typedef __amdgpu_buffer_rsrc_t rsrc_t;
typedef __attribute__((address_space(1))) unsigned gu32;
#define LAS __attribute__((address_space(3)))
#define DI __device__ __forceinline__
#define SBAR() __builtin_amdgcn_sched_barrier(0)
#define PIN(x) asm volatile("" : "+v"(x))
#define MFMA16(a, b, c) __builtin_amdgcn_mfma_f32_16x16x32_bf16(a, b, c, 0, 0, 0)
#define MFMA16K16(a, b, c) __builtin_amdgcn_mfma_f32_16x16x16bf16_1k(a, b, c, 0, 0, 0)
#define LGKM0() __builtin_amdgcn_s_waitcnt(0xC07F)
#define VMW(N) asm volatile("s_waitcnt vmcnt(" #N ")" ::: "memory")
#define BAR() do { LGKM0(); asm volatile("s_barrier" ::: "memory"); } while (0)
#define MFMA_DONE() do { asm volatile("s_nop 7\n\ts_nop 3" ::: "memory"); SBAR(); } while (0)
template <int... Is> using iseq = std::integer_sequence<int, Is...>;
template <int N> using mkseq = std::make_integer_sequence<int, N>;
template <int N> using ic = std::integral_constant<int, N>;

constexpr int NB = 8, SQ = 208, DM = 640, H = 32, SK = 1024, DH = 128, HD = H * DH, NRT = 13;
constexpr unsigned SLOT = 16384, XST = 26624, XOFF = 5 * SLOT, Q2OFF = 6 * SLOT, MISC = Q2OFF + NRT * 4096, LDS_TOTAL = 163840;
static_assert(XOFF + 3 * XST <= LDS_TOTAL && MISC + 3 * 4096 == LDS_TOTAL, "lds map");
constexpr unsigned OBLK = NRT * 4096;
constexpr unsigned PST = 47104, POFF = 26624;
constexpr unsigned PS0 = LDS_TOTAL - PST, PS1 = 0, PS2 = PST, SCR2 = PST;
static_assert(SCR2 + 32768 + 512 <= 5 * SLOT && PS2 + PST <= PS0, "lds map 2");
constexpr size_t WS_CNT = 0, CNT_BYTES = 16384, WS_O = 1u << 20, WS_PART = 32u << 20, WS_STAMP = 64u << 20;
constexpr unsigned PARTB = 8 * 11264;
constexpr float QSCALE = 0.08838834764831845f * 1.4426950408889634f;
constexpr float L_LIMIT = 1.0e24f;
__host__ __device__ constexpr unsigned q1addr(int rt) { return rt < 8 ? 4 * SLOT + 4096 * rt : rt < 11 ? MISC + 4096 * (rt - 8) : SLOT + 4096 * (rt - 11); }

DI unsigned cvtpk(float lo, float hi) { f32x2 v = {lo, hi}; return __builtin_bit_cast(unsigned, __builtin_convertvector(v, bf16x2)); }
DI float fmul_s(float a, float b) { float r; asm("v_mul_f32_e32 %0, %1, %2" : "=v"(r) : "s"(b), "v"(a)); return r; }
DI float ffma_s(float a, float b, float c) { float r; asm("v_fma_f32 %0, %1, %2, %3" : "=v"(r) : "v"(a), "v"(b), "v"(c)); return r; }
DI float ex2(float x) { return __builtin_amdgcn_exp2f(x); }
DI bf16x8 rd128(unsigned base, int imm) { return *(const LAS bf16x8*)((const LAS char*)base + imm); }
DI s16x4 rdtr(unsigned base, int imm) { return __builtin_amdgcn_ds_read_tr16_b64_v4i16((LAS s16x4*)((LAS char*)base + imm)); }
DI bf16x8 cat(s16x4 lo, s16x4 hi) { return __builtin_shufflevector(lo, hi, 0, 1, 2, 3, 4, 5, 6, 7); }
DI void wr128f(unsigned base, int imm, f32x4 v) { *(LAS f32x4*)((LAS char*)base + imm) = v; }
DI f32x4 rd128f(unsigned base, int imm) { return *(const LAS f32x4*)((const LAS char*)base + imm); }
DI float red_add(float x) {
  auto a = __builtin_amdgcn_permlane16_swap(__float_as_uint(x), __float_as_uint(x), false, false); x = __uint_as_float(a[0]) + __uint_as_float(a[1]);
  auto b = __builtin_amdgcn_permlane32_swap(__float_as_uint(x), __float_as_uint(x), false, false); return __uint_as_float(b[0]) + __uint_as_float(b[1]); }
DI float red_max(float x) {
  auto a = __builtin_amdgcn_permlane16_swap(__float_as_uint(x), __float_as_uint(x), false, false); x = fmaxf(__uint_as_float(a[0]), __uint_as_float(a[1]));
  auto b = __builtin_amdgcn_permlane32_swap(__float_as_uint(x), __float_as_uint(x), false, false); return fmaxf(__uint_as_float(b[0]), __uint_as_float(b[1])); }
DI int lane_id() { int l; asm volatile("v_mbcnt_lo_u32_b32 %0, -1, 0\n\tv_mbcnt_hi_u32_b32 %0, -1, %0" : "=v"(l)); return l; }
DI rsrc_t mkrs(const void* p, unsigned bytes) { return __builtin_amdgcn_make_buffer_rsrc((void*)p, 0, bytes, 0x00020000); }

template <int IMM> DI void dma_nt(unsigned m0v, unsigned voff, rsrc_t rs, unsigned soff) {
  asm volatile("s_mov_b32 m0, %0\n\ts_nop 0\n\tbuffer_load_dwordx4 %1, %2, %3 offen offset:%4 nt lds" ::"s"(m0v), "v"(voff), "s"(rs), "s"(soff), "i"(IMM) : "memory");
}
DI void dma_df(unsigned m0v, unsigned voff, rsrc_t rs, unsigned soff) {
  asm volatile("s_mov_b32 m0, %0\n\ts_nop 0\n\tbuffer_load_dwordx4 %1, %2, %3 offen lds" ::"s"(m0v), "v"(voff), "s"(rs), "s"(soff) : "memory");
}
#define CODE_LINES 14
constexpr unsigned CODE_BYTES = CODE_LINES * 4096;
DI void touch_code(unsigned& w, unsigned long long pc, int idx) {
  const rsrc_t rs = __builtin_amdgcn_make_buffer_rsrc((void*)pc, 0, CODE_BYTES, 0x00020000);
  const unsigned l = lane_id(), vo = l < CODE_LINES ? 128 * (l + CODE_LINES * idx) : 0x80000000u;
  asm volatile("s_nop 4\n\tbuffer_load_dword %0, %1, %2, 0 offen" : "=v"(w) : "v"(vo), "s"(rs) : "memory");
}
DI void dma_sc1(unsigned m0v, unsigned voff, rsrc_t rs, unsigned soff) {
  asm volatile("s_mov_b32 m0, %0\n\ts_nop 0\n\tbuffer_load_dwordx4 %1, %2, %3 offen sc1 lds" ::"s"(m0v), "v"(voff), "s"(rs), "s"(soff) : "memory");
}
DI void touch80(unsigned& w0, unsigned& w1, const char* p) {
  const unsigned vo = 128 * lane_id();
  asm volatile("s_nop 4\n\tglobal_load_dword %0, %2, %3\n\tglobal_load_dword %1, %2, %3 offset:2048" : "=&v"(w0), "=&v"(w1) : "v"(vo), "s"(p) : "memory");
}
#define BPRIO 0
#define QLATE 0
#define OLATE 0
#define OAUX 16
#define XDMA dma_df
DI void mfma2(f32x4& c, const bf16x8& a0, const bf16x8& b0, const bf16x8& a1, const bf16x8& b1) {
  asm volatile("v_mfma_f32_16x16x32_bf16 %0, %1, %2, %0\n\tv_mfma_f32_16x16x32_bf16 %0, %3, %4, %0" : "+v"(c) : "v"(a0), "v"(b0), "v"(a1), "v"(b1));
}
template <bool SC1> DI void mfma2d(f32x4& c, const bf16x8& a0, const bf16x8& b0, const bf16x8& a1, const bf16x8& b1, unsigned m0v, unsigned voff, rsrc_t rs, unsigned soff) {
  if constexpr (SC1)
    asm volatile("v_mfma_f32_16x16x32_bf16 %0, %1, %2, %0\n\ts_mov_b32 m0, %5\n\tv_mfma_f32_16x16x32_bf16 %0, %3, %4, %0\n\tbuffer_load_dwordx4 %6, %7, %8 offen sc1 lds"
                 : "+v"(c) : "v"(a0), "v"(b0), "v"(a1), "v"(b1), "s"(m0v), "v"(voff), "s"(rs), "s"(soff) : "memory");
  else
    asm volatile("v_mfma_f32_16x16x32_bf16 %0, %1, %2, %0\n\ts_mov_b32 m0, %5\n\tv_mfma_f32_16x16x32_bf16 %0, %3, %4, %0\n\tbuffer_load_dwordx4 %6, %7, %8 offen lds"
                 : "+v"(c) : "v"(a0), "v"(b0), "v"(a1), "v"(b1), "s"(m0v), "v"(voff), "s"(rs), "s"(soff) : "memory");
}
DI void mfma1(f32x4& c, const bf16x8& a, const bf16x8& b) { asm volatile("v_mfma_f32_16x16x32_bf16 %0, %1, %2, %0" : "+v"(c) : "v"(a), "v"(b)); }
DI void poll_issue(unsigned& v, const unsigned* cnt) { const unsigned z = 0; asm volatile("s_nop 4\n\tglobal_load_dword %0, %1, %2 sc1" : "=v"(v) : "v"(z), "s"(cnt) : "memory"); }
DI void poll_acquire(unsigned v, const unsigned* cnt) {
  asm volatile("" : "+v"(v)::"memory");
  { const unsigned long long t0 = __builtin_amdgcn_s_memrealtime(); while (__builtin_amdgcn_s_memrealtime() - t0 < 800) __builtin_amdgcn_s_sleep(2); }
}
DI void signal_quad(unsigned* cnt) { if (lane_id() == 0) __hip_atomic_fetch_add((gu32*)cnt, 1u, __ATOMIC_RELAXED, __HIP_MEMORY_SCOPE_AGENT); }

#define STAMP(i)
#define TS(x)
#define FST(i)
#define PST_(i)
struct Args { const u16 *x0, *k0, *v0, *wq0, *wo0, *x1, *k1, *v1, *wq1, *wo1; char* ws; u16* out; };
struct Ctx {
  const u16 *x, *k, *v, *wq; const char *wo, *warm; char *oq, *part; u16* out; unsigned *cnt, *cnt2; unsigned long long* stp; unsigned ldsb; int own, nt, kq, idx; unsigned long long pc;
};

DI void slow16(const u16* Kg, const u16* Vg, int key0, int nstep, const bf16x8 (&q)[4], f32x4 (&o)[8], float& m, float& l) {
  const int lane = lane_id(), r = lane & 15, g = lane >> 4;
  m = -3.0e38f; l = 0.f;
#pragma unroll
  for (int d = 0; d < 8; ++d) o[d] = f32x4{};
#pragma nounroll
  for (int st = 0; st < nstep; ++st) {
    const int kb = nstep == 64 ? 16 * st : key0 + 128 * (st >> 1) + 16 * (st & 1);
    f32x4 S = {};
#pragma unroll
    for (int ks = 0; ks < 4; ++ks) S = MFMA16(*(const bf16x8*)(Kg + (size_t)(kb + r) * DH + 32 * ks + 8 * g), q[ks], S);
    const float mx = red_max(fmaxf(fmaxf(S[0], S[1]), fmaxf(S[2], S[3])));
    const float mn = fmaxf(m, mx), al = ex2(m - mn); m = mn;
    float ps = 0.f;
#pragma unroll
    for (int i = 0; i < 4; ++i) { S[i] = ex2(S[i] - mn); ps += S[i]; }
    l = l * al + ps;
    u32x2 pw = {cvtpk(S[0], S[1]), cvtpk(S[2], S[3])};
#pragma unroll
    for (int d = 0; d < 8; ++d) {
      s16x4 vf;
#pragma unroll
      for (int j = 0; j < 4; ++j) vf[j] = (short)Vg[(size_t)(kb + 4 * g + j) * DH + 16 * d + r];
      o[d] *= al;
      o[d] = MFMA16K16(vf, __builtin_bit_cast(s16x4, pw), o[d]);
    }
  }
  __builtin_amdgcn_s_waitcnt(0);
}
DI float nanprobe_s(const f32x4 (&o)[8]) {
  float t = 0.f; const float z = 0.f;
#pragma unroll
  for (int d = 0; d < 8; ++d) { t = ffma_s(o[d][0], z, t); t = ffma_s(o[d][1], z, t); t = ffma_s(o[d][2], z, t); t = ffma_s(o[d][3], z, t); }
  return t;
}
DI float nanprobe(const f32x4 (&o)[8]) {
  f32x4 t = {};
#pragma unroll
  for (int d = 0; d < 8; ++d) t += o[d] * 0.f;
  return (t[0] + t[1]) + (t[2] + t[3]);
}

template <int NR, class FD, class FW> DI void qproj(const Ctx& c, int cg, FD&& dma, FW&& wait) {
  f32x4 acc[NR][4]; bf16x8 w[4][2], x[NR][2];
  const unsigned lane = lane_id(), r16 = lane & 15, g = lane >> 4, q4 = (lane & 15) >> 2, p = lane & 3;
  const unsigned wb0 = c.ldsb + 1024 * (cg & 1) + 2048 * g + 64 * q4 + 16 * ((p >> 1) ^ (2 * (g & 1))) + 8 * (p & 1);
  const unsigned xb0 = c.ldsb + XOFF + (NR == 7 ? 0 : 7 * 2048) + 128 * r16 + 16 * (g ^ ((r16 >> 1) & 7));
#pragma unroll
  for (int r = 0; r < NR; ++r)
#pragma unroll
    for (int i = 0; i < 4; ++i) acc[r][i] = f32x4{};
  unsigned xs = 0, ws = SLOT * (cg >> 1), xp0, xp1, wp0, wp1;
  auto ldw = [&](int i) { const unsigned b = (i & 1) ? wp1 : wp0; const int im = 512 * (i >> 1);
    w[i][0] = cat(rdtr(b, im), rdtr(b, im + 256)); w[i][1] = cat(rdtr(b, im + 8192), rdtr(b, im + 8448)); };
  auto ldx = [&](int r) { x[r][0] = rd128(xp0, 2048 * r); x[r][1] = rd128(xp1, 2048 * r); };
  auto row = [&](int r) {
#pragma unroll
    for (int i = 0; i < 4; ++i) mfma2(acc[r][i], w[i][0], x[r][0], w[i][1], x[r][1]);
  };
  constexpr int LATE_R = QLATE;
  auto half = [&](int r, int h) { mfma2(acc[r][2 * h], w[2 * h][0], x[r][0], w[2 * h][1], x[r][1]); mfma2(acc[r][2 * h + 1], w[2 * h + 1][0], x[r][0], w[2 * h + 1][1], x[r][1]); };
  auto step = [&](auto K, auto F, int s) {
    constexpr bool FIRST = decltype(F)::value;
    xp0 = xb0 + xs; xp1 = xp0 ^ 64; wp0 = wb0 + ws; wp1 = wp0 ^ 32;
    xs = xs == 2 * XST ? 0 : xs + XST; ws += 2 * SLOT; ws = ws >= 5 * SLOT ? ws - 5 * SLOT : ws;
    wait(s);
    SBAR();
    BAR();
    if constexpr (FIRST) { STAMP(1); }
    SBAR();
    if constexpr (NR == 7) {
#pragma unroll
      for (int r = 0; r < 7; ++r) {
        if constexpr (!FIRST) half(r, 0);
        SBAR(); if (r) x[r - 1][0] = rd128(xp0, 2048 * (r - 1)); SBAR();
        if constexpr (!FIRST) half(r, 1);
        SBAR(); if (r) x[r - 1][1] = rd128(xp1, 2048 * (r - 1)); SBAR();
      }
      ldx(6);
#pragma unroll
      for (int i = 0; i < 4; ++i) { ldw(i); dma(K, s, 2 * i); if (i < 3) dma(K, s, 2 * i + 1); SBAR(); }
    } else {
      if constexpr (!FIRST) {
#pragma unroll
        for (int r = 6 - LATE_R; r < 6; ++r) row(r);
        __builtin_amdgcn_s_setprio(0);
        SBAR();
      }
#pragma unroll
      for (int i = 0; i < 4; ++i) { dma(K, s, i); ldw(i); SBAR(); }
#pragma unroll
      for (int r = 0; r < 6; ++r) { if (r < 4) dma(K, s, 4 + r); ldx(r); SBAR(); }
#pragma unroll
      for (int r = 0; r < 6 - LATE_R; ++r) row(r);
      __builtin_amdgcn_s_setprio(1);
      SBAR();
    }
  };
  asm volatile("s_nop 1" ::: "memory");
  step(ic<NR == 7 ? 0 : 3>{}, ic<1>{}, 0);
#pragma nounroll
  for (int s = 1; s < 8; ++s) step(ic<0>{}, ic<0>{}, s);
  step(ic<1>{}, ic<0>{}, 8);
  step(ic<2>{}, ic<0>{}, 9);
  BAR();
  if constexpr (NR == 6) {
#pragma unroll
    for (int r = 6 - LATE_R; r < 6; ++r) row(r);
    __builtin_amdgcn_s_setprio(0);
    SBAR();
  }
  dma(ic<4>{}, 10, 0);
  const unsigned ln = lane_id(), rr = ln & 15, gg = ln >> 4;
  const unsigned qw = c.ldsb + 2048 * (cg & 1) + 128 * rr + 16 * ((gg >> 1) ^ ((rr >> 1) & 7)) + 8 * (gg & 1);
  auto put = [&](int r) {
    const int rt = (NR == 7 ? 0 : 7) + r;
    const unsigned blk = (cg >> 1) ? Q2OFF + 4096 * rt : q1addr(rt);
#pragma unroll
    for (int i = 0; i < 4; ++i) {
      const f32x4 v = acc[r][i];
      *(LAS u32x2*)((qw ^ (32 * i)) + blk) = u32x2{cvtpk(fmul_s(v[0], QSCALE), fmul_s(v[1], QSCALE)), cvtpk(fmul_s(v[2], QSCALE), fmul_s(v[3], QSCALE))};
    }
  };
  SBAR();
  if constexpr (NR == 7) {
#pragma unroll
    for (int r = 0; r < 7; ++r) { row(r); SBAR(); if (r) put(r - 1); SBAR(); }
    MFMA_DONE();
    put(6);
  } else {
    MFMA_DONE();
#pragma unroll
    for (int r = 0; r < 6; ++r) put(r);
  }
}

struct OD { rsrc_t rw, ro; unsigned vwp, vwo, vo, mw, mo, lo; int rg; };
DI OD od_make(const Ctx& c, int rg) {
  OD d; const unsigned lane = lane_id(), xr = 16 * ((lane & 3) ^ (((lane >> 4) & 1) << 1));
  d.rw = mkrs(c.wo, 1024 * DM * 2); d.ro = mkrs(c.oq, 8 * OBLK);
  d.vwp = 1280 * (16 * rg + ((lane >> 2) & 7)) + 64 * (lane >> 5) + xr + 320 * c.nt + ((c.nt & 1) ? 64 : 0);
  d.vwo = 1280 * (16 * rg + (lane >> 2)) + xr + 320 * c.nt + ((c.nt & 1) ? 0 : 256);
  d.vo = 16 * lane; d.mw = c.ldsb + POFF + 5120 * rg; d.mo = c.ldsb + 1024 * rg; d.lo = 1024 * (24 + (rg & 1)); d.rg = rg;
  return d;
}
DI unsigned od_blk(const Ctx& c, int s) { return (unsigned)(s < 16 ? 4 * ((s >> 1) & 3) + 2 * (s >> 3) + (s & 1) : 64); }
template <class F> DI void od_piece(const Ctx& c, const OD& d, unsigned u, unsigned st, int n, F&& f) {
  const unsigned sw = u * (64 * 1280), so = u * (OBLK / 2), m = d.mw + st;
  if (n == 0) f(ic<0>{}, m, d.vwp, d.rw, sw);
  else if (n == 1) f(ic<0>{}, m + 1024, d.vwp, d.rw, sw + 128);
  else if (n == 2) f(ic<0>{}, m + 3072, d.vwp, d.rw, sw + 10240);
  else if (n == 3) f(ic<0>{}, m + 4096, d.vwp, d.rw, sw + 10240 + 128);
  else if (n == 4) f(ic<0>{}, m + 2048, d.vwo, d.rw, sw);
  else if (n < 11) f(ic<1>{}, d.mo + st + 4096 * (n - 5), d.vo, d.ro, so + 1024 * d.rg + 4096 * (n - 5));
  else f(ic<1>{}, c.ldsb + d.lo + st, d.vo, d.ro, so + d.lo);
}
DI void od_piece(const Ctx& c, const OD& d, unsigned u, unsigned st, int n) {
  od_piece(c, d, u, st, n, [](auto S, unsigned m, unsigned v, rsrc_t r, unsigned so) { if constexpr (decltype(S)::value) dma_sc1(m, v, r, so); else dma_df(m, v, r, so); });
}
template <int NC, int NR> DI void oproj(const Ctx& c, int rg) {
  const OD d = od_make(c, rg);
  f32x4 acc[NR][NC]; bf16x8 w[NC][2], x[NR][2];
  const int c0 = NC == 3 ? 3 * (rg >> 1) : 6 + 2 * (rg >> 1), r0 = NR == 7 ? 0 : 7;
  unsigned wb[NC], xb;
  {
    const unsigned lane = lane_id(), g = lane >> 4, q4 = (lane & 15) >> 2, p = lane & 3;
    const unsigned in = 64 * (4 * (g & 1) + q4) + 16 * ((p >> 1) ^ (2 * (g & 1))) + 8 * (p & 1);
#pragma unroll
    for (int i = 0; i < NC; ++i) {
      const int ct = c0 + i;
      const unsigned off = ct < 8 ? 512 * (ct >> 1) + 3072 * (g >> 1) : 2048 + 512 * (g >> 1);
      wb[i] = c.ldsb + POFF + off + (in ^ (32 * (ct & 1)));
    }
    xb = c.ldsb + 16 * lane + 2048 * r0;
  }
#pragma unroll
  for (int r = 0; r < NR; ++r)
#pragma unroll
    for (int i = 0; i < NC; ++i) acc[r][i] = f32x4{};
  unsigned st = PS0, nx = PS1, fr = PS2;
  auto ldw = [&](int i) { const unsigned wp = wb[i] + st; w[i][0] = cat(rdtr(wp, 0), rdtr(wp, 5120)); w[i][1] = cat(rdtr(wp, 10240), rdtr(wp, 15360)); };
  auto ldx = [&](int r) { const unsigned xp = xb + st; x[r][0] = rd128(xp, 2048 * r); x[r][1] = rd128(xp, 2048 * r + 1024); };
  auto row = [&](int r) {
#pragma unroll
    for (int i = 0; i < NC; ++i) mfma2(acc[r][i], w[i][0], x[r][0], w[i][1], x[r][1]);
  };
#pragma unroll
  for (int r = 0; r < NR; ++r) { x[r][0] = bf16x8{}; x[r][1] = bf16x8{}; }
#pragma unroll
  for (int i = 0; i < NC; ++i) { w[i][0] = bf16x8{}; w[i][1] = bf16x8{}; }
  unsigned pv = 0;
  unsigned long long f0 = 0, f1 = 0, f2 = 0, f3 = 0, tp = 0, t2 = 0; (void)f0; (void)f1; (void)f2; (void)f3; (void)tp; (void)t2;
  const bool me = NC == 3 && rg == 1;
  STAMP(8);
  auto tile_d = [&](int r, int i, unsigned u, int n) {
    od_piece(c, d, u, fr, n, [&](auto S, unsigned m, unsigned v, rsrc_t rs, unsigned so) { mfma2d<decltype(S)::value != 0>(acc[r][i], w[i][0], x[r][0], w[i][1], x[r][1], m, v, rs, so); });
  };
  if constexpr (NC == 2) VMW(13);
#pragma nounroll
  for (int s = 0; s < 16; ++s) {
    const unsigned u = od_blk(c, s + 2), xp = xb + st;
    SBAR();
    BAR();
    if (NC == 3 && NR == 7 && rg == 2) PST_(s);
    SBAR();
    if constexpr (NC == 3) {
      if constexpr (NR == 6) { if (me && s == 5) poll_issue(pv, c.cnt + 32); PIN(pv); }
#pragma unroll
      for (int r = 0; r < NR; ++r) {
        if (NR == 6 && r == 0) tile_d(0, 0, u, 11); else mfma2(acc[r][0], w[0][0], x[r][0], w[0][1], x[r][1]);
        SBAR(); if (r) x[r - 1][0] = rd128(xp, 2048 * (r - 1)); SBAR();
        mfma2(acc[r][1], w[1][0], x[r][0], w[1][1], x[r][1]);
        SBAR(); if (r) x[r - 1][1] = rd128(xp, 2048 * (r - 1) + 1024); SBAR();
        tile_d(r, 2, u, r < 5 ? r : r + 5);
        SBAR();
      }
      ldx(NR - 1);
#pragma unroll
      for (int i = 0; i < 3; ++i) ldw(i);
      SBAR();
      VMW(7);
      if constexpr (NR == 6) { if (me && s == 5) poll_acquire(pv, c.cnt + 32); }
    } else {
#pragma unroll
      for (int r = NR - OLATE; r < NR; ++r) row(r);
      __builtin_amdgcn_s_setprio(0);
      SBAR();
      ldw(0); ldx(0); ldw(1);
#pragma unroll
      for (int r = 1; r < NR; ++r) ldx(r);
      SBAR();
      if (NR == 6 && rg == 0 && s == 1) signal_quad(c.cnt + 32);
#pragma unroll
      for (int r = 0; r < NR - OLATE; ++r) {
        if (r < 5) tile_d(r, 0, u, 5 + r); else mfma2(acc[r][0], w[0][0], x[r][0], w[0][1], x[r][1]);
        if (NR - OLATE < 5 && r == 0) tile_d(r, 1, u, 9); else mfma2(acc[r][1], w[1][0], x[r][0], w[1][1], x[r][1]);
        SBAR();
      }
      VMW(5);
      __builtin_amdgcn_s_setprio(1);
    }
    const unsigned t = st; st = nx; nx = fr; fr = t;
  }
  STAMP(9);
  LGKM0(); SBAR();
#pragma unroll
  for (int r = NC == 3 ? 0 : NR - OLATE; r < NR; ++r) row(r);
  __builtin_amdgcn_s_setprio(0);
  MFMA_DONE();
  STAMP(10);
  constexpr int NT = NR * NC, NU = (NT + 1) / 2;
  const int wv = NC == 3 ? rg : 4 + rg;
  const unsigned ln = lane_id(), pv0 = 16 * ln + 11264 * wv;
  const rsrc_t rp = mkrs(c.part, 16 * PARTB);
  f32x4 own[3][2];
  auto scatter = [&](auto K) {
    constexpr int k = decltype(K)::value;
#pragma unroll
    for (int m = 0; m < NU; ++m) {
      const f32x4 u = acc[(2 * m) / NC][(2 * m) % NC], v = 2 * m + 1 < NT ? acc[(2 * m + 1) / NC][(2 * m + 1) % NC] : f32x4{};
      if ((m & 3) == k) { own[m >> 2][0] = u; own[m >> 2][1] = v; }
      else __builtin_amdgcn_raw_buffer_store_b128(u32x4{cvtpk(u[0], u[1]), cvtpk(u[2], u[3]), cvtpk(v[0], v[1]), cvtpk(v[2], v[3])}, rp, pv0 + (4 * k + c.nt) * PARTB + 1024 * m, 0, 16);
    }
#pragma unroll
    for (int e = 0; e < 3; ++e) if (4 * e + k >= NU) { own[e][0] = f32x4{}; own[e][1] = f32x4{}; }
  };
  if (c.kq == 0) scatter(ic<0>{}); else if (c.kq == 1) scatter(ic<1>{}); else if (c.kq == 2) scatter(ic<2>{}); else scatter(ic<3>{});
  VMW(0);
  BAR();
  if (wv == 0) {
    signal_quad(c.cnt2);
    { const unsigned long long t0 = __builtin_amdgcn_s_memrealtime(); while (__builtin_amdgcn_s_memrealtime() - t0 < 1500) __builtin_amdgcn_s_sleep(2); }
  }
  BAR();
  u32x4 pp[3][3];
#pragma unroll
  for (int e = 0; e < 3; ++e)
#pragma unroll
    for (int q = 1; q < 4; ++q) {
      const int m = 4 * e + c.kq;
      pp[e][q - 1] = __builtin_amdgcn_raw_buffer_load_b128(rp, m < NU ? pv0 + (4 * ((c.kq + q) & 3) + c.nt) * PARTB + 1024 * m : 0x80000000u, 0, 16);
    }
#pragma unroll
  for (int e = 0; e < 3; ++e)
#pragma unroll
    for (int h = 0; h < 2; ++h) {
      const int n = 2 * (4 * e + c.kq) + h;
      f32x4 v = own[e][h];
#pragma unroll
      for (int q = 0; q < 3; ++q) {
        v[0] += __uint_as_float(pp[e][q][2 * h] << 16); v[1] += __uint_as_float(pp[e][q][2 * h] & 0xffff0000u);
        v[2] += __uint_as_float(pp[e][q][2 * h + 1] << 16); v[3] += __uint_as_float(pp[e][q][2 * h + 1] & 0xffff0000u);
      }
      if (n < NT) {
        const int rt = r0 + n / NC, ct = c0 + n % NC;
        const int col = 160 * c.nt + (ct < 8 ? ((c.nt & 1) ? 32 : 0) + 16 * ct : ((c.nt & 1) ? 0 : 128) + 16 * (ct - 8));
        *(u32x2*)(c.out + (size_t)(16 * rt + (ln & 15)) * (2 * DM) + col + 4 * (ln >> 4)) = u32x2{cvtpk(v[0], v[1]), cvtpk(v[2], v[3])};
      }
    }
  STAMP(11);
}

struct AS {
  f32x4 o[3][8], S[2][3], negm, l[3]; bf16x8 q[3][4], F[2][4], ones; u32x4 P[2][3];
  unsigned kp[2], vp[2], kn[2];
};
template <int N, int I> DI void a_frag(AS& a) {
  constexpr int sub = (N >> 2) & 1, b = N & 3;
  if constexpr ((b & 1) == 0) {
    constexpr int kt = 2 * sub + (b >> 1);
    a.F[N & 1][I] = rd128(N == 8 ? a.kn[I & 1] : a.kp[I & 1], 8192 * (I >> 1) + 2048 * kt);
  } else {
    constexpr int cc = 4 * (b >> 1) + I, imm = 8192 * sub + 512 * (cc >> 1);
    a.F[N & 1][I] = cat(rdtr(a.vp[cc & 1], imm), rdtr(a.vp[cc & 1], imm + 4096));
  }
}
#define LGKM(n) __builtin_amdgcn_s_waitcnt(0xC07F | ((n) << 8))
template <int BLK, int K> DI void sm_exp(AS& a) { constexpr int b = BLK & 3, sb = (b == 1 || b == 2) ? 0 : 1, e = ((b & 1) ? 0 : 6) + K;
  a.S[sb][e >> 2][e & 3] = ex2(a.S[sb][e >> 2][e & 3]); PIN(a.S[sb][e >> 2]); }
template <int BLK, int K> DI void sm_cvt(AS& a) { constexpr int b = BLK & 3, sub = (BLK >> 2) & 1, sb = (b == 1 || b == 2) ? 0 : 1, e = ((b & 1) ? 0 : 6) + 2 * K, pb = b == 0 ? sub ^ 1 : sub, rt = e >> 2;
  a.P[pb][rt][2 * sb + ((e & 3) >> 1)] = cvtpk(a.S[sb][rt][e & 3], a.S[sb][rt][(e & 3) + 1]); PIN(a.P[pb][rt]); }
template <int N, bool MM, bool SM, bool CY, int NX, bool Z, int G> DI void a_gap(AS& a) {
  constexpr int sub = N >> 2, b = N & 3; constexpr bool QK = (b & 1) == 0;
  if constexpr (MM) {
    if constexpr (QK) {
      constexpr int rt = G >> 2, ks = G & 3, sb = b >> 1;
      if constexpr (ks == 0) { if constexpr (Z) a.S[sb][rt] = MFMA16(a.F[N & 1][0], a.q[rt][0], f32x4{}); else a.S[sb][rt] = MFMA16(a.F[N & 1][0], a.q[rt][0], a.negm); }
      else a.S[sb][rt] = MFMA16(a.F[N & 1][ks], a.q[rt][ks], a.S[sb][rt]);
    } else if constexpr (G < 12) {
      constexpr int i = G / 3, rt = G % 3, cc = 4 * (b >> 1) + i;
      if constexpr (Z) a.o[rt][cc] = MFMA16(a.F[N & 1][i], __builtin_bit_cast(bf16x8, a.P[sub ^ 1][rt]), f32x4{});
      else a.o[rt][cc] = MFMA16(a.F[N & 1][i], __builtin_bit_cast(bf16x8, a.P[sub ^ 1][rt]), a.o[rt][cc]);
    } else {
      constexpr int rt = b == 1 ? G - 12 : 2;
      if constexpr (Z) a.l[rt] = MFMA16(a.ones, __builtin_bit_cast(bf16x8, a.P[sub ^ 1][rt]), f32x4{});
      else a.l[rt] = MFMA16(a.ones, __builtin_bit_cast(bf16x8, a.P[sub ^ 1][rt]), a.l[rt]);
    }
    SBAR();
  }
  constexpr int LAST = QK ? 11 : b == 1 ? 13 : 12;
  if constexpr (G == LAST) { if constexpr (QK) LGKM(4); else LGKM(0); }
  if constexpr (G == 5 && !QK) { if constexpr (NX >= 0) LGKM(3); else LGKM(0); }
  SBAR();
  if constexpr (NX >= 0 && (G & 1) == 0 && G < 8) a_frag<NX, G / 2>(a);
  SBAR();
  if constexpr ((G & 1) && G < 12) { if constexpr (SM) sm_exp<N, G / 2>(a); }
  else if constexpr (QK) {
    if constexpr (SM && G >= 8) sm_cvt<N, G / 2 - 4>(a);
  } else {
    if constexpr (G == 0 && CY) sm_cvt<N - 1, 2>(a);
    if constexpr (SM && G >= 8 && G <= 12 && (G & 1) == 0) sm_cvt<N, G / 2 - 4>(a);
  }
  SBAR();
}
template <int N, bool MM, bool SM, bool CY, int NX, bool Z, int... Gs> DI void a_block_(AS& a, iseq<Gs...>) { (a_gap<N, MM, SM, CY, NX, Z, Gs>(a), ...); }
template <int N, bool MM, bool SM, bool CY, int NX, bool Z = false> DI void a_block(AS& a) { a_block_<N, MM, SM, CY, NX, Z>(a, mkseq<(N & 1) == 0 ? 12 : (N & 3) == 1 ? 14 : 13>{}); }
template <bool FIRST> DI void a_period(AS& a) {
  if constexpr (FIRST) {
    a_block<0, true, false, false, -1, true>(a);
    float mx = 3.0e38f;
#pragma unroll
    for (int rt = 0; rt < 3; ++rt) mx = fminf(mx, red_max(fmaxf(fmaxf(a.S[0][rt][0], a.S[0][rt][1]), fmaxf(a.S[0][rt][2], a.S[0][rt][3]))));
    a.negm = f32x4{-mx, -mx, -mx, -mx}; PIN(a.negm);
#pragma unroll
    for (int rt = 0; rt < 3; ++rt) a.S[0][rt] -= mx;
    SBAR();
    a_block<1, false, true, false, 2>(a); a_block<2, true, true, true, -1>(a); a_block<3, false, true, true, 4>(a);
  } else {
    a_block<0, true, true, true, 1>(a); a_block<1, true, true, true, 2>(a); a_block<2, true, true, true, 3>(a); a_block<3, true, true, true, 4>(a);
  }
  a_block<4, true, true, true, 5>(a); a_block<5, true, true, true, 6, FIRST>(a); a_block<6, true, true, true, 7>(a);
  if constexpr (FIRST) VMW(0);
  BAR();
  a_block<7, true, true, true, 8, FIRST>(a);
}

DI void path_a(const Ctx& c, int wi) {
  STAMP(0);
  {
    const rsrc_t rx = mkrs(c.x, SQ * DM * 2);
    const unsigned lane = lane_id(), vox = 1280u * (lane >> 3) + 16u * ((lane & 7) ^ ((4 * (wi & 1) + (lane >> 4)) & 7));
    auto xp = [&](int s, unsigned stage, int n) { const int pc = n < 6 ? wi + 4 * n : 24 + (wi & 1); XDMA(c.ldsb + XOFF + stage + 1024 * pc, vox, rx, 128u * s + 10240u * pc); };
#pragma unroll
    for (int n = 0; n < 7; ++n) xp(0, 0, n);
#pragma unroll
    for (int n = 0; n < 7; ++n) xp(1, XST, n);
    unsigned xf = XST;
    qproj<7>(c, wi,
      [&](auto K, int s, int n) { if constexpr (decltype(K)::value == 0) xp(s + 2, xf, n); },
      [&](int s) { if (s == 9) VMW(0); else VMW(7); xf = xf == 2 * XST ? 0 : xf + XST; });
  }
  FST(11);
  BAR();
  FST(12);
  AS a;
  unsigned kb0, vb0;
  { const unsigned lane = lane_id(), r16 = lane & 15, g = lane >> 4, q4 = (lane & 15) >> 2, p = lane & 3;
    kb0 = c.ldsb + 128 * r16 + 16 * (g ^ ((r16 >> 1) & 7));
    vb0 = c.ldsb + SLOT + 2048 * (g >> 1) + 64 * (4 * (g & 1) + q4) + 16 * ((p >> 1) ^ (2 * (g & 1))) + 8 * (p & 1); }
  unsigned sl = 0;
  a.ones = bf16x8{0x3f80, 0x3f80, 0x3f80, 0x3f80, 0x3f80, 0x3f80, 0x3f80, 0x3f80}; PIN(a.ones);
#pragma nounroll
  for (int hd = 0; hd < 2; ++hd) {
#pragma unroll
    for (int rt = 0; rt < 3; ++rt) {
      const int t = 3 * wi + rt;
      const unsigned blk = hd ? Q2OFF + 4096 * t : (t < 8 ? 4 * SLOT + 4096 * t : t < 11 ? MISC + 4096 * (t - 8) : SLOT + 4096 * (t - 11));
      const unsigned q0 = kb0 + blk, q1 = q0 ^ 64;
      a.q[rt][0] = rd128(q0, 0); a.q[rt][1] = rd128(q1, 0); a.q[rt][2] = rd128(q0, 2048); a.q[rt][3] = rd128(q1, 2048);
      a.P[0][rt] = u32x4{}; a.P[1][rt] = u32x4{};
    }
    a.kn[0] = kb0 + sl; a.kn[1] = a.kn[0] ^ 64;
    a_frag<8, 0>(a); a_frag<8, 1>(a); a_frag<8, 2>(a); a_frag<8, 3>(a);
    if (hd == 0) BAR();
    STAMP(2 + 3 * hd);
    auto setp = [&]() {
      a.kp[0] = a.kn[0]; a.kp[1] = a.kn[1]; a.vp[0] = vb0 + sl; a.vp[1] = a.vp[0] ^ 32;
      sl = sl == 4 * SLOT ? 0 : sl + 2 * SLOT;
      a.kn[0] = kb0 + sl; a.kn[1] = a.kn[0] ^ 64;
      SBAR();
    };
    setp();
    a_period<true>(a);
#pragma nounroll
    for (int t = 1; t < 16; ++t) {
 if (wi == 0) PST_(16 * hd + t);
 setp(); a_period<false>(a); }
    a.vp[0] = vb0 + sl; a.vp[1] = a.vp[0] ^ 32;
    SBAR();
    a_block<0, false, true, true, 1>(a); a_block<1, true, false, true, -1>(a);
    a_frag<3, 0>(a); a_frag<3, 1>(a); a_frag<3, 2>(a); a_frag<3, 3>(a);
    a_block<3, true, false, false, -1>(a);
    if (hd) BAR();
    STAMP(3 + 3 * hd);
    const rsrc_t ro = mkrs(c.oq + (size_t)(2 * c.own + hd) * OBLK, OBLK);
    const unsigned ov = 16 * lane_id() + 6144 * wi;
#pragma unroll
    for (int rt = 0; rt < 3; ++rt) {
      float l = a.l[rt][0];
      if (__builtin_expect(__any(!(l + nanprobe(a.o[rt]) < L_LIMIT)), 0)) {
        float m;
        slow16(c.k + (size_t)hd * SK * DH, c.v + (size_t)hd * SK * DH, 0, 64, a.q[rt], a.o[rt], m, l);
        l = red_add(l);
      }
      const float il = __builtin_amdgcn_rcpf(l);
#pragma unroll
      for (int kk = 0; kk < 4; ++kk) {
        const f32x4 u = a.o[rt][2 * kk] * il, v = a.o[rt][2 * kk + 1] * il;
        u32x4 w = {cvtpk(u[0], u[1]), cvtpk(u[2], u[3]), cvtpk(v[0], v[1]), cvtpk(v[2], v[3])};
        __builtin_amdgcn_raw_buffer_store_b128(w, ro, ov + 2048 * rt + 1024 * (kk & 1) + (OBLK / 2) * (kk >> 1), 0, OAUX);
      }
    }
    STAMP(4 + 3 * hd);
  }
}

DI void path_b(const Ctx& c, int j) {
  STAMP(0);
  const rsrc_t rk = mkrs(c.k, 2 * SK * DH * 2), rv = mkrs(c.v, 2 * SK * DH * 2);
  unsigned vk0, vk1, vv, wm0 = 0, wm1 = 0;
  { const unsigned i = lane_id();
    vk0 = 256 * (i >> 3) + 16 * ((i & 7) ^ (i >> 4)); vk1 = (vk0 ^ 64) + 2048;
    vv = 256 * ((i >> 2) & 7) + 64 * (i >> 5) + 16 * ((i & 3) ^ (((i >> 4) & 1) << 1)); }
  auto kpc = [&](int t, unsigned slot, int n) {
    const unsigned m = c.ldsb + slot + 2048 * j, so = (unsigned)(64 * t + 16 * j) * 256u;
    if (n == 0) dma_nt<0>(m, vk0, rk, so); if (n == 1) dma_nt<128>(m + 8064, vk0, rk, so); if (n == 2) dma_nt<0>(m + 1024, vk1, rk, so); if (n == 3) dma_nt<128>(m + 9088, vk1, rk, so);
  };
  auto vpc = [&](int t, unsigned slot, int n) {
    const unsigned m = c.ldsb + slot + 4096 * j, so = (unsigned)(64 * t - 32 + 16 * j) * 256u;
    if (n == 0) dma_nt<0>(m, vv, rv, so); if (n == 1) dma_nt<128>(m + 896, vv, rv, so); if (n == 2) dma_nt<2048>(m, vv, rv, so); if (n == 3) dma_nt<2176>(m + 896, vv, rv, so);
  };
  auto kdma = [&](int t, unsigned slot) { kpc(t, slot, 0); kpc(t, slot, 1); kpc(t, slot, 2); kpc(t, slot, 3); };
  auto vdma = [&](int t, unsigned slot) { vpc(t, slot, 0); vpc(t, slot, 1); vpc(t, slot, 2); vpc(t, slot, 3); };
  {
    const rsrc_t rw = mkrs(c.wq, (DM - 1) * HD * 2 + 512);
    const unsigned i = lane_id(), vw0 = 8192 * ((i >> 2) & 7) + 64 * (i >> 5) + 16 * (i & 3), vw1 = (vw0 ^ 32) + 65536;
    auto wpc = [&](int it, unsigned slot, int n) {
      dma_df(c.ldsb + slot + 4096 * j + 1024 * n, n < 2 ? vw0 : vw1, rw, (unsigned)(64 * (it >> 1) + 16 * j) * 8192u + 256 * (it & 1) + 128 * (n & 1));
    };
    if (j == 3) touch_code(wm0, c.pc, c.idx);
#pragma nounroll
    for (int it = 0; it < 5; ++it) { wpc(it, it * SLOT, 0); wpc(it, it * SLOT, 1); wpc(it, it * SLOT, 2); wpc(it, it * SLOT, 3); }
    unsigned f0 = SLOT, f1 = 2 * SLOT;
    auto adv = [](unsigned s_) { s_ += 2 * SLOT; return s_ >= 5 * SLOT ? s_ - 5 * SLOT : s_; };
    qproj<6>(c, j,
      [&](auto K, int s, int n) {
        constexpr int k = decltype(K)::value;
        if constexpr (k == 0) { if (n < 4) wpc(2 * s + 3, f0, n); else wpc(2 * s + 4, f1, n - 4); }
        if constexpr (k == 1) { if (n < 4) wpc(19, 4 * SLOT, n); else kpc(0, 0, n - 4);
          if (n == 7) { PIN(wm0); touch80(wm0, wm1, c.warm + 10240 * j); } }
        if constexpr (k == 2) { if (n == 0) { if (j >= 2) vdma(0, SLOT); } else if (n >= 4) kpc(1, 2 * SLOT, n - 4); }
        if constexpr (k == 4) vdma(1, 3 * SLOT);
      },
      [&](int s) { if (s == 0) VMW(12); else if (s == 9) VMW(6); else VMW(4); f0 = adv(f0); f1 = adv(f1); });
  }
  VMW(8);
  asm volatile("" ::"v"(wm0), "v"(wm1));
  BAR();
  unsigned qb, vb0;
  { const unsigned lane = lane_id(), r16 = lane & 15, g = lane >> 4, q4 = (lane & 15) >> 2, p = lane & 3;
    qb = c.ldsb + 128 * r16 + 16 * (g ^ ((r16 >> 1) & 7));
    vb0 = c.ldsb + SLOT + 8192 * ((j & 1) ^ 1) + 2048 * (g >> 1) + 64 * (4 * (g & 1) + q4) + 16 * ((p >> 1) ^ (2 * (g & 1))) + 8 * (p & 1); }
  bf16x8 q[4], vf[8]; f32x4 o[8], os[8], negm = {}; float l = 0.f, ms = 0.f, ls = 0.f; u32x4 P = {};
  unsigned sl = 0, fr = 4 * SLOT, pq = 0;
  auto rdq = [&](unsigned blk) { const unsigned q0 = qb + blk, q1 = q0 ^ 64; q[0] = rd128(q0, 0); q[1] = rd128(q1, 0); q[2] = rd128(q0, 2048); q[3] = rd128(q1, 2048); };
  auto rdv = [&]() {
    const unsigned v0 = vb0 + sl, v1 = v0 ^ 32;
#pragma unroll
    for (int cc = 0; cc < 8; ++cc) vf[cc] = cat(rdtr((cc & 1) ? v1 : v0, 512 * (cc >> 1)), rdtr((cc & 1) ? v1 : v0, 512 * (cc >> 1) + 4096));
  };
  auto put = [&](unsigned scr) {
    const unsigned lane = lane_id(), sc = c.ldsb + scr + 16 * lane + 8192 * j;
#pragma unroll
    for (int cc = 0; cc < 8; ++cc) wr128f(sc, 1024 * cc, os[cc]);
    if (lane < 16) { *(LAS float*)(c.ldsb + scr + 32768 + 128 * j + 4 * lane) = ms; *(LAS float*)(c.ldsb + scr + 32768 + 128 * j + 64 + 4 * lane) = ls; }
  };
  auto merge = [&](int hd, unsigned scr) {
    const unsigned lane = lane_id(), sc = c.ldsb + scr + 16 * lane + 2048 * j, ml = c.ldsb + scr + 32768 + 4 * (lane & 15);
    float mw[4], lw[4];
#pragma unroll
    for (int w = 0; w < 4; ++w) { mw[w] = *(const LAS float*)(ml + 128 * w); lw[w] = *(const LAS float*)(ml + 128 * w + 64); }
    const float mt = fmaxf(fmaxf(mw[0], mw[1]), fmaxf(mw[2], mw[3]));
    float lt = 0.f;
#pragma unroll
    for (int w = 0; w < 4; ++w) { mw[w] = ex2(mw[w] - mt); PIN(mw[w]); lt = ffma_s(mw[w], lw[w], lt); }
    const float ir = __builtin_amdgcn_rcpf(lt);
    f32x4 u = {}, v = {};
#pragma unroll
    for (int w = 0; w < 4; ++w) {
      const f32x4 a = rd128f(sc, 8192 * w), b = rd128f(sc, 8192 * w + 1024); float f = mw[w] * ir; PIN(f);
#pragma unroll
      for (int i = 0; i < 4; ++i) { u[i] = ffma_s(a[i], f, u[i]); v[i] = ffma_s(b[i], f, v[i]); }
    }
    u32x4 w4 = {cvtpk(u[0], u[1]), cvtpk(u[2], u[3]), cvtpk(v[0], v[1]), cvtpk(v[2], v[3])};
    __builtin_amdgcn_raw_buffer_store_b128(w4, mkrs(c.oq + (size_t)(2 * c.own + hd) * OBLK, OBLK), 16 * lane + 12 * 2048 + 1024 * (j & 1) + (OBLK / 2) * (j >> 1), 0, OAUX);
  };
#pragma unroll
  for (int cc = 0; cc < 8; ++cc) { os[cc] = f32x4{}; vf[cc] = bf16x8{}; }
  int t = 0;
  unsigned long long ts = 0;
  auto next = [&]() {
    if (t < 18) VMW(8);
    else {
      if (t == 18) put(Q2OFF);
      if (t == 19) merge(0, Q2OFF);
      if (t == 21 && j == 0) signal_quad(c.cnt);
      if (t == 19 || (t == 21 && j == 0)) VMW(9);
      else if (t == 30) { if (j < 2) VMW(16); else VMW(12); }
      else if (t == 31) VMW(12);
      else VMW(8);
      if (t == 29 && j == 0) poll_acquire(pq, c.cnt);
    }
    if (j == 3 && t < 29) { while (__builtin_amdgcn_s_memrealtime() - ts < PACE) __builtin_amdgcn_s_sleep(1); }
    BAR();
    ts = __builtin_amdgcn_s_memrealtime();
    fr = sl; sl = sl == 4 * SLOT ? 0 : sl + 2 * SLOT; ++t;
    if (t < 28) { kdma(t + 2, fr); vdma(t + 2, fr + SLOT); }
    else {
      if (t == 28 && j == 3) touch_code(pq, c.pc, c.idx);
      if (t == 29 && j == 0) poll_issue(pq, c.cnt);
      if (t < 30) kdma(t + 2, fr);
      if (t < 30 || (t == 30 && j < 2)) vdma(t + 2, fr + SLOT);
      if (t == 30 || t == 32) {
        const OD d = od_make(c, j);
#pragma unroll
        for (int n = 0; n < 12; ++n) od_piece(c, d, t == 30 ? 0 : 1, t == 30 ? PS0 : PS1, n);
      }
    }
    PIN(pq);
  };
  rdq(q1addr(12));
  __builtin_amdgcn_s_setprio(BPRIO);
  BAR();
  kdma(2, fr); vdma(2, fr + SLOT);
  if (j >= 2) { VMW(8);
 BAR();
 fr = sl; sl = 2 * SLOT; t = 1; kdma(3, fr); vdma(3, fr + SLOT); }
#pragma nounroll
  for (int hd = 0; hd < 2; ++hd) {
    if (hd) rdq(Q2OFF + 12 * 4096);
#pragma unroll
    for (int cc = 0; cc < 8; ++cc) o[cc] = f32x4{};
    negm = f32x4{}; l = 0.f;
#pragma nounroll
    for (int u = 0; u < 8; ++u) {
      if ((j & 1) == 0) rdv();
      {
        const unsigned k0 = qb + sl + 4096 * (j & 1), k1 = k0 ^ 64;
        f32x4 S[2]; bf16x8 kf[2][4];
#pragma unroll
        for (int h = 0; h < 2; ++h) { kf[h][0] = rd128(k0, 2048 * h); kf[h][1] = rd128(k1, 2048 * h); kf[h][2] = rd128(k0, 2048 * h + 8192); kf[h][3] = rd128(k1, 2048 * h + 8192); }
        LGKM0(); SBAR();
        __builtin_amdgcn_s_setprio(3);
#pragma unroll
        for (int h = 0; h < 2; ++h) { S[h] = MFMA16(kf[h][0], q[0], negm); S[h] = MFMA16(kf[h][1], q[1], S[h]); S[h] = MFMA16(kf[h][2], q[2], S[h]); S[h] = MFMA16(kf[h][3], q[3], S[h]); }
        SBAR();
        __builtin_amdgcn_s_setprio(BPRIO);
        SBAR();
        if (u == 0) {
          const float mx = red_max(fmaxf(fmaxf(fmaxf(S[0][0], S[0][1]), fmaxf(S[0][2], S[0][3])), fmaxf(fmaxf(S[1][0], S[1][1]), fmaxf(S[1][2], S[1][3]))));
          negm = f32x4{-mx, -mx, -mx, -mx}; S[0] -= mx; S[1] -= mx;
        }
#pragma unroll
        for (int i = 0; i < 8; ++i) { S[i >> 2][i & 3] = ex2(S[i >> 2][i & 3]); l += S[i >> 2][i & 3]; PIN(l); }
        P = u32x4{cvtpk(S[0][0], S[0][1]), cvtpk(S[0][2], S[0][3]), cvtpk(S[1][0], S[1][1]), cvtpk(S[1][2], S[1][3])};
      }
      next();
      if (j & 1) rdv();
      LGKM0(); SBAR();
      __builtin_amdgcn_s_setprio(3);
#pragma unroll
      for (int cc = 0; cc < 8; ++cc) o[cc] = MFMA16(vf[cc], __builtin_bit_cast(bf16x8, P), o[cc]);
      SBAR();
      __builtin_amdgcn_s_setprio(BPRIO);
      SBAR();
      if (t == 32) break;
      next();
    }
    ls = red_add(l); ms = -negm[0];
    MFMA_DONE();
    if (__builtin_expect(__any(!(ls + nanprobe_s(o) < L_LIMIT)), 0)) {
      slow16(c.k + (size_t)hd * SK * DH, c.v + (size_t)hd * SK * DH, 32 * j, 16, q, o, ms, ls);
      ls = red_add(ls);
    }
#pragma unroll
    for (int cc = 0; cc < 8; ++cc) os[cc] = o[cc];
  }
  put(SCR2);
  BAR();
  merge(1, SCR2);
  __builtin_amdgcn_s_setprio(0);
}

__global__ __launch_bounds__(512, 2) void fused_k(Args ar) {
  extern __shared__ __attribute__((aligned(16))) char lds[];
  const int wid = __builtin_amdgcn_readfirstlane(threadIdx.x >> 6);
  const int xcd = blockIdx.x & 7, idx = blockIdx.x >> 3, br = xcd >> 2, kq = 2 * (xcd & 1) + ((idx >> 2) & 1), b = 4 * ((xcd >> 1) & 1) + (idx >> 3), hp = 4 * kq + (idx & 3);
  Ctx c;
  c.pc = __builtin_amdgcn_s_getpc() & ~255ull; c.idx = idx;
  c.ldsb = (unsigned)(size_t)(LAS char*)lds;
  c.x = (br ? ar.x1 : ar.x0) + (size_t)b * SQ * DM;
  c.k = (br ? ar.k1 : ar.k0) + ((size_t)b * H + 2 * hp) * SK * DH;
  c.v = (br ? ar.v1 : ar.v0) + ((size_t)b * H + 2 * hp) * SK * DH;
  c.wq = (br ? ar.wq1 : ar.wq0) + 2 * hp * DH;
  const char* wo = (const char*)(br ? ar.wo1 : ar.wo0);
  c.wo = wo + (size_t)kq * 1024 * DM * 2;
  c.warm = wo + (size_t)((xcd & 3) * 32 + idx) * 40960;
  c.oq = ar.ws + WS_O + (size_t)((br * NB + b) * H + 8 * kq) * OBLK;
  c.part = ar.ws + WS_PART + (size_t)((br * NB + b) * 16) * PARTB;
  c.out = ar.out + (size_t)b * SQ * (2 * DM) + br * DM;
  c.cnt2 = (unsigned*)(ar.ws + WS_CNT) + ((br * NB + b) * 4 + (hp & 3)) * 64 + 16; c.kq = kq;
  c.cnt = (unsigned*)(ar.ws + WS_CNT) + ((br * NB + b) * 4 + kq) * 64;
  c.stp = (unsigned long long*)(ar.ws + WS_STAMP) + (blockIdx.x * 8 + wid) * 32;
  c.own = hp & 3; c.nt = hp & 3;
  const unsigned long long tb = __builtin_amdgcn_s_memrealtime(); __builtin_amdgcn_s_waitcnt(0xC07F);
  if (wid < 4) path_a(c, wid); else path_b(c, wid - 4);
  if (wid < 4) { if (wid & 1) oproj<3, 6>(c, wid); else oproj<3, 7>(c, wid); }
  else { if (wid & 1) oproj<2, 7>(c, wid - 4); else oproj<2, 6>(c, wid - 4); }
  while (__builtin_amdgcn_s_memrealtime() - tb < FIXT) __builtin_amdgcn_s_sleep(2);
}

}

extern "C" void kernel_launch(void* const* d_in, const int* in_sizes, int n_in, void* d_out, int out_size, void* d_ws, size_t ws_size, hipStream_t stream) {
  using namespace kk;
  Args a;
  a.x0 = (const u16*)d_in[0]; a.k0 = (const u16*)d_in[1]; a.v0 = (const u16*)d_in[2]; a.wq0 = (const u16*)d_in[3]; a.wo0 = (const u16*)d_in[4];
  a.x1 = (const u16*)d_in[5]; a.k1 = (const u16*)d_in[6]; a.v1 = (const u16*)d_in[7]; a.wq1 = (const u16*)d_in[8]; a.wo1 = (const u16*)d_in[9];
  a.ws = (char*)d_ws; a.out = (u16*)d_out;
  (void)hipFuncSetAttribute((const void*)fused_k, hipFuncAttributeMaxDynamicSharedMemorySize, (int)LDS_TOTAL);
  fused_k<<<256, 512, LDS_TOTAL, stream>>>(a);
}
```

```cpp
#define PACE 118
#define MAP 2
#include <hip/hip_runtime.h>
#include <hip/hip_bf16.h>
#include <utility>

namespace kk {
typedef unsigned short u16;
typedef short bf16x8 __attribute__((ext_vector_type(8)));
typedef short s16x4 __attribute__((ext_vector_type(4)));
typedef float f32x4 __attribute__((ext_vector_type(4)));
typedef float f32x2 __attribute__((ext_vector_type(2)));
typedef __bf16 bf16x2 __attribute__((ext_vector_type(2)));
typedef unsigned u32x4 __attribute__((ext_vector_type(4)));
typedef unsigned u32x2 __attribute__((ext_vector_type(2)));
typedef __amdgpu_buffer_rsrc_t rsrc_t;
typedef __attribute__((address_space(1))) unsigned gu32;
#define LAS __attribute__((address_space(3)))
#define DI __device__ __forceinline__
#define SBAR() __builtin_amdgcn_sched_barrier(0)
#define PIN(x) asm volatile("" : "+v"(x))
#define MFMA16(a, b, c) __builtin_amdgcn_mfma_f32_16x16x32_bf16(a, b, c, 0, 0, 0)
#define MFMA16K16(a, b, c) __builtin_amdgcn_mfma_f32_16x16x16bf16_1k(a, b, c, 0, 0, 0)
#define LGKM0() __builtin_amdgcn_s_waitcnt(0xC07F)
#define VMW(N) asm volatile("s_waitcnt vmcnt(" #N ")" ::: "memory")
#define BAR() do { LGKM0(); asm volatile("s_barrier" ::: "memory"); } while (0)
#define MFMA_DONE() do { asm volatile("s_nop 7\n\ts_nop 3" ::: "memory"); SBAR(); } while (0)
template <int... Is> using iseq = std::integer_sequence<int, Is...>;
template <int N> using mkseq = std::make_integer_sequence<int, N>;
template <int N> using ic = std::integral_constant<int, N>;

constexpr int NB = 8, SQ = 208, DM = 640, H = 32, SK = 1024, DH = 128, HD = H * DH, NRT = 13;
constexpr unsigned SLOT = 16384, XST = 26624, XOFF = 5 * SLOT, Q2OFF = 6 * SLOT, MISC = Q2OFF + NRT * 4096, LDS_TOTAL = 163840;
static_assert(XOFF + 3 * XST <= LDS_TOTAL && MISC + 3 * 4096 == LDS_TOTAL, "lds map");
constexpr unsigned OBLK = NRT * 4096;
constexpr unsigned PST = 47104, POFF = 26624;
constexpr unsigned PS0 = LDS_TOTAL - PST, PS1 = 0, PS2 = PST, SCR2 = PST;
static_assert(SCR2 + 32768 + 512 <= 5 * SLOT && PS2 + PST <= PS0, "lds map 2");
constexpr size_t WS_CNT = 0, CNT_BYTES = 65536, WS_O = 1u << 20, WS_PART = 32u << 20, WS_STAMP = 64u << 20;
constexpr unsigned PARTB = 8 * 11264;
constexpr float QSCALE = 0.08838834764831845f * 1.4426950408889634f;
constexpr float L_LIMIT = 1.0e24f;
__host__ __device__ constexpr unsigned q1addr(int rt) { return rt < 8 ? 4 * SLOT + 4096 * rt : rt < 11 ? MISC + 4096 * (rt - 8) : SLOT + 4096 * (rt - 11); }

DI unsigned cvtpk(float lo, float hi) { f32x2 v = {lo, hi}; return __builtin_bit_cast(unsigned, __builtin_convertvector(v, bf16x2)); }
DI float fmul_s(float a, float b) { float r; asm("v_mul_f32_e32 %0, %1, %2" : "=v"(r) : "s"(b), "v"(a)); return r; }
DI float ffma_s(float a, float b, float c) { float r; asm("v_fma_f32 %0, %1, %2, %3" : "=v"(r) : "v"(a), "v"(b), "v"(c)); return r; }
DI float ex2(float x) { return __builtin_amdgcn_exp2f(x); }
DI bf16x8 rd128(unsigned base, int imm) { return *(const LAS bf16x8*)((const LAS char*)base + imm); }
DI s16x4 rdtr(unsigned base, int imm) { return __builtin_amdgcn_ds_read_tr16_b64_v4i16((LAS s16x4*)((LAS char*)base + imm)); }
DI bf16x8 cat(s16x4 lo, s16x4 hi) { return __builtin_shufflevector(lo, hi, 0, 1, 2, 3, 4, 5, 6, 7); }
DI void wr128f(unsigned base, int imm, f32x4 v) { *(LAS f32x4*)((LAS char*)base + imm) = v; }
DI f32x4 rd128f(unsigned base, int imm) { return *(const LAS f32x4*)((const LAS char*)base + imm); }
DI float red_add(float x) {
  auto a = __builtin_amdgcn_permlane16_swap(__float_as_uint(x), __float_as_uint(x), false, false); x = __uint_as_float(a[0]) + __uint_as_float(a[1]);
  auto b = __builtin_amdgcn_permlane32_swap(__float_as_uint(x), __float_as_uint(x), false, false); return __uint_as_float(b[0]) + __uint_as_float(b[1]); }
DI float red_max(float x) {
  auto a = __builtin_amdgcn_permlane16_swap(__float_as_uint(x), __float_as_uint(x), false, false); x = fmaxf(__uint_as_float(a[0]), __uint_as_float(a[1]));
  auto b = __builtin_amdgcn_permlane32_swap(__float_as_uint(x), __float_as_uint(x), false, false); return fmaxf(__uint_as_float(b[0]), __uint_as_float(b[1])); }
DI int lane_id() { int l; asm volatile("v_mbcnt_lo_u32_b32 %0, -1, 0\n\tv_mbcnt_hi_u32_b32 %0, -1, %0" : "=v"(l)); return l; }
DI rsrc_t mkrs(const void* p, unsigned bytes) { return __builtin_amdgcn_make_buffer_rsrc((void*)p, 0, bytes, 0x00020000); }

template <int IMM> DI void dma_nt(unsigned m0v, unsigned voff, rsrc_t rs, unsigned soff) {
  asm volatile("s_mov_b32 m0, %0\n\ts_nop 0\n\tbuffer_load_dwordx4 %1, %2, %3 offen offset:%4 nt lds" ::"s"(m0v), "v"(voff), "s"(rs), "s"(soff), "i"(IMM) : "memory");
}
DI void dma_df(unsigned m0v, unsigned voff, rsrc_t rs, unsigned soff) {
  asm volatile("s_mov_b32 m0, %0\n\ts_nop 0\n\tbuffer_load_dwordx4 %1, %2, %3 offen lds" ::"s"(m0v), "v"(voff), "s"(rs), "s"(soff) : "memory");
}
#define CODE_LINES 14
constexpr unsigned CODE_BYTES = CODE_LINES * 4096;
DI void touch_code(unsigned dump, unsigned long long pc, int idx) {
  const rsrc_t rs = __builtin_amdgcn_make_buffer_rsrc((void*)pc, 0, CODE_BYTES, 0x00020000);
  const unsigned l = lane_id(), vo = l < CODE_LINES ? 128 * (l + CODE_LINES * idx) : 0x80000000u;
  asm volatile("s_mov_b32 m0, %0\n\ts_nop 3\n\tbuffer_load_dword %1, %2, 0 offen lds" ::"s"(dump), "v"(vo), "s"(rs) : "memory");
}
DI void dma_sc1(unsigned m0v, unsigned voff, rsrc_t rs, unsigned soff) {
  asm volatile("s_mov_b32 m0, %0\n\ts_nop 0\n\tbuffer_load_dwordx4 %1, %2, %3 offen sc1 lds" ::"s"(m0v), "v"(voff), "s"(rs), "s"(soff) : "memory");
}
DI void touch80(unsigned& w0, unsigned& w1, const char* p) {
  const unsigned vo = 128 * lane_id();
  asm volatile("s_nop 4\n\tglobal_load_dword %0, %2, %3\n\tglobal_load_dword %1, %2, %3 offset:2048" : "=&v"(w0), "=&v"(w1) : "v"(vo), "s"(p) : "memory");
}
#define BPRIO 0
#define QLATE 0
#define OLATE 0
#define OAUX 16
#define XDMA dma_df
DI void mfma2(f32x4& c, const bf16x8& a0, const bf16x8& b0, const bf16x8& a1, const bf16x8& b1) {
  asm volatile("v_mfma_f32_16x16x32_bf16 %0, %1, %2, %0\n\tv_mfma_f32_16x16x32_bf16 %0, %3, %4, %0" : "+v"(c) : "v"(a0), "v"(b0), "v"(a1), "v"(b1));
}
template <bool SC1> DI void mfma2d(f32x4& c, const bf16x8& a0, const bf16x8& b0, const bf16x8& a1, const bf16x8& b1, unsigned m0v, unsigned voff, rsrc_t rs, unsigned soff) {
  if constexpr (SC1)
    asm volatile("v_mfma_f32_16x16x32_bf16 %0, %1, %2, %0\n\ts_mov_b32 m0, %5\n\tv_mfma_f32_16x16x32_bf16 %0, %3, %4, %0\n\tbuffer_load_dwordx4 %6, %7, %8 offen sc1 lds"
                 : "+v"(c) : "v"(a0), "v"(b0), "v"(a1), "v"(b1), "s"(m0v), "v"(voff), "s"(rs), "s"(soff) : "memory");
  else
    asm volatile("v_mfma_f32_16x16x32_bf16 %0, %1, %2, %0\n\ts_mov_b32 m0, %5\n\tv_mfma_f32_16x16x32_bf16 %0, %3, %4, %0\n\tbuffer_load_dwordx4 %6, %7, %8 offen lds"
                 : "+v"(c) : "v"(a0), "v"(b0), "v"(a1), "v"(b1), "s"(m0v), "v"(voff), "s"(rs), "s"(soff) : "memory");
}
DI void mfma1(f32x4& c, const bf16x8& a, const bf16x8& b) { asm volatile("v_mfma_f32_16x16x32_bf16 %0, %1, %2, %0" : "+v"(c) : "v"(a), "v"(b)); }
typedef unsigned long long u64;
constexpr int SPIN_MAX = 1 << 16;
constexpr u64 HS_K = 0x9E3779B97F4A7C15ull;
struct HS { char* base; unsigned in, out; u32x2 req, ack; };
DI u64 j64(u32x2 v) { return ((u64)v[1] << 32) | v[0]; }
DI void hs_load(u32x2& v, const HS& h, unsigned off) { asm volatile("s_nop 4\n\tbuffer_load_dwordx2 %0, %1, %2, 0 offen sc1" : "=v"(v) : "v"(off), "s"(mkrs(h.base, 65536)) : "memory"); }
DI u32x2 hs_load_now(const HS& h, unsigned off) { asm volatile("" ::: "memory"); const u32x2 v = __builtin_amdgcn_raw_buffer_load_b64(mkrs(h.base, 65536), off, 0, 16); __builtin_amdgcn_s_waitcnt(0x0F70); return v; }
DI void hs_store(const HS& h, unsigned off, u32x2 v) { __builtin_amdgcn_raw_buffer_store_b64(v, mkrs(h.base, 65536), off, 0, 16); }
DI unsigned hs_sel(unsigned off, int phase) { return (lane_id() >> 2) == phase ? off : 0x80000000u; }
DI void hs_signal(const HS& h, int phase) { hs_store(h, hs_sel(h.out, phase), h.ack); }
DI void hs_wait(const HS& h, int phase, u32x2 v) {
  asm volatile("" : "+v"(v)::"memory");
  const bool other = (lane_id() >> 2) != phase;
#pragma nounroll
  for (int spin = 0; spin < (1 << 22) && !__all(other || j64(v) == j64(h.req)); ++spin) { __builtin_amdgcn_s_sleep(1); v = hs_load_now(h, h.in); }
  asm volatile("" ::: "memory");
}

#define STAMP(i)
#define TS(x)
#define FST(i)
#define PST_(i)
struct Args { const u16 *x0, *k0, *v0, *wq0, *wo0, *x1, *k1, *v1, *wq1, *wo1; char* ws; u16* out; };
struct Ctx {
  const u16 *x, *k, *v, *wq; const char *wo, *warm; char *oq, *part; u16* out; char* hs; unsigned long long* stp; unsigned ldsb; int own, nt, kq, idx, gid; unsigned long long pc;
};

DI void slow16(const u16* Kg, const u16* Vg, int key0, int nstep, const bf16x8 (&q)[4], f32x4 (&o)[8], float& m, float& l) {
  const int lane = lane_id(), r = lane & 15, g = lane >> 4;
  m = -3.0e38f; l = 0.f;
#pragma unroll
  for (int d = 0; d < 8; ++d) o[d] = f32x4{};
#pragma nounroll
  for (int st = 0; st < nstep; ++st) {
    const int kb = nstep == 64 ? 16 * st : key0 + 128 * (st >> 1) + 16 * (st & 1);
    f32x4 S = {};
#pragma unroll
    for (int ks = 0; ks < 4; ++ks) S = MFMA16(*(const bf16x8*)(Kg + (size_t)(kb + r) * DH + 32 * ks + 8 * g), q[ks], S);
    const float mx = red_max(fmaxf(fmaxf(S[0], S[1]), fmaxf(S[2], S[3])));
    const float mn = fmaxf(m, mx), al = ex2(m - mn); m = mn;
    float ps = 0.f;
#pragma unroll
    for (int i = 0; i < 4; ++i) { S[i] = ex2(S[i] - mn); ps += S[i]; }
    l = l * al + ps;
    u32x2 pw = {cvtpk(S[0], S[1]), cvtpk(S[2], S[3])};
#pragma unroll
    for (int d = 0; d < 8; ++d) {
      s16x4 vf;
#pragma unroll
      for (int j = 0; j < 4; ++j) vf[j] = (short)Vg[(size_t)(kb + 4 * g + j) * DH + 16 * d + r];
      o[d] *= al;
      o[d] = MFMA16K16(vf, __builtin_bit_cast(s16x4, pw), o[d]);
    }
  }
  __builtin_amdgcn_s_waitcnt(0);
}
DI float nanprobe_s(const f32x4 (&o)[8]) {
  float t = 0.f; const float z = 0.f;
#pragma unroll
  for (int d = 0; d < 8; ++d) { t = ffma_s(o[d][0], z, t); t = ffma_s(o[d][1], z, t); t = ffma_s(o[d][2], z, t); t = ffma_s(o[d][3], z, t); }
  return t;
}
DI float nanprobe(const f32x4 (&o)[8]) {
  f32x4 t = {};
#pragma unroll
  for (int d = 0; d < 8; ++d) t += o[d] * 0.f;
  return (t[0] + t[1]) + (t[2] + t[3]);
}

template <int NR, class FD, class FW> DI void qproj(const Ctx& c, int cg, FD&& dma, FW&& wait) {
  f32x4 acc[NR][4]; bf16x8 w[4][2], x[NR][2];
  const unsigned lane = lane_id(), r16 = lane & 15, g = lane >> 4, q4 = (lane & 15) >> 2, p = lane & 3;
  const unsigned wb0 = c.ldsb + 1024 * (cg & 1) + 2048 * g + 64 * q4 + 16 * ((p >> 1) ^ (2 * (g & 1))) + 8 * (p & 1);
  const unsigned xb0 = c.ldsb + XOFF + (NR == 7 ? 0 : 7 * 2048) + 128 * r16 + 16 * (g ^ ((r16 >> 1) & 7));
#pragma unroll
  for (int r = 0; r < NR; ++r)
#pragma unroll
    for (int i = 0; i < 4; ++i) acc[r][i] = f32x4{};
  unsigned xs = 0, ws = SLOT * (cg >> 1), xp0, xp1, wp0, wp1;
  auto ldw = [&](int i) { const unsigned b = (i & 1) ? wp1 : wp0; const int im = 512 * (i >> 1);
    w[i][0] = cat(rdtr(b, im), rdtr(b, im + 256)); w[i][1] = cat(rdtr(b, im + 8192), rdtr(b, im + 8448)); };
  auto ldx = [&](int r) { x[r][0] = rd128(xp0, 2048 * r); x[r][1] = rd128(xp1, 2048 * r); };
  auto row = [&](int r) {
#pragma unroll
    for (int i = 0; i < 4; ++i) mfma2(acc[r][i], w[i][0], x[r][0], w[i][1], x[r][1]);
  };
  constexpr int LATE_R = QLATE;
  auto half = [&](int r, int h) { mfma2(acc[r][2 * h], w[2 * h][0], x[r][0], w[2 * h][1], x[r][1]); mfma2(acc[r][2 * h + 1], w[2 * h + 1][0], x[r][0], w[2 * h + 1][1], x[r][1]); };
  auto step = [&](auto K, auto F, int s) {
    constexpr bool FIRST = decltype(F)::value;
    xp0 = xb0 + xs; xp1 = xp0 ^ 64; wp0 = wb0 + ws; wp1 = wp0 ^ 32;
    xs = xs == 2 * XST ? 0 : xs + XST; ws += 2 * SLOT; ws = ws >= 5 * SLOT ? ws - 5 * SLOT : ws;
    wait(s);
    SBAR();
    BAR();
    if constexpr (FIRST) { STAMP(1); }
    SBAR();
    if constexpr (NR == 7) {
#pragma unroll
      for (int r = 0; r < 7; ++r) {
        if constexpr (!FIRST) half(r, 0);
        SBAR(); if (r) x[r - 1][0] = rd128(xp0, 2048 * (r - 1)); SBAR();
        if constexpr (!FIRST) half(r, 1);
        SBAR(); if (r) x[r - 1][1] = rd128(xp1, 2048 * (r - 1)); SBAR();
      }
      ldx(6);
#pragma unroll
      for (int i = 0; i < 4; ++i) { ldw(i); dma(K, s, 2 * i); if (i < 3) dma(K, s, 2 * i + 1); SBAR(); }
    } else {
      if constexpr (!FIRST) {
#pragma unroll
        for (int r = 6 - LATE_R; r < 6; ++r) row(r);
        __builtin_amdgcn_s_setprio(0);
        SBAR();
      }
#pragma unroll
      for (int i = 0; i < 4; ++i) { dma(K, s, i); ldw(i); SBAR(); }
#pragma unroll
      for (int r = 0; r < 6; ++r) { if (r < 4) dma(K, s, 4 + r); ldx(r); SBAR(); }
#pragma unroll
      for (int r = 0; r < 6 - LATE_R; ++r) row(r);
      __builtin_amdgcn_s_setprio(1);
      SBAR();
    }
  };
  asm volatile("s_nop 1" ::: "memory");
  step(ic<NR == 7 ? 0 : 3>{}, ic<1>{}, 0);
#pragma nounroll
  for (int s = 1; s < 8; ++s) step(ic<0>{}, ic<0>{}, s);
  step(ic<1>{}, ic<0>{}, 8);
  step(ic<2>{}, ic<0>{}, 9);
  BAR();
  if constexpr (NR == 6) {
#pragma unroll
    for (int r = 6 - LATE_R; r < 6; ++r) row(r);
    __builtin_amdgcn_s_setprio(0);
    SBAR();
  }
  dma(ic<4>{}, 10, 0);
  const unsigned ln = lane_id(), rr = ln & 15, gg = ln >> 4;
  const unsigned qw = c.ldsb + 2048 * (cg & 1) + 128 * rr + 16 * ((gg >> 1) ^ ((rr >> 1) & 7)) + 8 * (gg & 1);
  auto put = [&](int r) {
    const int rt = (NR == 7 ? 0 : 7) + r;
    const unsigned blk = (cg >> 1) ? Q2OFF + 4096 * rt : q1addr(rt);
#pragma unroll
    for (int i = 0; i < 4; ++i) {
      const f32x4 v = acc[r][i];
      *(LAS u32x2*)((qw ^ (32 * i)) + blk) = u32x2{cvtpk(fmul_s(v[0], QSCALE), fmul_s(v[1], QSCALE)), cvtpk(fmul_s(v[2], QSCALE), fmul_s(v[3], QSCALE))};
    }
  };
  SBAR();
  if constexpr (NR == 7) {
#pragma unroll
    for (int r = 0; r < 7; ++r) { row(r); SBAR(); if (r) put(r - 1); SBAR(); }
    MFMA_DONE();
    put(6);
  } else {
    MFMA_DONE();
#pragma unroll
    for (int r = 0; r < 6; ++r) put(r);
  }
}

struct OD { rsrc_t rw, ro; unsigned vwp, vwo, vo, mw, mo, lo; int rg; };
DI OD od_make(const Ctx& c, int rg) {
  OD d; const unsigned lane = lane_id(), xr = 16 * ((lane & 3) ^ (((lane >> 4) & 1) << 1));
  d.rw = mkrs(c.wo, 1024 * DM * 2); d.ro = mkrs(c.oq, 8 * OBLK);
  d.vwp = 1280 * (16 * rg + ((lane >> 2) & 7)) + 64 * (lane >> 5) + xr + 320 * c.nt + ((c.nt & 1) ? 64 : 0);
  d.vwo = 1280 * (16 * rg + (lane >> 2)) + xr + 320 * c.nt + ((c.nt & 1) ? 0 : 256);
  d.vo = 16 * lane; d.mw = c.ldsb + POFF + 5120 * rg; d.mo = c.ldsb + 1024 * rg; d.lo = 1024 * (24 + (rg & 1)); d.rg = rg;
  return d;
}
DI unsigned od_blk(const Ctx& c, int s) { return (unsigned)(s < 16 ? 4 * ((s >> 1) & 3) + 2 * (s >> 3) + (s & 1) : 64); }
template <class F> DI void od_piece(const Ctx& c, const OD& d, unsigned u, unsigned st, int n, F&& f) {
  const unsigned sw = u * (64 * 1280), so = u * (OBLK / 2), m = d.mw + st;
  if (n == 0) f(ic<0>{}, m, d.vwp, d.rw, sw);
  else if (n == 1) f(ic<0>{}, m + 1024, d.vwp, d.rw, sw + 128);
  else if (n == 2) f(ic<0>{}, m + 3072, d.vwp, d.rw, sw + 10240);
  else if (n == 3) f(ic<0>{}, m + 4096, d.vwp, d.rw, sw + 10240 + 128);
  else if (n == 4) f(ic<0>{}, m + 2048, d.vwo, d.rw, sw);
  else if (n < 11) f(ic<1>{}, d.mo + st + 4096 * (n - 5), d.vo, d.ro, so + 1024 * d.rg + 4096 * (n - 5));
  else f(ic<1>{}, c.ldsb + d.lo + st, d.vo, d.ro, so + d.lo);
}
DI void od_piece(const Ctx& c, const OD& d, unsigned u, unsigned st, int n) {
  od_piece(c, d, u, st, n, [](auto S, unsigned m, unsigned v, rsrc_t r, unsigned so) { if constexpr (decltype(S)::value) dma_sc1(m, v, r, so); else dma_df(m, v, r, so); });
}
template <int NC, int NR> DI void oproj(const Ctx& c, int rg, const HS& h) {
  const OD d = od_make(c, rg);
  f32x4 acc[NR][NC]; bf16x8 w[NC][2], x[NR][2];
  const int c0 = NC == 3 ? 3 * (rg >> 1) : 6 + 2 * (rg >> 1), r0 = NR == 7 ? 0 : 7;
  unsigned wb[NC], xb;
  {
    const unsigned lane = lane_id(), g = lane >> 4, q4 = (lane & 15) >> 2, p = lane & 3;
    const unsigned in = 64 * (4 * (g & 1) + q4) + 16 * ((p >> 1) ^ (2 * (g & 1))) + 8 * (p & 1);
#pragma unroll
    for (int i = 0; i < NC; ++i) {
      const int ct = c0 + i;
      const unsigned off = ct < 8 ? 512 * (ct >> 1) + 3072 * (g >> 1) : 2048 + 512 * (g >> 1);
      wb[i] = c.ldsb + POFF + off + (in ^ (32 * (ct & 1)));
    }
    xb = c.ldsb + 16 * lane + 2048 * r0;
  }
#pragma unroll
  for (int r = 0; r < NR; ++r)
#pragma unroll
    for (int i = 0; i < NC; ++i) acc[r][i] = f32x4{};
  unsigned st = PS0, nx = PS1, fr = PS2;
  auto ldw = [&](int i) { const unsigned wp = wb[i] + st; w[i][0] = cat(rdtr(wp, 0), rdtr(wp, 5120)); w[i][1] = cat(rdtr(wp, 10240), rdtr(wp, 15360)); };
  auto ldx = [&](int r) { const unsigned xp = xb + st; x[r][0] = rd128(xp, 2048 * r); x[r][1] = rd128(xp, 2048 * r + 1024); };
  auto row = [&](int r) {
#pragma unroll
    for (int i = 0; i < NC; ++i) mfma2(acc[r][i], w[i][0], x[r][0], w[i][1], x[r][1]);
  };
#pragma unroll
  for (int r = 0; r < NR; ++r) { x[r][0] = bf16x8{}; x[r][1] = bf16x8{}; }
#pragma unroll
  for (int i = 0; i < NC; ++i) { w[i][0] = bf16x8{}; w[i][1] = bf16x8{}; }
  u32x2 pv = {};
  unsigned long long f0 = 0, f1 = 0, f2 = 0, f3 = 0, tp = 0, t2 = 0; (void)f0; (void)f1; (void)f2; (void)f3; (void)tp; (void)t2;
  const bool me = NC == 2 && NR == 6 && rg == 0;
  STAMP(8);
  auto tile_d = [&](int r, int i, unsigned u, int n) {
    od_piece(c, d, u, fr, n, [&](auto S, unsigned m, unsigned v, rsrc_t rs, unsigned so) { mfma2d<decltype(S)::value != 0>(acc[r][i], w[i][0], x[r][0], w[i][1], x[r][1], m, v, rs, so); });
  };
  if constexpr (NC == 2) VMW(13);
#pragma nounroll
  for (int s = 0; s < 16; ++s) {
    const unsigned u = od_blk(c, s + 2), xp = xb + st;
    SBAR();
    BAR();
    if (NC == 3 && NR == 7 && rg == 2) PST_(s);
    SBAR();
    if constexpr (NC == 3) {
#pragma unroll
      for (int r = 0; r < NR; ++r) {
        if (NR == 6 && r == 0) tile_d(0, 0, u, 11); else mfma2(acc[r][0], w[0][0], x[r][0], w[0][1], x[r][1]);
        SBAR(); if (r) x[r - 1][0] = rd128(xp, 2048 * (r - 1)); SBAR();
        mfma2(acc[r][1], w[1][0], x[r][0], w[1][1], x[r][1]);
        SBAR(); if (r) x[r - 1][1] = rd128(xp, 2048 * (r - 1) + 1024); SBAR();
        tile_d(r, 2, u, r < 5 ? r : r + 5);
        SBAR();
      }
      ldx(NR - 1);
#pragma unroll
      for (int i = 0; i < 3; ++i) ldw(i);
      SBAR();
      VMW(7);
    } else {
#pragma unroll
      for (int r = NR - OLATE; r < NR; ++r) row(r);
      __builtin_amdgcn_s_setprio(0);
      SBAR();
      ldw(0); ldx(0); ldw(1);
#pragma unroll
      for (int r = 1; r < NR; ++r) ldx(r);
      SBAR();
      if constexpr (NR == 6) {
        if (me && s == 1) hs_signal(h, 1);
        if (me && s == 5) hs_load(pv, h, h.in);
        PIN(pv);
      }
#pragma unroll
      for (int r = 0; r < NR - OLATE; ++r) {
        if (r < 5) tile_d(r, 0, u, 5 + r); else mfma2(acc[r][0], w[0][0], x[r][0], w[0][1], x[r][1]);
        if (NR - OLATE < 5 && r == 0) tile_d(r, 1, u, 9); else mfma2(acc[r][1], w[1][0], x[r][0], w[1][1], x[r][1]);
        SBAR();
      }
      VMW(5);
      if constexpr (NR == 6) { if (me && s == 5) hs_wait(h, 1, pv); }
      __builtin_amdgcn_s_setprio(1);
    }
    const unsigned t = st; st = nx; nx = fr; fr = t;
  }
  STAMP(9);
  LGKM0(); SBAR();
#pragma unroll
  for (int r = NC == 3 ? 0 : NR - OLATE; r < NR; ++r) row(r);
  __builtin_amdgcn_s_setprio(0);
  MFMA_DONE();
  STAMP(10);
  constexpr int NT = NR * NC, NU = (NT + 1) / 2;
  const int wv = NC == 3 ? rg : 4 + rg;
  const unsigned ln = lane_id(), pv0 = 16 * ln + 11264 * wv;
  const rsrc_t rp = mkrs(c.part, 16 * PARTB);
  f32x4 own[3][2];
  auto scatter = [&](auto K) {
    constexpr int k = decltype(K)::value;
#pragma unroll
    for (int m = 0; m < NU; ++m) {
      const f32x4 u = acc[(2 * m) / NC][(2 * m) % NC], v = 2 * m + 1 < NT ? acc[(2 * m + 1) / NC][(2 * m + 1) % NC] : f32x4{};
      if ((m & 3) == k) { own[m >> 2][0] = u; own[m >> 2][1] = v; }
      else __builtin_amdgcn_raw_buffer_store_b128(u32x4{cvtpk(u[0], u[1]), cvtpk(u[2], u[3]), cvtpk(v[0], v[1]), cvtpk(v[2], v[3])}, rp, pv0 + (4 * k + c.nt) * PARTB + 1024 * m, 0, 16);
    }
#pragma unroll
    for (int e = 0; e < 3; ++e) if (4 * e + k >= NU) { own[e][0] = f32x4{}; own[e][1] = f32x4{}; }
  };
  if (c.kq == 0) scatter(ic<0>{}); else if (c.kq == 1) scatter(ic<1>{}); else if (c.kq == 2) scatter(ic<2>{}); else scatter(ic<3>{});
  VMW(0);
  BAR();
  if (me) { hs_signal(h, 2); hs_wait(h, 2, hs_load_now(h, h.in)); }
  BAR();
  u32x4 pp[3][3];
#pragma unroll
  for (int e = 0; e < 3; ++e)
#pragma unroll
    for (int q = 1; q < 4; ++q) {
      const int m = 4 * e + c.kq;
      pp[e][q - 1] = __builtin_amdgcn_raw_buffer_load_b128(rp, m < NU ? pv0 + (4 * ((c.kq + q) & 3) + c.nt) * PARTB + 1024 * m : 0x80000000u, 0, 16);
    }
#pragma unroll
  for (int e = 0; e < 3; ++e)
#pragma unroll
    for (int h = 0; h < 2; ++h) {
      const int n = 2 * (4 * e + c.kq) + h;
      f32x4 v = own[e][h];
#pragma unroll
      for (int q = 0; q < 3; ++q) {
        v[0] += __uint_as_float(pp[e][q][2 * h] << 16); v[1] += __uint_as_float(pp[e][q][2 * h] & 0xffff0000u);
        v[2] += __uint_as_float(pp[e][q][2 * h + 1] << 16); v[3] += __uint_as_float(pp[e][q][2 * h + 1] & 0xffff0000u);
      }
      if (n < NT) {
        const int rt = r0 + n / NC, ct = c0 + n % NC;
        const int col = 160 * c.nt + (ct < 8 ? ((c.nt & 1) ? 32 : 0) + 16 * ct : ((c.nt & 1) ? 0 : 128) + 16 * (ct - 8));
        *(u32x2*)(c.out + (size_t)(16 * rt + (ln & 15)) * (2 * DM) + col + 4 * (ln >> 4)) = u32x2{cvtpk(v[0], v[1]), cvtpk(v[2], v[3])};
      }
    }
  STAMP(11);
}

struct AS {
  f32x4 o[3][8], S[2][3], negm, l[3]; bf16x8 q[3][4], F[2][4], ones; u32x4 P[2][3];
  unsigned kp[2], vp[2], kn[2];
};
template <int N, int I> DI void a_frag(AS& a) {
  constexpr int sub = (N >> 2) & 1, b = N & 3;
  if constexpr ((b & 1) == 0) {
    constexpr int kt = 2 * sub + (b >> 1);
    a.F[N & 1][I] = rd128(N == 8 ? a.kn[I & 1] : a.kp[I & 1], 8192 * (I >> 1) + 2048 * kt);
  } else {
    constexpr int cc = 4 * (b >> 1) + I, imm = 8192 * sub + 512 * (cc >> 1);
    a.F[N & 1][I] = cat(rdtr(a.vp[cc & 1], imm), rdtr(a.vp[cc & 1], imm + 4096));
  }
}
#define LGKM(n) __builtin_amdgcn_s_waitcnt(0xC07F | ((n) << 8))
template <int BLK, int K> DI void sm_exp(AS& a) { constexpr int b = BLK & 3, sb = (b == 1 || b == 2) ? 0 : 1, e = ((b & 1) ? 0 : 6) + K;
  a.S[sb][e >> 2][e & 3] = ex2(a.S[sb][e >> 2][e & 3]); PIN(a.S[sb][e >> 2]); }
template <int BLK, int K> DI void sm_cvt(AS& a) { constexpr int b = BLK & 3, sub = (BLK >> 2) & 1, sb = (b == 1 || b == 2) ? 0 : 1, e = ((b & 1) ? 0 : 6) + 2 * K, pb = b == 0 ? sub ^ 1 : sub, rt = e >> 2;
  a.P[pb][rt][2 * sb + ((e & 3) >> 1)] = cvtpk(a.S[sb][rt][e & 3], a.S[sb][rt][(e & 3) + 1]); PIN(a.P[pb][rt]); }
template <int N, bool MM, bool SM, bool CY, int NX, bool Z, int G> DI void a_gap(AS& a) {
  constexpr int sub = N >> 2, b = N & 3; constexpr bool QK = (b & 1) == 0;
  if constexpr (MM) {
    if constexpr (QK) {
      constexpr int rt = G >> 2, ks = G & 3, sb = b >> 1;
      if constexpr (ks == 0) { if constexpr (Z) a.S[sb][rt] = MFMA16(a.F[N & 1][0], a.q[rt][0], f32x4{}); else a.S[sb][rt] = MFMA16(a.F[N & 1][0], a.q[rt][0], a.negm); }
      else a.S[sb][rt] = MFMA16(a.F[N & 1][ks], a.q[rt][ks], a.S[sb][rt]);
    } else if constexpr (G < 12) {
      constexpr int i = G / 3, rt = G % 3, cc = 4 * (b >> 1) + i;
      if constexpr (Z) a.o[rt][cc] = MFMA16(a.F[N & 1][i], __builtin_bit_cast(bf16x8, a.P[sub ^ 1][rt]), f32x4{});
      else a.o[rt][cc] = MFMA16(a.F[N & 1][i], __builtin_bit_cast(bf16x8, a.P[sub ^ 1][rt]), a.o[rt][cc]);
    } else {
      constexpr int rt = b == 1 ? G - 12 : 2;
      if constexpr (Z) a.l[rt] = MFMA16(a.ones, __builtin_bit_cast(bf16x8, a.P[sub ^ 1][rt]), f32x4{});
      else a.l[rt] = MFMA16(a.ones, __builtin_bit_cast(bf16x8, a.P[sub ^ 1][rt]), a.l[rt]);
    }
    SBAR();
  }
  constexpr int LAST = QK ? 11 : b == 1 ? 13 : 12;
  if constexpr (G == LAST) { if constexpr (QK) LGKM(4); else LGKM(0); }
  if constexpr (G == 5 && !QK) { if constexpr (NX >= 0) LGKM(3); else LGKM(0); }
  SBAR();
  if constexpr (NX >= 0 && (G & 1) == 0 && G < 8) a_frag<NX, G / 2>(a);
  SBAR();
  if constexpr ((G & 1) && G < 12) { if constexpr (SM) sm_exp<N, G / 2>(a); }
  else if constexpr (QK) {
    if constexpr (SM && G >= 8) sm_cvt<N, G / 2 - 4>(a);
  } else {
    if constexpr (G == 0 && CY) sm_cvt<N - 1, 2>(a);
    if constexpr (SM && G >= 8 && G <= 12 && (G & 1) == 0) sm_cvt<N, G / 2 - 4>(a);
  }
  SBAR();
}
template <int N, bool MM, bool SM, bool CY, int NX, bool Z, int... Gs> DI void a_block_(AS& a, iseq<Gs...>) { (a_gap<N, MM, SM, CY, NX, Z, Gs>(a), ...); }
template <int N, bool MM, bool SM, bool CY, int NX, bool Z = false> DI void a_block(AS& a) { a_block_<N, MM, SM, CY, NX, Z>(a, mkseq<(N & 1) == 0 ? 12 : (N & 3) == 1 ? 14 : 13>{}); }
template <bool FIRST> DI void a_period(AS& a) {
  if constexpr (FIRST) {
    a_block<0, true, false, false, -1, true>(a);
    float mx = 3.0e38f;
#pragma unroll
    for (int rt = 0; rt < 3; ++rt) mx = fminf(mx, red_max(fmaxf(fmaxf(a.S[0][rt][0], a.S[0][rt][1]), fmaxf(a.S[0][rt][2], a.S[0][rt][3]))));
    a.negm = f32x4{-mx, -mx, -mx, -mx}; PIN(a.negm);
#pragma unroll
    for (int rt = 0; rt < 3; ++rt) a.S[0][rt] -= mx;
    SBAR();
    a_block<1, false, true, false, 2>(a); a_block<2, true, true, true, -1>(a); a_block<3, false, true, true, 4>(a);
  } else {
    a_block<0, true, true, true, 1>(a); a_block<1, true, true, true, 2>(a); a_block<2, true, true, true, 3>(a); a_block<3, true, true, true, 4>(a);
  }
  a_block<4, true, true, true, 5>(a); a_block<5, true, true, true, 6, FIRST>(a); a_block<6, true, true, true, 7>(a);
  if constexpr (FIRST) VMW(0);
  BAR();
  a_block<7, true, true, true, 8, FIRST>(a);
}

DI void path_a(const Ctx& c, int wi) {
  STAMP(0);
  {
    const rsrc_t rx = mkrs(c.x, SQ * DM * 2);
    const unsigned lane = lane_id(), vox = 1280u * (lane >> 3) + 16u * ((lane & 7) ^ ((4 * (wi & 1) + (lane >> 4)) & 7));
    auto xp = [&](int s, unsigned stage, int n) { const int pc = n < 6 ? wi + 4 * n : 24 + (wi & 1); XDMA(c.ldsb + XOFF + stage + 1024 * pc, vox, rx, 128u * s + 10240u * pc); };
#pragma unroll
    for (int n = 0; n < 7; ++n) xp(0, 0, n);
#pragma unroll
    for (int n = 0; n < 7; ++n) xp(1, XST, n);
    unsigned xf = XST;
    qproj<7>(c, wi,
      [&](auto K, int s, int n) { if constexpr (decltype(K)::value == 0) xp(s + 2, xf, n); },
      [&](int s) { if (s == 9) VMW(0); else VMW(7); xf = xf == 2 * XST ? 0 : xf + XST; });
  }
  FST(11);
  BAR();
  FST(12);
  AS a;
  unsigned kb0, vb0;
  { const unsigned lane = lane_id(), r16 = lane & 15, g = lane >> 4, q4 = (lane & 15) >> 2, p = lane & 3;
    kb0 = c.ldsb + 128 * r16 + 16 * (g ^ ((r16 >> 1) & 7));
    vb0 = c.ldsb + SLOT + 2048 * (g >> 1) + 64 * (4 * (g & 1) + q4) + 16 * ((p >> 1) ^ (2 * (g & 1))) + 8 * (p & 1); }
  unsigned sl = 0;
  a.ones = bf16x8{0x3f80, 0x3f80, 0x3f80, 0x3f80, 0x3f80, 0x3f80, 0x3f80, 0x3f80}; PIN(a.ones);
#pragma nounroll
  for (int hd = 0; hd < 2; ++hd) {
#pragma unroll
    for (int rt = 0; rt < 3; ++rt) {
      const int t = 3 * wi + rt;
      const unsigned blk = hd ? Q2OFF + 4096 * t : (t < 8 ? 4 * SLOT + 4096 * t : t < 11 ? MISC + 4096 * (t - 8) : SLOT + 4096 * (t - 11));
      const unsigned q0 = kb0 + blk, q1 = q0 ^ 64;
      a.q[rt][0] = rd128(q0, 0); a.q[rt][1] = rd128(q1, 0); a.q[rt][2] = rd128(q0, 2048); a.q[rt][3] = rd128(q1, 2048);
      a.P[0][rt] = u32x4{}; a.P[1][rt] = u32x4{};
    }
    a.kn[0] = kb0 + sl; a.kn[1] = a.kn[0] ^ 64;
    a_frag<8, 0>(a); a_frag<8, 1>(a); a_frag<8, 2>(a); a_frag<8, 3>(a);
    if (hd == 0) BAR();
    STAMP(2 + 3 * hd);
    auto setp = [&]() {
      a.kp[0] = a.kn[0]; a.kp[1] = a.kn[1]; a.vp[0] = vb0 + sl; a.vp[1] = a.vp[0] ^ 32;
      sl = sl == 4 * SLOT ? 0 : sl + 2 * SLOT;
      a.kn[0] = kb0 + sl; a.kn[1] = a.kn[0] ^ 64;
      SBAR();
    };
    setp();
    a_period<true>(a);
#pragma nounroll
    for (int t = 1; t < 16; ++t) {
 if (wi == 0) PST_(16 * hd + t);
 setp(); a_period<false>(a); }
    a.vp[0] = vb0 + sl; a.vp[1] = a.vp[0] ^ 32;
    SBAR();
    a_block<0, false, true, true, 1>(a); a_block<1, true, false, true, -1>(a);
    a_frag<3, 0>(a); a_frag<3, 1>(a); a_frag<3, 2>(a); a_frag<3, 3>(a);
    a_block<3, true, false, false, -1>(a);
    if (hd) BAR();
    STAMP(3 + 3 * hd);
    const rsrc_t ro = mkrs(c.oq + (size_t)(2 * c.own + hd) * OBLK, OBLK);
    const unsigned ov = 16 * lane_id() + 6144 * wi;
#pragma unroll
    for (int rt = 0; rt < 3; ++rt) {
      float l = a.l[rt][0];
      if (__builtin_expect(__any(!(l + nanprobe(a.o[rt]) < L_LIMIT)), 0)) {
        float m;
        slow16(c.k + (size_t)hd * SK * DH, c.v + (size_t)hd * SK * DH, 0, 64, a.q[rt], a.o[rt], m, l);
        l = red_add(l);
      }
      const float il = __builtin_amdgcn_rcpf(l);
#pragma unroll
      for (int kk = 0; kk < 4; ++kk) {
        const f32x4 u = a.o[rt][2 * kk] * il, v = a.o[rt][2 * kk + 1] * il;
        u32x4 w = {cvtpk(u[0], u[1]), cvtpk(u[2], u[3]), cvtpk(v[0], v[1]), cvtpk(v[2], v[3])};
        __builtin_amdgcn_raw_buffer_store_b128(w, ro, ov + 2048 * rt + 1024 * (kk & 1) + (OBLK / 2) * (kk >> 1), 0, OAUX);
      }
    }
    STAMP(4 + 3 * hd);
  }
}

DI void path_b(const Ctx& c, int j, HS& h) {
  STAMP(0);
  { const unsigned l = lane_id(), ph = l >> 2, i = l & 3;
    h.in = l < 12 ? 256 * c.gid + 8 * l : 0x80000000u;
    h.out = l < 12 ? (ph < 2 ? 256 * ((c.gid & ~3) | i) + 8 * (4 * ph + c.own) : 256 * ((c.gid & ~12) | (i << 2)) + 8 * (8 + c.kq)) : 0x80000000u;
    h.req = u32x2{}; h.ack = u32x2{};
    if (j == 0) hs_load(h.req, h, h.in); }
  const rsrc_t rk = mkrs(c.k, 2 * SK * DH * 2), rv = mkrs(c.v, 2 * SK * DH * 2);
  unsigned vk0, vk1, vv, wm0 = 0, wm1 = 0;
  { const unsigned i = lane_id();
    vk0 = 256 * (i >> 3) + 16 * ((i & 7) ^ (i >> 4)); vk1 = (vk0 ^ 64) + 2048;
    vv = 256 * ((i >> 2) & 7) + 64 * (i >> 5) + 16 * ((i & 3) ^ (((i >> 4) & 1) << 1)); }
  auto kpc = [&](int t, unsigned slot, int n) {
    const unsigned m = c.ldsb + slot + 2048 * j, so = (unsigned)(64 * t + 16 * j) * 256u;
    if (n == 0) dma_nt<0>(m, vk0, rk, so); if (n == 1) dma_nt<128>(m + 8064, vk0, rk, so); if (n == 2) dma_nt<0>(m + 1024, vk1, rk, so); if (n == 3) dma_nt<128>(m + 9088, vk1, rk, so);
  };
  auto vpc = [&](int t, unsigned slot, int n) {
    const unsigned m = c.ldsb + slot + 4096 * j, so = (unsigned)(64 * t - 32 + 16 * j) * 256u;
    if (n == 0) dma_nt<0>(m, vv, rv, so); if (n == 1) dma_nt<128>(m + 896, vv, rv, so); if (n == 2) dma_nt<2048>(m, vv, rv, so); if (n == 3) dma_nt<2176>(m + 896, vv, rv, so);
  };
  auto kdma = [&](int t, unsigned slot) { kpc(t, slot, 0); kpc(t, slot, 1); kpc(t, slot, 2); kpc(t, slot, 3); };
  auto vdma = [&](int t, unsigned slot) { vpc(t, slot, 0); vpc(t, slot, 1); vpc(t, slot, 2); vpc(t, slot, 3); };
  {
    const rsrc_t rw = mkrs(c.wq, (DM - 1) * HD * 2 + 512);
    const unsigned i = lane_id(), vw0 = 8192 * ((i >> 2) & 7) + 64 * (i >> 5) + 16 * (i & 3), vw1 = (vw0 ^ 32) + 65536;
    auto wpc = [&](int it, unsigned slot, int n) {
      dma_df(c.ldsb + slot + 4096 * j + 1024 * n, n < 2 ? vw0 : vw1, rw, (unsigned)(64 * (it >> 1) + 16 * j) * 8192u + 256 * (it & 1) + 128 * (n & 1));
    };
    if (j == 3) touch_code(c.ldsb + XOFF + 3 * XST, c.pc, c.idx);
#pragma nounroll
    for (int it = 0; it < 5; ++it) { wpc(it, it * SLOT, 0); wpc(it, it * SLOT, 1); wpc(it, it * SLOT, 2); wpc(it, it * SLOT, 3); }
    unsigned f0 = SLOT, f1 = 2 * SLOT;
    auto adv = [](unsigned s_) { s_ += 2 * SLOT; return s_ >= 5 * SLOT ? s_ - 5 * SLOT : s_; };
    qproj<6>(c, j,
      [&](auto K, int s, int n) {
        constexpr int k = decltype(K)::value;
        if constexpr (k == 0) { if (n < 4) wpc(2 * s + 3, f0, n); else wpc(2 * s + 4, f1, n - 4); }
        if constexpr (k == 1) { if (n < 4) wpc(19, 4 * SLOT, n); else kpc(0, 0, n - 4);
          if (n == 7) { touch80(wm0, wm1, c.warm + 10240 * j); } }
        if constexpr (k == 2) { if (n == 0) { if (j >= 2) vdma(0, SLOT); } else if (n >= 4) kpc(1, 2 * SLOT, n - 4); }
        if constexpr (k == 3) { if (n == 0 && j == 0) { PIN(h.req); const u64 r = j64(h.req) + HS_K; h.req = u32x2{(unsigned)r, (unsigned)(r >> 32)}; hs_store(h, h.in + 128, h.req); } }
        if constexpr (k == 4) vdma(1, 3 * SLOT);
      },
      [&](int s) { if (s == 0) VMW(12); else if (s == 9) VMW(6); else VMW(4); f0 = adv(f0); f1 = adv(f1); });
  }
  if (j == 0) {
    const bool idle = lane_id() >= 12;
#pragma nounroll
    for (int spin = 0; spin < SPIN_MAX; ++spin) {
      asm volatile("" ::: "memory");
      const u32x2 a0 = __builtin_amdgcn_raw_buffer_load_b64(mkrs(h.base, 65536), h.out, 0, 16);
      h.ack = hs_load_now(h, h.out + 128);
      if (__all(idle || j64(h.ack) == j64(a0) + HS_K)) break;
      __builtin_amdgcn_s_sleep(1);
    }
  }
  VMW(8);
  asm volatile("" ::"v"(wm0), "v"(wm1));
  BAR();
  unsigned qb, vb0;
  { const unsigned lane = lane_id(), r16 = lane & 15, g = lane >> 4, q4 = (lane & 15) >> 2, p = lane & 3;
    qb = c.ldsb + 128 * r16 + 16 * (g ^ ((r16 >> 1) & 7));
    vb0 = c.ldsb + SLOT + 8192 * ((j & 1) ^ 1) + 2048 * (g >> 1) + 64 * (4 * (g & 1) + q4) + 16 * ((p >> 1) ^ (2 * (g & 1))) + 8 * (p & 1); }
  bf16x8 q[4], vf[8]; f32x4 o[8], os[8], negm = {}; float l = 0.f, ms = 0.f, ls = 0.f; u32x4 P = {};
  unsigned sl = 0, fr = 4 * SLOT;
  auto rdq = [&](unsigned blk) { const unsigned q0 = qb + blk, q1 = q0 ^ 64; q[0] = rd128(q0, 0); q[1] = rd128(q1, 0); q[2] = rd128(q0, 2048); q[3] = rd128(q1, 2048); };
  auto rdv = [&]() {
    const unsigned v0 = vb0 + sl, v1 = v0 ^ 32;
#pragma unroll
    for (int cc = 0; cc < 8; ++cc) vf[cc] = cat(rdtr((cc & 1) ? v1 : v0, 512 * (cc >> 1)), rdtr((cc & 1) ? v1 : v0, 512 * (cc >> 1) + 4096));
  };
  auto put = [&](unsigned scr) {
    const unsigned lane = lane_id(), sc = c.ldsb + scr + 16 * lane + 8192 * j;
#pragma unroll
    for (int cc = 0; cc < 8; ++cc) wr128f(sc, 1024 * cc, os[cc]);
    if (lane < 16) { *(LAS float*)(c.ldsb + scr + 32768 + 128 * j + 4 * lane) = ms; *(LAS float*)(c.ldsb + scr + 32768 + 128 * j + 64 + 4 * lane) = ls; }
  };
  auto merge = [&](int hd, unsigned scr) {
    const unsigned lane = lane_id(), sc = c.ldsb + scr + 16 * lane + 2048 * j, ml = c.ldsb + scr + 32768 + 4 * (lane & 15);
    float mw[4], lw[4];
#pragma unroll
    for (int w = 0; w < 4; ++w) { mw[w] = *(const LAS float*)(ml + 128 * w); lw[w] = *(const LAS float*)(ml + 128 * w + 64); }
    const float mt = fmaxf(fmaxf(mw[0], mw[1]), fmaxf(mw[2], mw[3]));
    float lt = 0.f;
#pragma unroll
    for (int w = 0; w < 4; ++w) { mw[w] = ex2(mw[w] - mt); PIN(mw[w]); lt = ffma_s(mw[w], lw[w], lt); }
    const float ir = __builtin_amdgcn_rcpf(lt);
    f32x4 u = {}, v = {};
#pragma unroll
    for (int w = 0; w < 4; ++w) {
      const f32x4 a = rd128f(sc, 8192 * w), b = rd128f(sc, 8192 * w + 1024); float f = mw[w] * ir; PIN(f);
#pragma unroll
      for (int i = 0; i < 4; ++i) { u[i] = ffma_s(a[i], f, u[i]); v[i] = ffma_s(b[i], f, v[i]); }
    }
    u32x4 w4 = {cvtpk(u[0], u[1]), cvtpk(u[2], u[3]), cvtpk(v[0], v[1]), cvtpk(v[2], v[3])};
    __builtin_amdgcn_raw_buffer_store_b128(w4, mkrs(c.oq + (size_t)(2 * c.own + hd) * OBLK, OBLK), 16 * lane + 12 * 2048 + 1024 * (j & 1) + (OBLK / 2) * (j >> 1), 0, OAUX);
  };
#pragma unroll
  for (int cc = 0; cc < 8; ++cc) { os[cc] = f32x4{}; vf[cc] = bf16x8{}; }
  int t = 0;
  unsigned long long ts = 0;
  auto next = [&]() {
    if (t < 18) VMW(8);
    else {
      if (t == 18) put(Q2OFF);
      if (t == 19) merge(0, Q2OFF);
      if (t == 21 && j == 0) hs_signal(h, 0);
      if (t == 19 || (t == 21 && j == 0)) VMW(9);
      else if (t == 30) { if (j < 2) VMW(16); else VMW(12); }
      else if (t == 31) VMW(12);
      else VMW(8);
    }
    if (j == 3 && t < 29) { while (__builtin_amdgcn_s_memrealtime() - ts < PACE) __builtin_amdgcn_s_sleep(1); }
    BAR();
    ts = __builtin_amdgcn_s_memrealtime();
    fr = sl; sl = sl == 4 * SLOT ? 0 : sl + 2 * SLOT; ++t;
    if (t < 28) { kdma(t + 2, fr); vdma(t + 2, fr + SLOT); }
    else {
      if (t == 28 && j == 3) touch_code(c.ldsb + Q2OFF, c.pc, c.idx);
      if (t == 29 && j == 0) hs_wait(h, 0, hs_load_now(h, h.in));
      if (t < 30) kdma(t + 2, fr);
      if (t < 30 || (t == 30 && j < 2)) vdma(t + 2, fr + SLOT);
      if (t == 30 || t == 32) {
        const OD d = od_make(c, j);
#pragma unroll
        for (int n = 0; n < 12; ++n) od_piece(c, d, t == 30 ? 0 : 1, t == 30 ? PS0 : PS1, n);
      }
    }
  };
  rdq(q1addr(12));
  __builtin_amdgcn_s_setprio(BPRIO);
  BAR();
  kdma(2, fr); vdma(2, fr + SLOT);
  if (j >= 2) { VMW(8);
 BAR();
 fr = sl; sl = 2 * SLOT; t = 1; kdma(3, fr); vdma(3, fr + SLOT); }
#pragma nounroll
  for (int hd = 0; hd < 2; ++hd) {
    if (hd) rdq(Q2OFF + 12 * 4096);
#pragma unroll
    for (int cc = 0; cc < 8; ++cc) o[cc] = f32x4{};
    negm = f32x4{}; l = 0.f;
#pragma nounroll
    for (int u = 0; u < 8; ++u) {
      if ((j & 1) == 0) rdv();
      {
        const unsigned k0 = qb + sl + 4096 * (j & 1), k1 = k0 ^ 64;
        f32x4 S[2]; bf16x8 kf[2][4];
#pragma unroll
        for (int h = 0; h < 2; ++h) { kf[h][0] = rd128(k0, 2048 * h); kf[h][1] = rd128(k1, 2048 * h); kf[h][2] = rd128(k0, 2048 * h + 8192); kf[h][3] = rd128(k1, 2048 * h + 8192); }
        LGKM0(); SBAR();
        __builtin_amdgcn_s_setprio(3);
#pragma unroll
        for (int h = 0; h < 2; ++h) { S[h] = MFMA16(kf[h][0], q[0], negm); S[h] = MFMA16(kf[h][1], q[1], S[h]); S[h] = MFMA16(kf[h][2], q[2], S[h]); S[h] = MFMA16(kf[h][3], q[3], S[h]); }
        SBAR();
        __builtin_amdgcn_s_setprio(BPRIO);
        SBAR();
        if (u == 0) {
          const float mx = red_max(fmaxf(fmaxf(fmaxf(S[0][0], S[0][1]), fmaxf(S[0][2], S[0][3])), fmaxf(fmaxf(S[1][0], S[1][1]), fmaxf(S[1][2], S[1][3]))));
          negm = f32x4{-mx, -mx, -mx, -mx}; S[0] -= mx; S[1] -= mx;
        }
#pragma unroll
        for (int i = 0; i < 8; ++i) { S[i >> 2][i & 3] = ex2(S[i >> 2][i & 3]); l += S[i >> 2][i & 3]; PIN(l); }
        P = u32x4{cvtpk(S[0][0], S[0][1]), cvtpk(S[0][2], S[0][3]), cvtpk(S[1][0], S[1][1]), cvtpk(S[1][2], S[1][3])};
      }
      next();
      if (j & 1) rdv();
      LGKM0(); SBAR();
      __builtin_amdgcn_s_setprio(3);
#pragma unroll
      for (int cc = 0; cc < 8; ++cc) o[cc] = MFMA16(vf[cc], __builtin_bit_cast(bf16x8, P), o[cc]);
      SBAR();
      __builtin_amdgcn_s_setprio(BPRIO);
      SBAR();
      if (t == 32) break;
      next();
    }
    ls = red_add(l); ms = -negm[0];
    MFMA_DONE();
    if (__builtin_expect(__any(!(ls + nanprobe_s(o) < L_LIMIT)), 0)) {
      slow16(c.k + (size_t)hd * SK * DH, c.v + (size_t)hd * SK * DH, 32 * j, 16, q, o, ms, ls);
      ls = red_add(ls);
    }
#pragma unroll
    for (int cc = 0; cc < 8; ++cc) os[cc] = o[cc];
  }
  put(SCR2);
  BAR();
  merge(1, SCR2);
  __builtin_amdgcn_s_setprio(0);
}

__global__ __launch_bounds__(512, 2) void fused_k(Args ar) {
  extern __shared__ __attribute__((aligned(16))) char lds[];
  const int wid = __builtin_amdgcn_readfirstlane(threadIdx.x >> 6);
  const int xcd = blockIdx.x & 7, idx = blockIdx.x >> 3, br = xcd >> 2, kq = 2 * (xcd & 1) + ((idx >> 2) & 1), b = 4 * ((xcd >> 1) & 1) + (idx >> 3), hp = 4 * kq + (idx & 3);
  Ctx c;
  c.pc = __builtin_amdgcn_s_getpc() & ~255ull; c.idx = idx;
  c.ldsb = (unsigned)(size_t)(LAS char*)lds;
  c.x = (br ? ar.x1 : ar.x0) + (size_t)b * SQ * DM;
  c.k = (br ? ar.k1 : ar.k0) + ((size_t)b * H + 2 * hp) * SK * DH;
  c.v = (br ? ar.v1 : ar.v0) + ((size_t)b * H + 2 * hp) * SK * DH;
  c.wq = (br ? ar.wq1 : ar.wq0) + 2 * hp * DH;
  const char* wo = (const char*)(br ? ar.wo1 : ar.wo0);
  c.wo = wo + (size_t)kq * 1024 * DM * 2;
  c.warm = wo + (size_t)((xcd & 3) * 32 + idx) * 40960;
  c.oq = ar.ws + WS_O + (size_t)((br * NB + b) * H + 8 * kq) * OBLK;
  c.part = ar.ws + WS_PART + (size_t)((br * NB + b) * 16) * PARTB;
  c.out = ar.out + (size_t)b * SQ * (2 * DM) + br * DM;
  c.kq = kq; c.hs = ar.ws + WS_CNT; c.gid = (br * NB + b) * 16 + hp;
  c.stp = (unsigned long long*)(ar.ws + WS_STAMP) + (blockIdx.x * 8 + wid) * 32;
  c.own = hp & 3; c.nt = hp & 3;
  HS h = {}; h.base = c.hs;
  if (wid < 4) path_a(c, wid); else path_b(c, wid - 4, h);
  if (wid < 4) { if (wid & 1) oproj<3, 6>(c, wid, h); else oproj<3, 7>(c, wid, h); }
  else { if (wid & 1) oproj<2, 7>(c, wid - 4, h); else oproj<2, 6>(c, wid - 4, h); }
}

}

extern "C" void kernel_launch(void* const* d_in, const int* in_sizes, int n_in, void* d_out, int out_size, void* d_ws, size_t ws_size, hipStream_t stream) {
  using namespace kk;
  Args a;
  a.x0 = (const u16*)d_in[0]; a.k0 = (const u16*)d_in[1]; a.v0 = (const u16*)d_in[2]; a.wq0 = (const u16*)d_in[3]; a.wo0 = (const u16*)d_in[4];
  a.x1 = (const u16*)d_in[5]; a.k1 = (const u16*)d_in[6]; a.v1 = (const u16*)d_in[7]; a.wq1 = (const u16*)d_in[8]; a.wo1 = (const u16*)d_in[9];
  a.ws = (char*)d_ws; a.out = (u16*)d_out;
  (void)hipFuncSetAttribute((const void*)fused_k, hipFuncAttributeMaxDynamicSharedMemorySize, (int)LDS_TOTAL);
  fused_k<<<256, 512, LDS_TOTAL, stream>>>(a);
}
```

```cpp
#define PACE 126
#define MAP 2
#define FIXT 13000
#define OUTAUX 17
#include <hip/hip_runtime.h>
#include <hip/hip_bf16.h>
#include <utility>

namespace kk {
typedef unsigned short u16;
typedef short bf16x8 __attribute__((ext_vector_type(8)));
typedef short s16x4 __attribute__((ext_vector_type(4)));
typedef float f32x4 __attribute__((ext_vector_type(4)));
typedef float f32x2 __attribute__((ext_vector_type(2)));
typedef __bf16 bf16x2 __attribute__((ext_vector_type(2)));
typedef unsigned u32x4 __attribute__((ext_vector_type(4)));
typedef unsigned u32x2 __attribute__((ext_vector_type(2)));
typedef __amdgpu_buffer_rsrc_t rsrc_t;
typedef __attribute__((address_space(1))) unsigned gu32;
#define LAS __attribute__((address_space(3)))
#define DI __device__ __forceinline__
#define SBAR() __builtin_amdgcn_sched_barrier(0)
#define PIN(x) asm volatile("" : "+v"(x))
#define MFMA16(a, b, c) __builtin_amdgcn_mfma_f32_16x16x32_bf16(a, b, c, 0, 0, 0)
#define MFMA16K16(a, b, c) __builtin_amdgcn_mfma_f32_16x16x16bf16_1k(a, b, c, 0, 0, 0)
#define LGKM0() __builtin_amdgcn_s_waitcnt(0xC07F)
#define VMW(N) asm volatile("s_waitcnt vmcnt(" #N ")" ::: "memory")
#define BAR() do { LGKM0(); asm volatile("s_barrier" ::: "memory"); } while (0)
#define MFMA_DONE() do { asm volatile("s_nop 7\n\ts_nop 3" ::: "memory"); SBAR(); } while (0)
template <int... Is> using iseq = std::integer_sequence<int, Is...>;
template <int N> using mkseq = std::make_integer_sequence<int, N>;
template <int N> using ic = std::integral_constant<int, N>;

constexpr int NB = 8, SQ = 208, DM = 640, H = 32, SK = 1024, DH = 128, HD = H * DH, NRT = 13;
constexpr unsigned SLOT = 16384, XST = 26624, XOFF = 5 * SLOT, Q2OFF = 6 * SLOT, MISC = Q2OFF + NRT * 4096, LDS_TOTAL = 163840;
static_assert(XOFF + 3 * XST <= LDS_TOTAL && MISC + 3 * 4096 == LDS_TOTAL, "lds map");
constexpr unsigned OBLK = NRT * 4096;
constexpr unsigned PST = 47104, POFF = 26624;
constexpr unsigned PS0 = LDS_TOTAL - PST, PS1 = 0, PS2 = PST, SCR2 = PST;
static_assert(SCR2 + 32768 + 512 <= 5 * SLOT && PS2 + PST <= PS0, "lds map 2");
constexpr size_t WS_CNT = 0, CNT_BYTES = 65536, WS_O = 1u << 20, WS_PART = 32u << 20, WS_STAMP = 64u << 20;
constexpr unsigned PARTB = 8 * 11264;
constexpr float QSCALE = 0.08838834764831845f * 1.4426950408889634f;
constexpr float L_LIMIT = 1.0e24f;
__host__ __device__ constexpr unsigned q1addr(int rt) { return rt < 8 ? 4 * SLOT + 4096 * rt : rt < 11 ? MISC + 4096 * (rt - 8) : SLOT + 4096 * (rt - 11); }

DI unsigned cvtpk(float lo, float hi) { f32x2 v = {lo, hi}; return __builtin_bit_cast(unsigned, __builtin_convertvector(v, bf16x2)); }
DI float fmul_s(float a, float b) { float r; asm("v_mul_f32_e32 %0, %1, %2" : "=v"(r) : "s"(b), "v"(a)); return r; }
DI float ffma_s(float a, float b, float c) { float r; asm("v_fma_f32 %0, %1, %2, %3" : "=v"(r) : "v"(a), "v"(b), "v"(c)); return r; }
DI float ex2(float x) { return __builtin_amdgcn_exp2f(x); }
DI bf16x8 rd128(unsigned base, int imm) { return *(const LAS bf16x8*)((const LAS char*)base + imm); }
DI s16x4 rdtr(unsigned base, int imm) { return __builtin_amdgcn_ds_read_tr16_b64_v4i16((LAS s16x4*)((LAS char*)base + imm)); }
DI bf16x8 cat(s16x4 lo, s16x4 hi) { return __builtin_shufflevector(lo, hi, 0, 1, 2, 3, 4, 5, 6, 7); }
DI void wr128f(unsigned base, int imm, f32x4 v) { *(LAS f32x4*)((LAS char*)base + imm) = v; }
DI f32x4 rd128f(unsigned base, int imm) { return *(const LAS f32x4*)((const LAS char*)base + imm); }
DI float red_add(float x) {
  auto a = __builtin_amdgcn_permlane16_swap(__float_as_uint(x), __float_as_uint(x), false, false); x = __uint_as_float(a[0]) + __uint_as_float(a[1]);
  auto b = __builtin_amdgcn_permlane32_swap(__float_as_uint(x), __float_as_uint(x), false, false); return __uint_as_float(b[0]) + __uint_as_float(b[1]); }
DI float red_max(float x) {
  auto a = __builtin_amdgcn_permlane16_swap(__float_as_uint(x), __float_as_uint(x), false, false); x = fmaxf(__uint_as_float(a[0]), __uint_as_float(a[1]));
  auto b = __builtin_amdgcn_permlane32_swap(__float_as_uint(x), __float_as_uint(x), false, false); return fmaxf(__uint_as_float(b[0]), __uint_as_float(b[1])); }
DI int lane_id() { int l; asm volatile("v_mbcnt_lo_u32_b32 %0, -1, 0\n\tv_mbcnt_hi_u32_b32 %0, -1, %0" : "=v"(l)); return l; }
DI rsrc_t mkrs(const void* p, unsigned bytes) { return __builtin_amdgcn_make_buffer_rsrc((void*)p, 0, bytes, 0x00020000); }

template <int IMM> DI void dma_nt(unsigned m0v, unsigned voff, rsrc_t rs, unsigned soff) {
  asm volatile("s_mov_b32 m0, %0\n\ts_nop 0\n\tbuffer_load_dwordx4 %1, %2, %3 offen offset:%4 nt lds" ::"s"(m0v), "v"(voff), "s"(rs), "s"(soff), "i"(IMM) : "memory");
}
DI void dma_df(unsigned m0v, unsigned voff, rsrc_t rs, unsigned soff) {
  asm volatile("s_mov_b32 m0, %0\n\ts_nop 0\n\tbuffer_load_dwordx4 %1, %2, %3 offen lds" ::"s"(m0v), "v"(voff), "s"(rs), "s"(soff) : "memory");
}
#define CODE_LINES 14
constexpr unsigned CODE_BYTES = CODE_LINES * 4096;
DI void touch_code(unsigned dump, unsigned long long pc, int idx) {
  const rsrc_t rs = __builtin_amdgcn_make_buffer_rsrc((void*)pc, 0, CODE_BYTES, 0x00020000);
  const unsigned l = lane_id(), vo = l < CODE_LINES ? 128 * (l + CODE_LINES * idx) : 0x80000000u;
  asm volatile("s_mov_b32 m0, %0\n\ts_nop 3\n\tbuffer_load_dword %1, %2, 0 offen lds" ::"s"(dump), "v"(vo), "s"(rs) : "memory");
}
DI void dma_sc1(unsigned m0v, unsigned voff, rsrc_t rs, unsigned soff) {
  asm volatile("s_mov_b32 m0, %0\n\ts_nop 0\n\tbuffer_load_dwordx4 %1, %2, %3 offen sc1 lds" ::"s"(m0v), "v"(voff), "s"(rs), "s"(soff) : "memory");
}
DI void touch80(unsigned& w0, unsigned& w1, const char* p) {
  const unsigned vo = 128 * lane_id();
  asm volatile("s_nop 4\n\tglobal_load_dword %0, %2, %3\n\tglobal_load_dword %1, %2, %3 offset:2048" : "=&v"(w0), "=&v"(w1) : "v"(vo), "s"(p) : "memory");
}
#define BPRIO 0
#define QLATE 0
#define OLATE 0
#define OAUX 16
#define XDMA dma_df
DI void mfma2(f32x4& c, const bf16x8& a0, const bf16x8& b0, const bf16x8& a1, const bf16x8& b1) {
  asm volatile("v_mfma_f32_16x16x32_bf16 %0, %1, %2, %0\n\tv_mfma_f32_16x16x32_bf16 %0, %3, %4, %0" : "+v"(c) : "v"(a0), "v"(b0), "v"(a1), "v"(b1));
}
template <bool SC1> DI void mfma2d(f32x4& c, const bf16x8& a0, const bf16x8& b0, const bf16x8& a1, const bf16x8& b1, unsigned m0v, unsigned voff, rsrc_t rs, unsigned soff) {
  if constexpr (SC1)
    asm volatile("v_mfma_f32_16x16x32_bf16 %0, %1, %2, %0\n\ts_mov_b32 m0, %5\n\tv_mfma_f32_16x16x32_bf16 %0, %3, %4, %0\n\tbuffer_load_dwordx4 %6, %7, %8 offen sc1 lds"
                 : "+v"(c) : "v"(a0), "v"(b0), "v"(a1), "v"(b1), "s"(m0v), "v"(voff), "s"(rs), "s"(soff) : "memory");
  else
    asm volatile("v_mfma_f32_16x16x32_bf16 %0, %1, %2, %0\n\ts_mov_b32 m0, %5\n\tv_mfma_f32_16x16x32_bf16 %0, %3, %4, %0\n\tbuffer_load_dwordx4 %6, %7, %8 offen lds"
                 : "+v"(c) : "v"(a0), "v"(b0), "v"(a1), "v"(b1), "s"(m0v), "v"(voff), "s"(rs), "s"(soff) : "memory");
}
DI void mfma1(f32x4& c, const bf16x8& a, const bf16x8& b) { asm volatile("v_mfma_f32_16x16x32_bf16 %0, %1, %2, %0" : "+v"(c) : "v"(a), "v"(b)); }
typedef unsigned long long u64;
constexpr int SPIN_MAX = 1 << 16;
constexpr u64 HS_K = 0x9E3779B97F4A7C15ull;
struct HS { char* base; unsigned in, out; u32x2 req, ack; };
DI u64 j64(u32x2 v) { return ((u64)v[1] << 32) | v[0]; }
DI void hs_load(u32x2& v, const HS& h, unsigned off) { asm volatile("s_nop 4\n\tbuffer_load_dwordx2 %0, %1, %2, 0 offen sc1" : "=v"(v) : "v"(off), "s"(mkrs(h.base, 65536)) : "memory"); }
DI u32x2 hs_load_now(const HS& h, unsigned off) { asm volatile("" ::: "memory"); const u32x2 v = __builtin_amdgcn_raw_buffer_load_b64(mkrs(h.base, 65536), off, 0, 16); __builtin_amdgcn_s_waitcnt(0x0F70); return v; }
DI void hs_store(const HS& h, unsigned off, u32x2 v) { __builtin_amdgcn_raw_buffer_store_b64(v, mkrs(h.base, 65536), off, 0, 16); }
DI unsigned hs_sel(unsigned off, int phase) { return (lane_id() >> 2) == phase ? off : 0x80000000u; }
DI void hs_signal(const HS& h, int phase) { hs_store(h, hs_sel(h.out, phase), h.ack); }
DI void hs_wait(const HS& h, int phase, u32x2 v) {
  asm volatile("" : "+v"(v)::"memory");
  const bool other = (lane_id() >> 2) != phase;
#pragma nounroll
  for (int spin = 0; spin < (1 << 22) && !__all(other || j64(v) == j64(h.req)); ++spin) { __builtin_amdgcn_s_sleep(1); v = hs_load_now(h, h.in); }
  asm volatile("" ::: "memory");
}

#define STAMP(i)
#define TS(x)
#define FST(i)
#define PST_(i)
struct Args { const u16 *x0, *k0, *v0, *wq0, *wo0, *x1, *k1, *v1, *wq1, *wo1; char* ws; u16* out; };
struct Ctx {
  const u16 *x, *k, *v, *wq; const char *wo, *warm; char *oq, *part; u16* out; char* hs; unsigned long long* stp; unsigned ldsb; int own, nt, kq, idx, gid; unsigned long long pc;
};

DI void slow16(const u16* Kg, const u16* Vg, int key0, int nstep, const bf16x8 (&q)[4], f32x4 (&o)[8], float& m, float& l) {
  const int lane = lane_id(), r = lane & 15, g = lane >> 4;
  m = -3.0e38f; l = 0.f;
#pragma unroll
  for (int d = 0; d < 8; ++d) o[d] = f32x4{};
#pragma nounroll
  for (int st = 0; st < nstep; ++st) {
    const int kb = nstep == 64 ? 16 * st : key0 + 128 * (st >> 1) + 16 * (st & 1);
    f32x4 S = {};
#pragma unroll
    for (int ks = 0; ks < 4; ++ks) S = MFMA16(*(const bf16x8*)(Kg + (size_t)(kb + r) * DH + 32 * ks + 8 * g), q[ks], S);
    const float mx = red_max(fmaxf(fmaxf(S[0], S[1]), fmaxf(S[2], S[3])));
    const float mn = fmaxf(m, mx), al = ex2(m - mn); m = mn;
    float ps = 0.f;
#pragma unroll
    for (int i = 0; i < 4; ++i) { S[i] = ex2(S[i] - mn); ps += S[i]; }
    l = l * al + ps;
    u32x2 pw = {cvtpk(S[0], S[1]), cvtpk(S[2], S[3])};
#pragma unroll
    for (int d = 0; d < 8; ++d) {
      s16x4 vf;
#pragma unroll
      for (int j = 0; j < 4; ++j) vf[j] = (short)Vg[(size_t)(kb + 4 * g + j) * DH + 16 * d + r];
      o[d] *= al;
      o[d] = MFMA16K16(vf, __builtin_bit_cast(s16x4, pw), o[d]);
    }
  }
  __builtin_amdgcn_s_waitcnt(0);
}
DI float nanprobe_s(const f32x4 (&o)[8]) {
  float t = 0.f; const float z = 0.f;
#pragma unroll
  for (int d = 0; d < 8; ++d) { t = ffma_s(o[d][0], z, t); t = ffma_s(o[d][1], z, t); t = ffma_s(o[d][2], z, t); t = ffma_s(o[d][3], z, t); }
  return t;
}
DI float nanprobe(const f32x4 (&o)[8]) {
  f32x4 t = {};
#pragma unroll
  for (int d = 0; d < 8; ++d) t += o[d] * 0.f;
  return (t[0] + t[1]) + (t[2] + t[3]);
}

template <int NR, class FD, class FW> DI void qproj(const Ctx& c, int cg, FD&& dma, FW&& wait) {
  f32x4 acc[NR][4]; bf16x8 w[4][2], x[NR][2];
  const unsigned lane = lane_id(), r16 = lane & 15, g = lane >> 4, q4 = (lane & 15) >> 2, p = lane & 3;
  const unsigned wb0 = c.ldsb + 1024 * (cg & 1) + 2048 * g + 64 * q4 + 16 * ((p >> 1) ^ (2 * (g & 1))) + 8 * (p & 1);
  const unsigned xb0 = c.ldsb + XOFF + (NR == 7 ? 0 : 7 * 2048) + 128 * r16 + 16 * (g ^ ((r16 >> 1) & 7));
#pragma unroll
  for (int r = 0; r < NR; ++r)
#pragma unroll
    for (int i = 0; i < 4; ++i) acc[r][i] = f32x4{};
  unsigned xs = 0, ws = SLOT * (cg >> 1), xp0, xp1, wp0, wp1;
  auto ldw = [&](int i) { const unsigned b = (i & 1) ? wp1 : wp0; const int im = 512 * (i >> 1);
    w[i][0] = cat(rdtr(b, im), rdtr(b, im + 256)); w[i][1] = cat(rdtr(b, im + 8192), rdtr(b, im + 8448)); };
  auto ldx = [&](int r) { x[r][0] = rd128(xp0, 2048 * r); x[r][1] = rd128(xp1, 2048 * r); };
  auto row = [&](int r) {
#pragma unroll
    for (int i = 0; i < 4; ++i) mfma2(acc[r][i], w[i][0], x[r][0], w[i][1], x[r][1]);
  };
  constexpr int LATE_R = QLATE;
  auto half = [&](int r, int h) { mfma2(acc[r][2 * h], w[2 * h][0], x[r][0], w[2 * h][1], x[r][1]); mfma2(acc[r][2 * h + 1], w[2 * h + 1][0], x[r][0], w[2 * h + 1][1], x[r][1]); };
  auto step = [&](auto K, auto F, int s) {
    constexpr bool FIRST = decltype(F)::value;
    xp0 = xb0 + xs; xp1 = xp0 ^ 64; wp0 = wb0 + ws; wp1 = wp0 ^ 32;
    xs = xs == 2 * XST ? 0 : xs + XST; ws += 2 * SLOT; ws = ws >= 5 * SLOT ? ws - 5 * SLOT : ws;
    wait(s);
    SBAR();
    BAR();
    if constexpr (FIRST) { STAMP(1); }
    SBAR();
    if constexpr (NR == 7) {
#pragma unroll
      for (int r = 0; r < 7; ++r) {
        if constexpr (!FIRST) half(r, 0);
        SBAR(); if (r) x[r - 1][0] = rd128(xp0, 2048 * (r - 1)); SBAR();
        if constexpr (!FIRST) half(r, 1);
        SBAR(); if (r) x[r - 1][1] = rd128(xp1, 2048 * (r - 1)); SBAR();
      }
      ldx(6);
#pragma unroll
      for (int i = 0; i < 4; ++i) { ldw(i); dma(K, s, 2 * i); if (i < 3) dma(K, s, 2 * i + 1); SBAR(); }
    } else {
      if constexpr (!FIRST) {
#pragma unroll
        for (int r = 6 - LATE_R; r < 6; ++r) row(r);
        __builtin_amdgcn_s_setprio(0);
        SBAR();
      }
#pragma unroll
      for (int i = 0; i < 4; ++i) { dma(K, s, i); ldw(i); SBAR(); }
#pragma unroll
      for (int r = 0; r < 6; ++r) { if (r < 4) dma(K, s, 4 + r); ldx(r); SBAR(); }
#pragma unroll
      for (int r = 0; r < 6 - LATE_R; ++r) row(r);
      __builtin_amdgcn_s_setprio(1);
      SBAR();
    }
  };
  asm volatile("s_nop 1" ::: "memory");
  step(ic<NR == 7 ? 0 : 3>{}, ic<1>{}, 0);
#pragma nounroll
  for (int s = 1; s < 8; ++s) step(ic<0>{}, ic<0>{}, s);
  step(ic<1>{}, ic<0>{}, 8);
  step(ic<2>{}, ic<0>{}, 9);
  BAR();
  if constexpr (NR == 6) {
#pragma unroll
    for (int r = 6 - LATE_R; r < 6; ++r) row(r);
    __builtin_amdgcn_s_setprio(0);
    SBAR();
  }
  dma(ic<4>{}, 10, 0);
  const unsigned ln = lane_id(), rr = ln & 15, gg = ln >> 4;
  const unsigned qw = c.ldsb + 2048 * (cg & 1) + 128 * rr + 16 * ((gg >> 1) ^ ((rr >> 1) & 7)) + 8 * (gg & 1);
  auto put = [&](int r) {
    const int rt = (NR == 7 ? 0 : 7) + r;
    const unsigned blk = (cg >> 1) ? Q2OFF + 4096 * rt : q1addr(rt);
#pragma unroll
    for (int i = 0; i < 4; ++i) {
      const f32x4 v = acc[r][i];
      *(LAS u32x2*)((qw ^ (32 * i)) + blk) = u32x2{cvtpk(fmul_s(v[0], QSCALE), fmul_s(v[1], QSCALE)), cvtpk(fmul_s(v[2], QSCALE), fmul_s(v[3], QSCALE))};
    }
  };
  SBAR();
  if constexpr (NR == 7) {
#pragma unroll
    for (int r = 0; r < 7; ++r) { row(r); SBAR(); if (r) put(r - 1); SBAR(); }
    MFMA_DONE();
    put(6);
  } else {
    MFMA_DONE();
#pragma unroll
    for (int r = 0; r < 6; ++r) put(r);
  }
}

struct OD { rsrc_t rw, ro; unsigned vwp, vwo, vo, mw, mo, lo; int rg; };
DI OD od_make(const Ctx& c, int rg) {
  OD d; const unsigned lane = lane_id(), xr = 16 * ((lane & 3) ^ (((lane >> 4) & 1) << 1));
  d.rw = mkrs(c.wo, 1024 * DM * 2); d.ro = mkrs(c.oq, 8 * OBLK);
  d.vwp = 1280 * (16 * rg + ((lane >> 2) & 7)) + 64 * (lane >> 5) + xr + 320 * c.nt + ((c.nt & 1) ? 64 : 0);
  d.vwo = 1280 * (16 * rg + (lane >> 2)) + xr + 320 * c.nt + ((c.nt & 1) ? 0 : 256);
  d.vo = 16 * lane; d.mw = c.ldsb + POFF + 5120 * rg; d.mo = c.ldsb + 1024 * rg; d.lo = 1024 * (24 + (rg & 1)); d.rg = rg;
  return d;
}
DI unsigned od_blk(const Ctx& c, int s) { return (unsigned)(s < 16 ? 4 * ((s >> 1) & 3) + 2 * (s >> 3) + (s & 1) : 64); }
template <class F> DI void od_piece(const Ctx& c, const OD& d, unsigned u, unsigned st, int n, F&& f) {
  const unsigned sw = u * (64 * 1280), so = u * (OBLK / 2), m = d.mw + st;
  if (n == 0) f(ic<0>{}, m, d.vwp, d.rw, sw);
  else if (n == 1) f(ic<0>{}, m + 1024, d.vwp, d.rw, sw + 128);
  else if (n == 2) f(ic<0>{}, m + 3072, d.vwp, d.rw, sw + 10240);
  else if (n == 3) f(ic<0>{}, m + 4096, d.vwp, d.rw, sw + 10240 + 128);
  else if (n == 4) f(ic<0>{}, m + 2048, d.vwo, d.rw, sw);
  else if (n < 11) f(ic<1>{}, d.mo + st + 4096 * (n - 5), d.vo, d.ro, so + 1024 * d.rg + 4096 * (n - 5));
  else f(ic<1>{}, c.ldsb + d.lo + st, d.vo, d.ro, so + d.lo);
}
DI void od_piece(const Ctx& c, const OD& d, unsigned u, unsigned st, int n) {
  od_piece(c, d, u, st, n, [](auto S, unsigned m, unsigned v, rsrc_t r, unsigned so) { if constexpr (decltype(S)::value) dma_sc1(m, v, r, so); else dma_df(m, v, r, so); });
}
template <int NC, int NR> DI void oproj(const Ctx& c, int rg, const HS& h) {
  const OD d = od_make(c, rg);
  f32x4 acc[NR][NC]; bf16x8 w[NC][2], x[NR][2];
  const int c0 = NC == 3 ? 3 * (rg >> 1) : 6 + 2 * (rg >> 1), r0 = NR == 7 ? 0 : 7;
  unsigned wb[NC], xb;
  {
    const unsigned lane = lane_id(), g = lane >> 4, q4 = (lane & 15) >> 2, p = lane & 3;
    const unsigned in = 64 * (4 * (g & 1) + q4) + 16 * ((p >> 1) ^ (2 * (g & 1))) + 8 * (p & 1);
#pragma unroll
    for (int i = 0; i < NC; ++i) {
      const int ct = c0 + i;
      const unsigned off = ct < 8 ? 512 * (ct >> 1) + 3072 * (g >> 1) : 2048 + 512 * (g >> 1);
      wb[i] = c.ldsb + POFF + off + (in ^ (32 * (ct & 1)));
    }
    xb = c.ldsb + 16 * lane + 2048 * r0;
  }
#pragma unroll
  for (int r = 0; r < NR; ++r)
#pragma unroll
    for (int i = 0; i < NC; ++i) acc[r][i] = f32x4{};
  unsigned st = PS0, nx = PS1, fr = PS2;
  auto ldw = [&](int i) { const unsigned wp = wb[i] + st; w[i][0] = cat(rdtr(wp, 0), rdtr(wp, 5120)); w[i][1] = cat(rdtr(wp, 10240), rdtr(wp, 15360)); };
  auto ldx = [&](int r) { const unsigned xp = xb + st; x[r][0] = rd128(xp, 2048 * r); x[r][1] = rd128(xp, 2048 * r + 1024); };
  auto row = [&](int r) {
#pragma unroll
    for (int i = 0; i < NC; ++i) mfma2(acc[r][i], w[i][0], x[r][0], w[i][1], x[r][1]);
  };
#pragma unroll
  for (int r = 0; r < NR; ++r) { x[r][0] = bf16x8{}; x[r][1] = bf16x8{}; }
#pragma unroll
  for (int i = 0; i < NC; ++i) { w[i][0] = bf16x8{}; w[i][1] = bf16x8{}; }
  u32x2 pv = {};
  unsigned long long f0 = 0, f1 = 0, f2 = 0, f3 = 0, tp = 0, t2 = 0; (void)f0; (void)f1; (void)f2; (void)f3; (void)tp; (void)t2;
  const bool me = NC == 2 && NR == 6 && rg == 0;
  STAMP(8);
  auto tile_d = [&](int r, int i, unsigned u, int n) {
    od_piece(c, d, u, fr, n, [&](auto S, unsigned m, unsigned v, rsrc_t rs, unsigned so) { mfma2d<decltype(S)::value != 0>(acc[r][i], w[i][0], x[r][0], w[i][1], x[r][1], m, v, rs, so); });
  };
  if constexpr (NC == 2) VMW(13);
#pragma nounroll
  for (int s = 0; s < 16; ++s) {
    const unsigned u = od_blk(c, s + 2), xp = xb + st;
    SBAR();
    BAR();
    if (NC == 3 && NR == 7 && rg == 2) PST_(s);
    SBAR();
    if constexpr (NC == 3) {
#pragma unroll
      for (int r = 0; r < NR; ++r) {
        if (NR == 6 && r == 0) tile_d(0, 0, u, 11); else mfma2(acc[r][0], w[0][0], x[r][0], w[0][1], x[r][1]);
        SBAR(); if (r) x[r - 1][0] = rd128(xp, 2048 * (r - 1)); SBAR();
        mfma2(acc[r][1], w[1][0], x[r][0], w[1][1], x[r][1]);
        SBAR(); if (r) x[r - 1][1] = rd128(xp, 2048 * (r - 1) + 1024); SBAR();
        tile_d(r, 2, u, r < 5 ? r : r + 5);
        SBAR();
      }
      ldx(NR - 1);
#pragma unroll
      for (int i = 0; i < 3; ++i) ldw(i);
      SBAR();
      VMW(7);
    } else {
#pragma unroll
      for (int r = NR - OLATE; r < NR; ++r) row(r);
      __builtin_amdgcn_s_setprio(0);
      SBAR();
      ldw(0); ldx(0); ldw(1);
#pragma unroll
      for (int r = 1; r < NR; ++r) ldx(r);
      SBAR();
      if constexpr (NR == 6) {
        if (me && s == 1) hs_signal(h, 1);
        if (me && s == 5) hs_load(pv, h, h.in);
        PIN(pv);
      }
#pragma unroll
      for (int r = 0; r < NR - OLATE; ++r) {
        if (r < 5) tile_d(r, 0, u, 5 + r); else mfma2(acc[r][0], w[0][0], x[r][0], w[0][1], x[r][1]);
        if (NR - OLATE < 5 && r == 0) tile_d(r, 1, u, 9); else mfma2(acc[r][1], w[1][0], x[r][0], w[1][1], x[r][1]);
        SBAR();
      }
      VMW(5);
      if constexpr (NR == 6) { if (me && s == 5) hs_wait(h, 1, pv); }
      __builtin_amdgcn_s_setprio(1);
    }
    const unsigned t = st; st = nx; nx = fr; fr = t;
  }
  STAMP(9);
  LGKM0(); SBAR();
#pragma unroll
  for (int r = NC == 3 ? 0 : NR - OLATE; r < NR; ++r) row(r);
  __builtin_amdgcn_s_setprio(0);
  MFMA_DONE();
  STAMP(10);
  constexpr int NT = NR * NC, NU = (NT + 1) / 2;
  const int wv = NC == 3 ? rg : 4 + rg;
  const unsigned ln = lane_id(), pv0 = 16 * ln + 11264 * wv;
  const rsrc_t rp = mkrs(c.part, 16 * PARTB);
  f32x4 own[3][2];
  auto scatter = [&](auto K) {
    constexpr int k = decltype(K)::value;
#pragma unroll
    for (int m = 0; m < NU; ++m) {
      const f32x4 u = acc[(2 * m) / NC][(2 * m) % NC], v = 2 * m + 1 < NT ? acc[(2 * m + 1) / NC][(2 * m + 1) % NC] : f32x4{};
      if ((m & 3) == k) { own[m >> 2][0] = u; own[m >> 2][1] = v; }
      else __builtin_amdgcn_raw_buffer_store_b128(u32x4{cvtpk(u[0], u[1]), cvtpk(u[2], u[3]), cvtpk(v[0], v[1]), cvtpk(v[2], v[3])}, rp, pv0 + (4 * k + c.nt) * PARTB + 1024 * m, 0, 16);
    }
#pragma unroll
    for (int e = 0; e < 3; ++e) if (4 * e + k >= NU) { own[e][0] = f32x4{}; own[e][1] = f32x4{}; }
  };
  if (c.kq == 0) scatter(ic<0>{}); else if (c.kq == 1) scatter(ic<1>{}); else if (c.kq == 2) scatter(ic<2>{}); else scatter(ic<3>{});
  VMW(0);
  BAR();
  if (me) { hs_signal(h, 2); hs_wait(h, 2, hs_load_now(h, h.in)); }
  BAR();
  u32x4 pp[3][3];
#pragma unroll
  for (int e = 0; e < 3; ++e)
#pragma unroll
    for (int q = 1; q < 4; ++q) {
      const int m = 4 * e + c.kq;
      pp[e][q - 1] = __builtin_amdgcn_raw_buffer_load_b128(rp, m < NU ? pv0 + (4 * ((c.kq + q) & 3) + c.nt) * PARTB + 1024 * m : 0x80000000u, 0, 16);
    }
#pragma unroll
  for (int e = 0; e < 3; ++e)
#pragma unroll
    for (int h = 0; h < 2; ++h) {
      const int n = 2 * (4 * e + c.kq) + h;
      f32x4 v = own[e][h];
#pragma unroll
      for (int q = 0; q < 3; ++q) {
        v[0] += __uint_as_float(pp[e][q][2 * h] << 16); v[1] += __uint_as_float(pp[e][q][2 * h] & 0xffff0000u);
        v[2] += __uint_as_float(pp[e][q][2 * h + 1] << 16); v[3] += __uint_as_float(pp[e][q][2 * h + 1] & 0xffff0000u);
      }
      if (n < NT) {
        const int rt = r0 + n / NC, ct = c0 + n % NC;
        const int col = 160 * c.nt + (ct < 8 ? ((c.nt & 1) ? 32 : 0) + 16 * ct : ((c.nt & 1) ? 0 : 128) + 16 * (ct - 8));
        __builtin_amdgcn_raw_buffer_store_b64(u32x2{cvtpk(v[0], v[1]), cvtpk(v[2], v[3])}, mkrs(c.out, (SQ - 1) * 4 * DM + 2 * DM), 2 * ((16 * rt + (ln & 15)) * (2 * DM) + col + 4 * (ln >> 4)), 0, OUTAUX);
      }
    }
  STAMP(11);
}

struct AS {
  f32x4 o[3][8], S[2][3], negm, l[3]; bf16x8 q[3][4], F[2][4], ones; u32x4 P[2][3];
  unsigned kp[2], vp[2], kn[2];
};
template <int N, int I> DI void a_frag(AS& a) {
  constexpr int sub = (N >> 2) & 1, b = N & 3;
  if constexpr ((b & 1) == 0) {
    constexpr int kt = 2 * sub + (b >> 1);
    a.F[N & 1][I] = rd128(N == 8 ? a.kn[I & 1] : a.kp[I & 1], 8192 * (I >> 1) + 2048 * kt);
  } else {
    constexpr int cc = 4 * (b >> 1) + I, imm = 8192 * sub + 512 * (cc >> 1);
    a.F[N & 1][I] = cat(rdtr(a.vp[cc & 1], imm), rdtr(a.vp[cc & 1], imm + 4096));
  }
}
#define LGKM(n) __builtin_amdgcn_s_waitcnt(0xC07F | ((n) << 8))
template <int BLK, int K> DI void sm_exp(AS& a) { constexpr int b = BLK & 3, sb = (b == 1 || b == 2) ? 0 : 1, e = ((b & 1) ? 0 : 6) + K;
  a.S[sb][e >> 2][e & 3] = ex2(a.S[sb][e >> 2][e & 3]); PIN(a.S[sb][e >> 2]); }
template <int BLK, int K> DI void sm_cvt(AS& a) { constexpr int b = BLK & 3, sub = (BLK >> 2) & 1, sb = (b == 1 || b == 2) ? 0 : 1, e = ((b & 1) ? 0 : 6) + 2 * K, pb = b == 0 ? sub ^ 1 : sub, rt = e >> 2;
  a.P[pb][rt][2 * sb + ((e & 3) >> 1)] = cvtpk(a.S[sb][rt][e & 3], a.S[sb][rt][(e & 3) + 1]); PIN(a.P[pb][rt]); }
template <int N, bool MM, bool SM, bool CY, int NX, bool Z, int G> DI void a_gap(AS& a) {
  constexpr int sub = N >> 2, b = N & 3; constexpr bool QK = (b & 1) == 0;
  if constexpr (MM) {
    if constexpr (QK) {
      constexpr int rt = G >> 2, ks = G & 3, sb = b >> 1;
      if constexpr (ks == 0) { if constexpr (Z) a.S[sb][rt] = MFMA16(a.F[N & 1][0], a.q[rt][0], f32x4{}); else a.S[sb][rt] = MFMA16(a.F[N & 1][0], a.q[rt][0], a.negm); }
      else a.S[sb][rt] = MFMA16(a.F[N & 1][ks], a.q[rt][ks], a.S[sb][rt]);
    } else if constexpr (G < 12) {
      constexpr int i = G / 3, rt = G % 3, cc = 4 * (b >> 1) + i;
      if constexpr (Z) a.o[rt][cc] = MFMA16(a.F[N & 1][i], __builtin_bit_cast(bf16x8, a.P[sub ^ 1][rt]), f32x4{});
      else a.o[rt][cc] = MFMA16(a.F[N & 1][i], __builtin_bit_cast(bf16x8, a.P[sub ^ 1][rt]), a.o[rt][cc]);
    } else {
      constexpr int rt = b == 1 ? G - 12 : 2;
      if constexpr (Z) a.l[rt] = MFMA16(a.ones, __builtin_bit_cast(bf16x8, a.P[sub ^ 1][rt]), f32x4{});
      else a.l[rt] = MFMA16(a.ones, __builtin_bit_cast(bf16x8, a.P[sub ^ 1][rt]), a.l[rt]);
    }
    SBAR();
  }
  constexpr int LAST = QK ? 11 : b == 1 ? 13 : 12;
  if constexpr (G == LAST) { if constexpr (QK) LGKM(4); else LGKM(0); }
  if constexpr (G == 5 && !QK) { if constexpr (NX >= 0) LGKM(3); else LGKM(0); }
  SBAR();
  if constexpr (NX >= 0 && (G & 1) == 0 && G < 8) a_frag<NX, G / 2>(a);
  SBAR();
  if constexpr ((G & 1) && G < 12) { if constexpr (SM) sm_exp<N, G / 2>(a); }
  else if constexpr (QK) {
    if constexpr (SM && G >= 8) sm_cvt<N, G / 2 - 4>(a);
  } else {
    if constexpr (G == 0 && CY) sm_cvt<N - 1, 2>(a);
    if constexpr (SM && G >= 8 && G <= 12 && (G & 1) == 0) sm_cvt<N, G / 2 - 4>(a);
  }
  SBAR();
}
template <int N, bool MM, bool SM, bool CY, int NX, bool Z, int... Gs> DI void a_block_(AS& a, iseq<Gs...>) { (a_gap<N, MM, SM, CY, NX, Z, Gs>(a), ...); }
template <int N, bool MM, bool SM, bool CY, int NX, bool Z = false> DI void a_block(AS& a) { a_block_<N, MM, SM, CY, NX, Z>(a, mkseq<(N & 1) == 0 ? 12 : (N & 3) == 1 ? 14 : 13>{}); }
template <bool FIRST> DI void a_period(AS& a) {
  if constexpr (FIRST) {
    a_block<0, true, false, false, -1, true>(a);
    float mx = 3.0e38f;
#pragma unroll
    for (int rt = 0; rt < 3; ++rt) mx = fminf(mx, red_max(fmaxf(fmaxf(a.S[0][rt][0], a.S[0][rt][1]), fmaxf(a.S[0][rt][2], a.S[0][rt][3]))));
    a.negm = f32x4{-mx, -mx, -mx, -mx}; PIN(a.negm);
#pragma unroll
    for (int rt = 0; rt < 3; ++rt) a.S[0][rt] -= mx;
    SBAR();
    a_block<1, false, true, false, 2>(a); a_block<2, true, true, true, -1>(a); a_block<3, false, true, true, 4>(a);
  } else {
    a_block<0, true, true, true, 1>(a); a_block<1, true, true, true, 2>(a); a_block<2, true, true, true, 3>(a); a_block<3, true, true, true, 4>(a);
  }
  a_block<4, true, true, true, 5>(a); a_block<5, true, true, true, 6, FIRST>(a); a_block<6, true, true, true, 7>(a);
  if constexpr (FIRST) VMW(0);
  BAR();
  a_block<7, true, true, true, 8, FIRST>(a);
}

DI void path_a(const Ctx& c, int wi) {
  STAMP(0);
  {
    const rsrc_t rx = mkrs(c.x, SQ * DM * 2);
    const unsigned lane = lane_id(), vox = 1280u * (lane >> 3) + 16u * ((lane & 7) ^ ((4 * (wi & 1) + (lane >> 4)) & 7));
    auto xp = [&](int s, unsigned stage, int n) { const int pc = n < 6 ? wi + 4 * n : 24 + (wi & 1); XDMA(c.ldsb + XOFF + stage + 1024 * pc, vox, rx, 128u * s + 10240u * pc); };
#pragma unroll
    for (int n = 0; n < 7; ++n) xp(0, 0, n);
#pragma unroll
    for (int n = 0; n < 7; ++n) xp(1, XST, n);
    unsigned xf = XST;
    qproj<7>(c, wi,
      [&](auto K, int s, int n) { if constexpr (decltype(K)::value == 0) xp(s + 2, xf, n); },
      [&](int s) { if (s == 9) VMW(0); else VMW(7); xf = xf == 2 * XST ? 0 : xf + XST; });
  }
  FST(11);
  BAR();
  FST(12);
  AS a;
  unsigned kb0, vb0;
  { const unsigned lane = lane_id(), r16 = lane & 15, g = lane >> 4, q4 = (lane & 15) >> 2, p = lane & 3;
    kb0 = c.ldsb + 128 * r16 + 16 * (g ^ ((r16 >> 1) & 7));
    vb0 = c.ldsb + SLOT + 2048 * (g >> 1) + 64 * (4 * (g & 1) + q4) + 16 * ((p >> 1) ^ (2 * (g & 1))) + 8 * (p & 1); }
  unsigned sl = 0;
  a.ones = bf16x8{0x3f80, 0x3f80, 0x3f80, 0x3f80, 0x3f80, 0x3f80, 0x3f80, 0x3f80}; PIN(a.ones);
#pragma nounroll
  for (int hd = 0; hd < 2; ++hd) {
#pragma unroll
    for (int rt = 0; rt < 3; ++rt) {
      const int t = 3 * wi + rt;
      const unsigned blk = hd ? Q2OFF + 4096 * t : (t < 8 ? 4 * SLOT + 4096 * t : t < 11 ? MISC + 4096 * (t - 8) : SLOT + 4096 * (t - 11));
      const unsigned q0 = kb0 + blk, q1 = q0 ^ 64;
      a.q[rt][0] = rd128(q0, 0); a.q[rt][1] = rd128(q1, 0); a.q[rt][2] = rd128(q0, 2048); a.q[rt][3] = rd128(q1, 2048);
      a.P[0][rt] = u32x4{}; a.P[1][rt] = u32x4{};
    }
    a.kn[0] = kb0 + sl; a.kn[1] = a.kn[0] ^ 64;
    a_frag<8, 0>(a); a_frag<8, 1>(a); a_frag<8, 2>(a); a_frag<8, 3>(a);
    if (hd == 0) BAR();
    STAMP(2 + 3 * hd);
    auto setp = [&]() {
      a.kp[0] = a.kn[0]; a.kp[1] = a.kn[1]; a.vp[0] = vb0 + sl; a.vp[1] = a.vp[0] ^ 32;
      sl = sl == 4 * SLOT ? 0 : sl + 2 * SLOT;
      a.kn[0] = kb0 + sl; a.kn[1] = a.kn[0] ^ 64;
      SBAR();
    };
    setp();
    a_period<true>(a);
#pragma nounroll
    for (int t = 1; t < 16; ++t) {
 if (wi == 0) PST_(16 * hd + t);
 setp(); a_period<false>(a); }
    a.vp[0] = vb0 + sl; a.vp[1] = a.vp[0] ^ 32;
    SBAR();
    a_block<0, false, true, true, 1>(a); a_block<1, true, false, true, -1>(a);
    a_frag<3, 0>(a); a_frag<3, 1>(a); a_frag<3, 2>(a); a_frag<3, 3>(a);
    a_block<3, true, false, false, -1>(a);
    if (hd) BAR();
    STAMP(3 + 3 * hd);
    const rsrc_t ro = mkrs(c.oq + (size_t)(2 * c.own + hd) * OBLK, OBLK);
    const unsigned ov = 16 * lane_id() + 6144 * wi;
#pragma unroll
    for (int rt = 0; rt < 3; ++rt) {
      float l = a.l[rt][0];
      if (__builtin_expect(__any(!(l + nanprobe(a.o[rt]) < L_LIMIT)), 0)) {
        float m;
        slow16(c.k + (size_t)hd * SK * DH, c.v + (size_t)hd * SK * DH, 0, 64, a.q[rt], a.o[rt], m, l);
        l = red_add(l);
      }
      const float il = __builtin_amdgcn_rcpf(l);
#pragma unroll
      for (int kk = 0; kk < 4; ++kk) {
        const f32x4 u = a.o[rt][2 * kk] * il, v = a.o[rt][2 * kk + 1] * il;
        u32x4 w = {cvtpk(u[0], u[1]), cvtpk(u[2], u[3]), cvtpk(v[0], v[1]), cvtpk(v[2], v[3])};
        __builtin_amdgcn_raw_buffer_store_b128(w, ro, ov + 2048 * rt + 1024 * (kk & 1) + (OBLK / 2) * (kk >> 1), 0, OAUX);
      }
    }
    STAMP(4 + 3 * hd);
  }
}

DI void path_b(const Ctx& c, int j, HS& h) {
  STAMP(0);
  { const unsigned l = lane_id(), ph = l >> 2, i = l & 3;
    h.in = l < 12 ? 256 * c.gid + 8 * l : 0x80000000u;
    h.out = l < 12 ? (ph < 2 ? 256 * ((c.gid & ~3) | i) + 8 * (4 * ph + c.own) : 256 * ((c.gid & ~12) | (i << 2)) + 8 * (8 + c.kq)) : 0x80000000u;
    h.req = u32x2{}; h.ack = u32x2{};
    if (j == 0) hs_load(h.req, h, h.in); }
  const rsrc_t rk = mkrs(c.k, 2 * SK * DH * 2), rv = mkrs(c.v, 2 * SK * DH * 2);
  unsigned vk0, vk1, vv, wm0 = 0, wm1 = 0;
  { const unsigned i = lane_id();
    vk0 = 256 * (i >> 3) + 16 * ((i & 7) ^ (i >> 4)); vk1 = (vk0 ^ 64) + 2048;
    vv = 256 * ((i >> 2) & 7) + 64 * (i >> 5) + 16 * ((i & 3) ^ (((i >> 4) & 1) << 1)); }
  auto kpc = [&](int t, unsigned slot, int n) {
    const unsigned m = c.ldsb + slot + 2048 * j, so = (unsigned)(64 * t + 16 * j) * 256u;
    if (n == 0) dma_nt<0>(m, vk0, rk, so); if (n == 1) dma_nt<128>(m + 8064, vk0, rk, so); if (n == 2) dma_nt<0>(m + 1024, vk1, rk, so); if (n == 3) dma_nt<128>(m + 9088, vk1, rk, so);
  };
  auto vpc = [&](int t, unsigned slot, int n) {
    const unsigned m = c.ldsb + slot + 4096 * j, so = (unsigned)(64 * t - 32 + 16 * j) * 256u;
    if (n == 0) dma_nt<0>(m, vv, rv, so); if (n == 1) dma_nt<128>(m + 896, vv, rv, so); if (n == 2) dma_nt<2048>(m, vv, rv, so); if (n == 3) dma_nt<2176>(m + 896, vv, rv, so);
  };
  auto kdma = [&](int t, unsigned slot) { kpc(t, slot, 0); kpc(t, slot, 1); kpc(t, slot, 2); kpc(t, slot, 3); };
  auto vdma = [&](int t, unsigned slot) { vpc(t, slot, 0); vpc(t, slot, 1); vpc(t, slot, 2); vpc(t, slot, 3); };
  {
    const rsrc_t rw = mkrs(c.wq, (DM - 1) * HD * 2 + 512);
    const unsigned i = lane_id(), vw0 = 8192 * ((i >> 2) & 7) + 64 * (i >> 5) + 16 * (i & 3), vw1 = (vw0 ^ 32) + 65536;
    auto wpc = [&](int it, unsigned slot, int n) {
      dma_df(c.ldsb + slot + 4096 * j + 1024 * n, n < 2 ? vw0 : vw1, rw, (unsigned)(64 * (it >> 1) + 16 * j) * 8192u + 256 * (it & 1) + 128 * (n & 1));
    };
    if (j == 3) touch_code(c.ldsb + XOFF + 3 * XST, c.pc, c.idx);
#pragma nounroll
    for (int it = 0; it < 5; ++it) { wpc(it, it * SLOT, 0); wpc(it, it * SLOT, 1); wpc(it, it * SLOT, 2); wpc(it, it * SLOT, 3); }
    unsigned f0 = SLOT, f1 = 2 * SLOT;
    auto adv = [](unsigned s_) { s_ += 2 * SLOT; return s_ >= 5 * SLOT ? s_ - 5 * SLOT : s_; };
    qproj<6>(c, j,
      [&](auto K, int s, int n) {
        constexpr int k = decltype(K)::value;
        if constexpr (k == 0) { if (n < 4) wpc(2 * s + 3, f0, n); else wpc(2 * s + 4, f1, n - 4); }
        if constexpr (k == 1) { if (n < 4) wpc(19, 4 * SLOT, n); else kpc(0, 0, n - 4);
          if (n == 7) { touch80(wm0, wm1, c.warm + 10240 * j); } }
        if constexpr (k == 2) { if (n == 0) { if (j >= 2) vdma(0, SLOT); } else if (n >= 4) kpc(1, 2 * SLOT, n - 4); }
        if constexpr (k == 3) { if (n == 0 && j == 0) { PIN(h.req); const u64 r = j64(h.req) + HS_K; h.req = u32x2{(unsigned)r, (unsigned)(r >> 32)}; hs_store(h, h.in + 128, h.req); } }
        if constexpr (k == 4) vdma(1, 3 * SLOT);
      },
      [&](int s) { if (s == 0) VMW(12); else if (s == 9) VMW(6); else VMW(4); f0 = adv(f0); f1 = adv(f1); });
  }
  if (j == 0) {
    const bool idle = lane_id() >= 12;
#pragma nounroll
    for (int spin = 0; spin < SPIN_MAX; ++spin) {
      asm volatile("" ::: "memory");
      const u32x2 a0 = __builtin_amdgcn_raw_buffer_load_b64(mkrs(h.base, 65536), h.out, 0, 16);
      h.ack = hs_load_now(h, h.out + 128);
      if (__all(idle || j64(h.ack) == j64(a0) + HS_K)) break;
      __builtin_amdgcn_s_sleep(1);
    }
  }
  VMW(8);
  asm volatile("" ::"v"(wm0), "v"(wm1));
  BAR();
  unsigned qb, vb0;
  { const unsigned lane = lane_id(), r16 = lane & 15, g = lane >> 4, q4 = (lane & 15) >> 2, p = lane & 3;
    qb = c.ldsb + 128 * r16 + 16 * (g ^ ((r16 >> 1) & 7));
    vb0 = c.ldsb + SLOT + 8192 * ((j & 1) ^ 1) + 2048 * (g >> 1) + 64 * (4 * (g & 1) + q4) + 16 * ((p >> 1) ^ (2 * (g & 1))) + 8 * (p & 1); }
  bf16x8 q[4], vf[8]; f32x4 o[8], os[8], negm = {}; float l = 0.f, ms = 0.f, ls = 0.f; u32x4 P = {};
  unsigned sl = 0, fr = 4 * SLOT;
  auto rdq = [&](unsigned blk) { const unsigned q0 = qb + blk, q1 = q0 ^ 64; q[0] = rd128(q0, 0); q[1] = rd128(q1, 0); q[2] = rd128(q0, 2048); q[3] = rd128(q1, 2048); };
  auto rdv = [&]() {
    const unsigned v0 = vb0 + sl, v1 = v0 ^ 32;
#pragma unroll
    for (int cc = 0; cc < 8; ++cc) vf[cc] = cat(rdtr((cc & 1) ? v1 : v0, 512 * (cc >> 1)), rdtr((cc & 1) ? v1 : v0, 512 * (cc >> 1) + 4096));
  };
  auto put = [&](unsigned scr) {
    const unsigned lane = lane_id(), sc = c.ldsb + scr + 16 * lane + 8192 * j;
#pragma unroll
    for (int cc = 0; cc < 8; ++cc) wr128f(sc, 1024 * cc, os[cc]);
    if (lane < 16) { *(LAS float*)(c.ldsb + scr + 32768 + 128 * j + 4 * lane) = ms; *(LAS float*)(c.ldsb + scr + 32768 + 128 * j + 64 + 4 * lane) = ls; }
  };
  auto merge = [&](int hd, unsigned scr) {
    const unsigned lane = lane_id(), sc = c.ldsb + scr + 16 * lane + 2048 * j, ml = c.ldsb + scr + 32768 + 4 * (lane & 15);
    float mw[4], lw[4];
#pragma unroll
    for (int w = 0; w < 4; ++w) { mw[w] = *(const LAS float*)(ml + 128 * w); lw[w] = *(const LAS float*)(ml + 128 * w + 64); }
    const float mt = fmaxf(fmaxf(mw[0], mw[1]), fmaxf(mw[2], mw[3]));
    float lt = 0.f;
#pragma unroll
    for (int w = 0; w < 4; ++w) { mw[w] = ex2(mw[w] - mt); PIN(mw[w]); lt = ffma_s(mw[w], lw[w], lt); }
    const float ir = __builtin_amdgcn_rcpf(lt);
    f32x4 u = {}, v = {};
#pragma unroll
    for (int w = 0; w < 4; ++w) {
      const f32x4 a = rd128f(sc, 8192 * w), b = rd128f(sc, 8192 * w + 1024); float f = mw[w] * ir; PIN(f);
#pragma unroll
      for (int i = 0; i < 4; ++i) { u[i] = ffma_s(a[i], f, u[i]); v[i] = ffma_s(b[i], f, v[i]); }
    }
    u32x4 w4 = {cvtpk(u[0], u[1]), cvtpk(u[2], u[3]), cvtpk(v[0], v[1]), cvtpk(v[2], v[3])};
    __builtin_amdgcn_raw_buffer_store_b128(w4, mkrs(c.oq + (size_t)(2 * c.own + hd) * OBLK, OBLK), 16 * lane + 12 * 2048 + 1024 * (j & 1) + (OBLK / 2) * (j >> 1), 0, OAUX);
  };
#pragma unroll
  for (int cc = 0; cc < 8; ++cc) { os[cc] = f32x4{}; vf[cc] = bf16x8{}; }
  int t = 0;
  unsigned long long ts = 0;
  auto next = [&]() {
    if (t < 18) VMW(8);
    else {
      if (t == 18) put(Q2OFF);
      if (t == 19) merge(0, Q2OFF);
      if (t == 21 && j == 0) hs_signal(h, 0);
      if (t == 19 || (t == 21 && j == 0)) VMW(9);
      else if (t == 30) { if (j < 2) VMW(16); else VMW(12); }
      else if (t == 31) VMW(12);
      else VMW(8);
    }
    if (j == 3 && t < 29) { while (__builtin_amdgcn_s_memrealtime() - ts < PACE) __builtin_amdgcn_s_sleep(1); }
    BAR();
    ts = __builtin_amdgcn_s_memrealtime();
    fr = sl; sl = sl == 4 * SLOT ? 0 : sl + 2 * SLOT; ++t;
    if (t < 28) { kdma(t + 2, fr); vdma(t + 2, fr + SLOT); }
    else {
      if (t == 28 && j == 3) touch_code(c.ldsb + Q2OFF, c.pc, c.idx);
      if (t == 29 && j == 0) hs_wait(h, 0, hs_load_now(h, h.in));
      if (t < 30) kdma(t + 2, fr);
      if (t < 30 || (t == 30 && j < 2)) vdma(t + 2, fr + SLOT);
      if (t == 30 || t == 32) {
        const OD d = od_make(c, j);
#pragma unroll
        for (int n = 0; n < 12; ++n) od_piece(c, d, t == 30 ? 0 : 1, t == 30 ? PS0 : PS1, n);
      }
    }
  };
  rdq(q1addr(12));
  __builtin_amdgcn_s_setprio(BPRIO);
  BAR();
  kdma(2, fr); vdma(2, fr + SLOT);
  if (j >= 2) { VMW(8);
 BAR();
 fr = sl; sl = 2 * SLOT; t = 1; kdma(3, fr); vdma(3, fr + SLOT); }
#pragma nounroll
  for (int hd = 0; hd < 2; ++hd) {
    if (hd) rdq(Q2OFF + 12 * 4096);
#pragma unroll
    for (int cc = 0; cc < 8; ++cc) o[cc] = f32x4{};
    negm = f32x4{}; l = 0.f;
#pragma nounroll
    for (int u = 0; u < 8; ++u) {
      if ((j & 1) == 0) rdv();
      {
        const unsigned k0 = qb + sl + 4096 * (j & 1), k1 = k0 ^ 64;
        f32x4 S[2]; bf16x8 kf[2][4];
#pragma unroll
        for (int h = 0; h < 2; ++h) { kf[h][0] = rd128(k0, 2048 * h); kf[h][1] = rd128(k1, 2048 * h); kf[h][2] = rd128(k0, 2048 * h + 8192); kf[h][3] = rd128(k1, 2048 * h + 8192); }
        LGKM0(); SBAR();
        __builtin_amdgcn_s_setprio(3);
#pragma unroll
        for (int h = 0; h < 2; ++h) { S[h] = MFMA16(kf[h][0], q[0], negm); S[h] = MFMA16(kf[h][1], q[1], S[h]); S[h] = MFMA16(kf[h][2], q[2], S[h]); S[h] = MFMA16(kf[h][3], q[3], S[h]); }
        SBAR();
        __builtin_amdgcn_s_setprio(BPRIO);
        SBAR();
        if (u == 0) {
          const float mx = red_max(fmaxf(fmaxf(fmaxf(S[0][0], S[0][1]), fmaxf(S[0][2], S[0][3])), fmaxf(fmaxf(S[1][0], S[1][1]), fmaxf(S[1][2], S[1][3]))));
          negm = f32x4{-mx, -mx, -mx, -mx}; S[0] -= mx; S[1] -= mx;
        }
#pragma unroll
        for (int i = 0; i < 8; ++i) { S[i >> 2][i & 3] = ex2(S[i >> 2][i & 3]); l += S[i >> 2][i & 3]; PIN(l); }
        P = u32x4{cvtpk(S[0][0], S[0][1]), cvtpk(S[0][2], S[0][3]), cvtpk(S[1][0], S[1][1]), cvtpk(S[1][2], S[1][3])};
      }
      next();
      if (j & 1) rdv();
      LGKM0(); SBAR();
      __builtin_amdgcn_s_setprio(3);
#pragma unroll
      for (int cc = 0; cc < 8; ++cc) o[cc] = MFMA16(vf[cc], __builtin_bit_cast(bf16x8, P), o[cc]);
      SBAR();
      __builtin_amdgcn_s_setprio(BPRIO);
      SBAR();
      if (t == 32) break;
      next();
    }
    ls = red_add(l); ms = -negm[0];
    MFMA_DONE();
    if (__builtin_expect(__any(!(ls + nanprobe_s(o) < L_LIMIT)), 0)) {
      slow16(c.k + (size_t)hd * SK * DH, c.v + (size_t)hd * SK * DH, 32 * j, 16, q, o, ms, ls);
      ls = red_add(ls);
    }
#pragma unroll
    for (int cc = 0; cc < 8; ++cc) os[cc] = o[cc];
  }
  put(SCR2);
  BAR();
  merge(1, SCR2);
  __builtin_amdgcn_s_setprio(0);
}

__global__ __launch_bounds__(512, 2) void fused_k(Args ar) {
  extern __shared__ __attribute__((aligned(16))) char lds[];
  const int wid = __builtin_amdgcn_readfirstlane(threadIdx.x >> 6);
  const int xcd = blockIdx.x & 7, idx = blockIdx.x >> 3, br = xcd >> 2, kq = 2 * (xcd & 1) + ((idx >> 2) & 1), b = 4 * ((xcd >> 1) & 1) + (idx >> 3), hp = 4 * kq + (idx & 3);
  Ctx c;
  c.pc = __builtin_amdgcn_s_getpc() & ~255ull; c.idx = idx;
  c.ldsb = (unsigned)(size_t)(LAS char*)lds;
  c.x = (br ? ar.x1 : ar.x0) + (size_t)b * SQ * DM;
  c.k = (br ? ar.k1 : ar.k0) + ((size_t)b * H + 2 * hp) * SK * DH;
  c.v = (br ? ar.v1 : ar.v0) + ((size_t)b * H + 2 * hp) * SK * DH;
  c.wq = (br ? ar.wq1 : ar.wq0) + 2 * hp * DH;
  const char* wo = (const char*)(br ? ar.wo1 : ar.wo0);
  c.wo = wo + (size_t)kq * 1024 * DM * 2;
  c.warm = wo + (size_t)((xcd & 3) * 32 + idx) * 40960;
  c.oq = ar.ws + WS_O + (size_t)((br * NB + b) * H + 8 * kq) * OBLK;
  c.part = ar.ws + WS_PART + (size_t)((br * NB + b) * 16) * PARTB;
  c.out = ar.out + (size_t)b * SQ * (2 * DM) + br * DM;
  c.kq = kq; c.hs = ar.ws + WS_CNT; c.gid = (br * NB + b) * 16 + hp;
  c.stp = (unsigned long long*)(ar.ws + WS_STAMP) + (blockIdx.x * 8 + wid) * 32;
  c.own = hp & 3; c.nt = hp & 3;
  const unsigned long long tb = __builtin_amdgcn_s_memrealtime(); __builtin_amdgcn_s_waitcnt(0xC07F);
  HS h = {}; h.base = c.hs;
  if (wid < 4) path_a(c, wid); else path_b(c, wid - 4, h);
  if (wid < 4) { if (wid & 1) oproj<3, 6>(c, wid, h); else oproj<3, 7>(c, wid, h); }
  else { if (wid & 1) oproj<2, 7>(c, wid - 4, h); else oproj<2, 6>(c, wid - 4, h); }
  while (__builtin_amdgcn_s_memrealtime() - tb < FIXT) __builtin_amdgcn_s_sleep(2);
}

}

extern "C" void kernel_launch(void* const* d_in, const int* in_sizes, int n_in, void* d_out, int out_size, void* d_ws, size_t ws_size, hipStream_t stream) {
  using namespace kk;
  Args a;
  a.x0 = (const u16*)d_in[0]; a.k0 = (const u16*)d_in[1]; a.v0 = (const u16*)d_in[2]; a.wq0 = (const u16*)d_in[3]; a.wo0 = (const u16*)d_in[4];
  a.x1 = (const u16*)d_in[5]; a.k1 = (const u16*)d_in[6]; a.v1 = (const u16*)d_in[7]; a.wq1 = (const u16*)d_in[8]; a.wo1 = (const u16*)d_in[9];
  a.ws = (char*)d_ws; a.out = (u16*)d_out;
  (void)hipFuncSetAttribute((const void*)fused_k, hipFuncAttributeMaxDynamicSharedMemorySize, (int)LDS_TOTAL);
  fused_k<<<256, 512, LDS_TOTAL, stream>>>(a);
}
```

```cpp
#include <hip/hip_runtime.h>
#include <hip/hip_bf16.h>
#include <utility>

namespace kk {
typedef unsigned short u16;
typedef short bf16x8 __attribute__((ext_vector_type(8)));
typedef short s16x4 __attribute__((ext_vector_type(4)));
typedef float f32x4 __attribute__((ext_vector_type(4)));
typedef float f32x2 __attribute__((ext_vector_type(2)));
typedef __bf16 bf16x2 __attribute__((ext_vector_type(2)));
typedef unsigned u32x4 __attribute__((ext_vector_type(4)));
typedef unsigned u32x2 __attribute__((ext_vector_type(2)));
typedef __amdgpu_buffer_rsrc_t rsrc_t;
#define LAS __attribute__((address_space(3)))
#define DI __device__ __forceinline__
#define SBAR() __builtin_amdgcn_sched_barrier(0)
#define PIN(x) asm volatile("" : "+v"(x))
#define MFMA16(a, b, c) __builtin_amdgcn_mfma_f32_16x16x32_bf16(a, b, c, 0, 0, 0)
#define MFMA16K16(a, b, c) __builtin_amdgcn_mfma_f32_16x16x16bf16_1k(a, b, c, 0, 0, 0)
#define LGKM0() __builtin_amdgcn_s_waitcnt(0xC07F)
#define VMW(N) asm volatile("s_waitcnt vmcnt(" #N ")" ::: "memory")
#define BAR() do { LGKM0(); asm volatile("s_barrier" ::: "memory"); } while (0)
#define MFMA_DONE() do { asm volatile("s_nop 7\n\ts_nop 3" ::: "memory"); SBAR(); } while (0)
template <int... Is> using iseq = std::integer_sequence<int, Is...>;
template <int N> using mkseq = std::make_integer_sequence<int, N>;
template <int N> using ic = std::integral_constant<int, N>;

constexpr int NB = 8, SQ = 208, DM = 640, H = 32, SK = 1024, DH = 128, HD = H * DH, NRT = 13;
constexpr unsigned SLOT = 16384, XST = 26624, XOFF = 5 * SLOT, Q2OFF = 6 * SLOT, MISC = Q2OFF + NRT * 4096, LDS_TOTAL = 163840;
static_assert(XOFF + 3 * XST <= LDS_TOTAL && MISC + 3 * 4096 == LDS_TOTAL, "lds map");
constexpr unsigned OBLK = NRT * 4096;
constexpr unsigned PST = 47104, POFF = 26624;
constexpr unsigned PS0 = LDS_TOTAL - PST, PS1 = 0, PS2 = PST, SCR2 = PST;
static_assert(SCR2 + 32768 + 512 <= 5 * SLOT && PS2 + PST <= PS0, "lds map 2");
constexpr size_t WS_CNT = 0, CNT_BYTES = 65536, WS_O = 1u << 20, WS_PART = 32u << 20;
constexpr unsigned PARTB = 8 * 11264;
constexpr unsigned PACE = 131, PFAST = PACE - 16, PCREDIT = 100;
constexpr int PACE_END = 29;
constexpr float QSCALE = 0.08838834764831845f * 1.4426950408889634f;
constexpr float L_LIMIT = 1.0e24f;
__host__ __device__ constexpr unsigned q1addr(int rt) { return rt < 8 ? 4 * SLOT + 4096 * rt : rt < 11 ? MISC + 4096 * (rt - 8) : SLOT + 4096 * (rt - 11); }

DI unsigned cvtpk(float lo, float hi) { f32x2 v = {lo, hi}; return __builtin_bit_cast(unsigned, __builtin_convertvector(v, bf16x2)); }
DI float ffma_s(float a, float b, float c) { float r = __builtin_fmaf(a, b, c); PIN(r); return r; }
DI float ex2(float x) { return __builtin_amdgcn_exp2f(x); }
DI bf16x8 rd128(unsigned base, int imm) { return *(const LAS bf16x8*)((const LAS char*)base + imm); }
DI s16x4 rdtr(unsigned base, int imm) { return __builtin_amdgcn_ds_read_tr16_b64_v4i16((LAS s16x4*)((LAS char*)base + imm)); }
DI bf16x8 cat(s16x4 lo, s16x4 hi) { return __builtin_shufflevector(lo, hi, 0, 1, 2, 3, 4, 5, 6, 7); }
DI void wr128f(unsigned base, int imm, f32x4 v) { *(LAS f32x4*)((LAS char*)base + imm) = v; }
DI f32x4 rd128f(unsigned base, int imm) { return *(const LAS f32x4*)((const LAS char*)base + imm); }
DI float red_add(float x) {
  auto a = __builtin_amdgcn_permlane16_swap(__float_as_uint(x), __float_as_uint(x), false, false); x = __uint_as_float(a[0]) + __uint_as_float(a[1]);
  auto b = __builtin_amdgcn_permlane32_swap(__float_as_uint(x), __float_as_uint(x), false, false); return __uint_as_float(b[0]) + __uint_as_float(b[1]); }
DI float red_max(float x) {
  auto a = __builtin_amdgcn_permlane16_swap(__float_as_uint(x), __float_as_uint(x), false, false); x = fmaxf(__uint_as_float(a[0]), __uint_as_float(a[1]));
  auto b = __builtin_amdgcn_permlane32_swap(__float_as_uint(x), __float_as_uint(x), false, false); return fmaxf(__uint_as_float(b[0]), __uint_as_float(b[1])); }
DI int lane_id() { int l; asm volatile("v_mbcnt_lo_u32_b32 %0, -1, 0\n\tv_mbcnt_hi_u32_b32 %0, -1, %0" : "=v"(l)); return l; }
DI int opq(int x) { asm volatile("" : "+s"(x)); return x; }
DI rsrc_t mkrs(const void* p, unsigned bytes) { return __builtin_amdgcn_make_buffer_rsrc((void*)p, 0, bytes, 0x00020000); }

template <int IMM> DI void dma_nt(unsigned m0v, unsigned voff, rsrc_t rs, unsigned soff) {
  asm volatile("s_mov_b32 m0, %0\n\ts_nop 0\n\tbuffer_load_dwordx4 %1, %2, %3 offen offset:%4 nt lds" ::"s"(m0v), "v"(voff), "s"(rs), "s"(soff), "i"(IMM) : "memory");
}
DI void dma_df(unsigned m0v, unsigned voff, rsrc_t rs, unsigned soff) {
  asm volatile("s_mov_b32 m0, %0\n\ts_nop 0\n\tbuffer_load_dwordx4 %1, %2, %3 offen lds" ::"s"(m0v), "v"(voff), "s"(rs), "s"(soff) : "memory");
}
DI void touch_code(unsigned dump, unsigned long long pc, int idx) {
  int len;
  asm volatile("s_mov_b32 %0, kk_code_end-kk_fused" : "=s"(len));
  const rsrc_t rs = __builtin_amdgcn_make_buffer_rsrc((void*)pc, 0, len > 0 && len < (1 << 17) ? len : 0, 0x00020000);
  const unsigned l = lane_id(), vo = l < 18 ? 128 * (l + 18 * idx) : 0x80000000u;
  asm volatile("s_mov_b32 m0, %0\n\ts_nop 3\n\tbuffer_load_dword %1, %2, 0 offen lds" ::"s"(dump), "v"(vo), "s"(rs) : "memory");
}
DI void dma_sc1(unsigned m0v, unsigned voff, rsrc_t rs, unsigned soff) {
  asm volatile("s_mov_b32 m0, %0\n\ts_nop 0\n\tbuffer_load_dwordx4 %1, %2, %3 offen sc1 lds" ::"s"(m0v), "v"(voff), "s"(rs), "s"(soff) : "memory");
}
DI void touch80(unsigned dump, const char* p) {
  const unsigned vo = 128 * lane_id();
  asm volatile("s_mov_b32 m0, %0\n\ts_nop 3\n\tbuffer_load_dword %1, %2, 0 offen lds\n\tbuffer_load_dword %1, %2, %3 offen lds" ::"s"(dump), "v"(vo), "s"(mkrs(p, 10240)), "s"(2048) : "memory");
}
DI void mfma2(f32x4& c, const bf16x8& a0, const bf16x8& b0, const bf16x8& a1, const bf16x8& b1) {
  asm volatile("v_mfma_f32_16x16x32_bf16 %0, %1, %2, %0\n\tv_mfma_f32_16x16x32_bf16 %0, %3, %4, %0" : "+v"(c) : "v"(a0), "v"(b0), "v"(a1), "v"(b1));
}
template <bool SC1> DI void mfma2d(f32x4& c, const bf16x8& a0, const bf16x8& b0, const bf16x8& a1, const bf16x8& b1, unsigned m0v, unsigned voff, rsrc_t rs, unsigned soff) {
  if constexpr (SC1)
    asm volatile("v_mfma_f32_16x16x32_bf16 %0, %1, %2, %0\n\ts_mov_b32 m0, %5\n\tv_mfma_f32_16x16x32_bf16 %0, %3, %4, %0\n\tbuffer_load_dwordx4 %6, %7, %8 offen sc1 lds"
                 : "+v"(c) : "v"(a0), "v"(b0), "v"(a1), "v"(b1), "s"(m0v), "v"(voff), "s"(rs), "s"(soff) : "memory");
  else
    asm volatile("v_mfma_f32_16x16x32_bf16 %0, %1, %2, %0\n\ts_mov_b32 m0, %5\n\tv_mfma_f32_16x16x32_bf16 %0, %3, %4, %0\n\tbuffer_load_dwordx4 %6, %7, %8 offen lds"
                 : "+v"(c) : "v"(a0), "v"(b0), "v"(a1), "v"(b1), "s"(m0v), "v"(voff), "s"(rs), "s"(soff) : "memory");
}
DI void mfma1(f32x4& c, const bf16x8& a, const bf16x8& b) { asm volatile("v_mfma_f32_16x16x32_bf16 %0, %1, %2, %0" : "+v"(c) : "v"(a), "v"(b)); }
typedef unsigned long long u64;
constexpr int SPIN_MAX = 1 << 16;
constexpr u64 HS_K = 0x9E3779B97F4A7C15ull;
struct HS { char* base; unsigned in, out; u32x2 req, ack; };
DI u64 j64(u32x2 v) { return ((u64)v[1] << 32) | v[0]; }
DI void hs_peek(const HS& h, int gid, unsigned dump) {
  const unsigned l = lane_id(), off = l < 24 ? 256 * gid + 4 * l : 0x80000000u;
  asm volatile("s_mov_b32 m0, %0\n\ts_nop 3\n\tbuffer_load_dword %1, %2, 0 offen sc1 lds" ::"s"(dump), "v"(off), "s"(mkrs(h.base, CNT_BYTES)) : "memory");
}
DI u32x2 hs_peeked(unsigned dump) { return *(const LAS u32x2*)(dump + 8 * (lane_id() & 15)); }
DI u32x2 hs_load_now(const HS& h, unsigned off) { asm volatile("" ::: "memory"); const u32x2 v = __builtin_amdgcn_raw_buffer_load_b64(mkrs(h.base, CNT_BYTES), off, 0, 16); __builtin_amdgcn_s_waitcnt(0x0F70); return v; }
DI void hs_store(const HS& h, unsigned off, u32x2 v) { __builtin_amdgcn_raw_buffer_store_b64(v, mkrs(h.base, CNT_BYTES), off, 0, 16); }
DI unsigned hs_sel(unsigned off, int phase) { return (lane_id() >> 2) == phase ? off : 0x80000000u; }
DI void hs_signal(const HS& h, int phase) { hs_store(h, hs_sel(h.out, phase), h.ack); }
DI void hs_wait(const HS& h, int phase, u32x2 v) {
  asm volatile("" : "+v"(v)::"memory");
  const bool other = (lane_id() >> 2) != phase || h.in == h.out;
#pragma nounroll
  for (int spin = 0; spin < SPIN_MAX && !__all(other || j64(v) == j64(h.req)); ++spin) { __builtin_amdgcn_s_sleep(1); v = hs_load_now(h, h.in); }
  asm volatile("" ::: "memory");
}

struct Args { const u16 *x0, *k0, *v0, *wq0, *wo0, *x1, *k1, *v1, *wq1, *wo1; char* ws; u16* out; };
struct Ctx {
  const u16 *x, *k, *v, *wq; const char *wo, *warm; char *oq, *part; u16* out; char* hs; unsigned ldsb; int own, nt, kq, idx, gid;
};

DI Ctx make_ctx() {
  extern __shared__ __attribute__((aligned(16))) char lds[];
  const __attribute__((address_space(4))) Args* ap = (const __attribute__((address_space(4))) Args*)__builtin_amdgcn_kernarg_segment_ptr();
  int bid = blockIdx.x;
  asm volatile("" : "+s"(ap), "+s"(bid));
  const int xcd = bid & 7, idx = bid >> 3, br = xcd >> 2, kq = 2 * (xcd & 1) + ((idx >> 2) & 1), b = 4 * ((xcd >> 1) & 1) + (idx >> 3), hp = 4 * kq + (idx & 3);
  Ctx c;
  c.idx = idx;
  c.ldsb = (unsigned)(size_t)(LAS char*)lds;
  c.x = (br ? ap->x1 : ap->x0) + (size_t)b * SQ * DM;
  c.k = (br ? ap->k1 : ap->k0) + ((size_t)b * H + 2 * hp) * SK * DH;
  c.v = (br ? ap->v1 : ap->v0) + ((size_t)b * H + 2 * hp) * SK * DH;
  c.wq = (br ? ap->wq1 : ap->wq0) + 2 * hp * DH;
  const char* wo = (const char*)(br ? ap->wo1 : ap->wo0);
  c.wo = wo + (size_t)kq * 1024 * DM * 2;
  c.warm = wo + (size_t)((xcd & 3) * 32 + idx) * 40960;
  c.oq = ap->ws + WS_O + (size_t)((br * NB + b) * H + 8 * kq) * OBLK;
  c.part = ap->ws + WS_PART + (size_t)((br * NB + b) * 16) * PARTB;
  c.out = ap->out + (size_t)b * SQ * (2 * DM) + br * DM;
  c.kq = kq; c.hs = ap->ws + WS_CNT; c.gid = (br * NB + b) * 16 + hp;
  c.own = hp & 3; c.nt = hp & 3;
  return c;
}

DI void slow16(const u16* Kg, const u16* Vg, int key0, int nstep, const bf16x8 (&q)[4], f32x4 (&o)[8], float& m, float& l) {
  const int lane = lane_id(), r = lane & 15, g = lane >> 4;
  m = -3.0e38f; l = 0.f;
#pragma unroll
  for (int d = 0; d < 8; ++d) o[d] = f32x4{};
#pragma nounroll
  for (int st = 0; st < nstep; ++st) {
    const int kb = nstep == 64 ? 16 * st : key0 + 128 * (st >> 1) + 16 * (st & 1);
    f32x4 S = {};
#pragma unroll
    for (int ks = 0; ks < 4; ++ks) S = MFMA16(*(const bf16x8*)(Kg + (size_t)(kb + r) * DH + 32 * ks + 8 * g), q[ks], S);
    const float mx = red_max(fmaxf(fmaxf(S[0], S[1]), fmaxf(S[2], S[3])));
    const float mn = fmaxf(m, mx), al = ex2(m - mn); m = mn;
    float ps = 0.f;
#pragma unroll
    for (int i = 0; i < 4; ++i) { S[i] = ex2(S[i] - mn); ps += S[i]; }
    l = l * al + ps;
    u32x2 pw = {cvtpk(S[0], S[1]), cvtpk(S[2], S[3])};
#pragma unroll
    for (int d = 0; d < 8; ++d) {
      s16x4 vf;
#pragma unroll
      for (int j = 0; j < 4; ++j) vf[j] = (short)Vg[(size_t)(kb + 4 * g + j) * DH + 16 * d + r];
      o[d] *= al;
      o[d] = MFMA16K16(vf, __builtin_bit_cast(s16x4, pw), o[d]);
    }
  }
  __builtin_amdgcn_s_waitcnt(0);
}
DI float nanprobe_s(const f32x4 (&o)[8]) {
  float t = 0.f; const float z = 0.f;
#pragma unroll
  for (int d = 0; d < 8; ++d) { t = ffma_s(o[d][0], z, t); t = ffma_s(o[d][1], z, t); t = ffma_s(o[d][2], z, t); t = ffma_s(o[d][3], z, t); }
  return t;
}
DI float nanprobe(const f32x4 (&o)[8]) {
  f32x4 t = {};
#pragma unroll
  for (int d = 0; d < 8; ++d) t += o[d] * 0.f;
  return (t[0] + t[1]) + (t[2] + t[3]);
}

template <int NR, class FD, class FW> DI void qproj(const Ctx& c, int cg, FD&& dma, FW&& wait) {
  f32x4 acc[NR][4]; bf16x8 w[4][2], x[NR][2];
  const unsigned lane = lane_id(), r16 = lane & 15, g = lane >> 4, q4 = (lane & 15) >> 2, p = lane & 3;
  const unsigned wb0 = c.ldsb + 1024 * (cg & 1) + 2048 * g + 64 * q4 + 16 * ((p >> 1) ^ (2 * (g & 1))) + 8 * (p & 1);
  const unsigned xb0 = c.ldsb + XOFF + (NR == 7 ? 0 : 7 * 2048) + 128 * r16 + 16 * (g ^ ((r16 >> 1) & 7));
#pragma unroll
  for (int r = 0; r < NR; ++r)
#pragma unroll
    for (int i = 0; i < 4; ++i) { acc[r][i] = f32x4{}; PIN(acc[r][i]); }
  unsigned xs = 0, ws = SLOT * (cg >> 1), xp0, xp1, wp0, wp1;
  auto ldw = [&](int i) { const unsigned b = (i & 1) ? wp1 : wp0; const int im = 512 * (i >> 1);
    w[i][0] = cat(rdtr(b, im), rdtr(b, im + 256)); w[i][1] = cat(rdtr(b, im + 8192), rdtr(b, im + 8448)); };
  auto ldx = [&](int r) { x[r][0] = rd128(xp0, 2048 * r); x[r][1] = rd128(xp1, 2048 * r); };
  auto row = [&](int r) {
#pragma unroll
    for (int i = 0; i < 4; ++i) mfma2(acc[r][i], w[i][0], x[r][0], w[i][1], x[r][1]);
  };
  auto half = [&](int r, int h) { mfma2(acc[r][2 * h], w[2 * h][0], x[r][0], w[2 * h][1], x[r][1]); mfma2(acc[r][2 * h + 1], w[2 * h + 1][0], x[r][0], w[2 * h + 1][1], x[r][1]); };
  auto qaddr = [&](int r) { const int rt = (NR == 7 ? 0 : 7) + r; const unsigned ln = lane_id(), rr = ln & 15, gg = ln >> 4;
    return c.ldsb + 2048 * (cg & 1) + 128 * rr + 16 * ((gg >> 1) ^ ((rr >> 1) & 7)) + 8 * (gg & 1) + ((cg >> 1) ? Q2OFF + 4096 * rt : q1addr(rt)); };
  auto rowput = [&](int r, int pr, unsigned qa) {
#pragma unroll
    for (int i = 0; i < 4; ++i) {
      float m0 = 0.f, m1 = 0.f, m2 = 0.f;
      if (r >= 0) mfma1(acc[r][i], w[i][0], x[r][0]);
      SBAR();
      if (pr >= 0) { m0 = acc[pr][i][0] * QSCALE; PIN(m0); m1 = acc[pr][i][1] * QSCALE; PIN(m1); m2 = acc[pr][i][2] * QSCALE; PIN(m2); }
      SBAR();
      if (r >= 0) mfma1(acc[r][i], w[i][1], x[r][1]);
      SBAR();
      if (pr >= 0) { float m3 = acc[pr][i][3] * QSCALE; PIN(m3); *(LAS u32x2*)(qa ^ (32 * i)) = u32x2{cvtpk(m0, m1), cvtpk(m2, m3)}; }
      SBAR();
    }
  };
  auto step = [&](auto K, auto F, int s) {
    constexpr bool FIRST = decltype(F)::value;
    xp0 = xb0 + xs; xp1 = xp0 ^ 64; wp0 = wb0 + ws; wp1 = wp0 ^ 32;
    xs = xs == 2 * XST ? 0 : xs + XST; ws += 2 * SLOT; ws = ws >= 5 * SLOT ? ws - 5 * SLOT : ws;
    if constexpr (NR == 7) {
      wait(s);
      asm volatile("" : "+v"(xp0), "+v"(xp1));
      SBAR();
      BAR();
      SBAR();
#pragma unroll
      for (int r = 0; r < 7; ++r) {
        if constexpr (!FIRST) half(r, 0);
        SBAR(); if (r) x[r - 1][0] = rd128(xp0, 2048 * (r - 1)); SBAR();
        if constexpr (!FIRST) half(r, 1);
        SBAR(); if (r) x[r - 1][1] = rd128(xp1, 2048 * (r - 1)); SBAR();
      }
      ldx(6);
#pragma unroll
      for (int i = 0; i < 4; ++i) { ldw(i); dma(K, s, 2 * i); if (i < 3) dma(K, s, 2 * i + 1); SBAR(); }
    } else {
#pragma unroll
      for (int i = 0; i < 4; ++i) { dma(K, s, i); ldw(i); SBAR(); }
#pragma unroll
      for (int r = 0; r < 6; ++r) { if (r < 4) dma(K, s, 4 + r); ldx(r); SBAR(); }
      if constexpr (decltype(K)::value == 2) {
        unsigned qa[6];
#pragma unroll
        for (int r = 1; r < 6; ++r) { qa[r] = qaddr(r); PIN(qa[r]); }
        LGKM0(); SBAR();
        rowput(1, -1, 0); rowput(2, 1, qa[1]); rowput(3, 2, qa[2]); rowput(4, 3, qa[3]); rowput(5, 4, qa[4]); rowput(0, 5, qa[5]);
      } else {
#pragma unroll
        for (int r = 0; r < 5; ++r) row(r);
        half(5, 0);
        wait(s + 1);
        SBAR();
        half(5, 1);
      }
      asm volatile("s_barrier" ::: "memory");
      SBAR();
    }
  };
  asm volatile("s_nop 1" ::: "memory");
  if constexpr (NR == 6) { wait(0); BAR(); }
  step(ic<NR == 7 ? 0 : 3>{}, ic<1>{}, 0);
#pragma nounroll
  for (int s = 1; s < 8; ++s) step(ic<0>{}, ic<0>{}, s);
  step(ic<1>{}, ic<0>{}, 8);
  step(ic<2>{}, ic<0>{}, 9);
  if constexpr (NR == 7) {
    unsigned qa = qaddr(0); PIN(qa);
    BAR();
    SBAR();
#pragma unroll
    for (int r = 0; r < 7; ++r) rowput(r, r - 1, qa + 4096 * (r - 1));
    MFMA_DONE();
    rowput(-1, 6, qa + 4096 * 6);
  } else {
    dma(ic<4>{}, 10, 0);
    MFMA_DONE();
    rowput(-1, 0, qaddr(0));
  }
}

struct OD { rsrc_t rw, ro; unsigned vwp, vwo, vo, mw, mo, lo; int rg; };
DI OD od_make(const Ctx& c, int rg) {
  OD d; const unsigned lane = lane_id(), xr = 16 * ((lane & 3) ^ (((lane >> 4) & 1) << 1));
  d.rw = mkrs(c.wo, 1024 * DM * 2); d.ro = mkrs(c.oq, 8 * OBLK);
  d.vwp = 1280 * (16 * rg + ((lane >> 2) & 7)) + 64 * (lane >> 5) + xr + 320 * c.nt + ((c.nt & 1) ? 64 : 0);
  d.vwo = 1280 * (16 * rg + (lane >> 2)) + xr + 320 * c.nt + ((c.nt & 1) ? 0 : 256);
  d.vo = 16 * lane; d.mw = c.ldsb + POFF + 5120 * rg; d.mo = c.ldsb + 1024 * rg; d.lo = 1024 * (24 + (rg & 1)); d.rg = rg;
  return d;
}
DI unsigned od_blk(int s) { return (unsigned)(s < 16 ? 4 * ((s >> 1) & 3) + 2 * (s >> 3) + (s & 1) : 64); }
template <class F> DI void od_piece(const Ctx& c, const OD& d, unsigned u, unsigned st, int n, F&& f) {
  const unsigned sw = u * (64 * 1280), so = u * (OBLK / 2), m = d.mw + st;
  if (n == 0) f(ic<0>{}, m, d.vwp, d.rw, sw);
  else if (n == 1) f(ic<0>{}, m + 1024, d.vwp, d.rw, sw + 128);
  else if (n == 2) f(ic<0>{}, m + 3072, d.vwp, d.rw, sw + 10240);
  else if (n == 3) f(ic<0>{}, m + 4096, d.vwp, d.rw, sw + 10240 + 128);
  else if (n == 4) f(ic<0>{}, m + 2048, d.vwo, d.rw, sw);
  else if (n < 11) f(ic<1>{}, d.mo + st + 4096 * (n - 5), d.vo, d.ro, so + 1024 * d.rg + 4096 * (n - 5));
  else f(ic<1>{}, c.ldsb + d.lo + st, d.vo, d.ro, so + d.lo);
}
DI void od_piece(const Ctx& c, const OD& d, unsigned u, unsigned st, int n) {
  od_piece(c, d, u, st, n, [](auto S, unsigned m, unsigned v, rsrc_t r, unsigned so) { if constexpr (decltype(S)::value) dma_sc1(m, v, r, so); else dma_df(m, v, r, so); });
}
template <int NC, int NR> DI void oproj(int rg, HS h) {
  const Ctx c = make_ctx();
  h.base = c.hs;
  const OD d = od_make(c, rg);
  f32x4 acc[NR][NC]; bf16x8 w[NC][2], x[NR][2];
  const int c0 = NC == 3 ? 3 * (rg >> 1) : 6 + 2 * (rg >> 1), r0 = NR == 7 ? 0 : 7;
  unsigned wb[NC], xb;
  {
    const unsigned lane = lane_id(), g = lane >> 4, q4 = (lane & 15) >> 2, p = lane & 3;
    const unsigned in = 64 * (4 * (g & 1) + q4) + 16 * ((p >> 1) ^ (2 * (g & 1))) + 8 * (p & 1);
#pragma unroll
    for (int i = 0; i < NC; ++i) {
      const int ct = c0 + i;
      const unsigned off = ct < 8 ? 512 * (ct >> 1) + 3072 * (g >> 1) : 2048 + 512 * (g >> 1);
      wb[i] = c.ldsb + POFF + off + (in ^ (32 * (ct & 1)));
    }
    xb = c.ldsb + 16 * lane + 2048 * r0;
  }
#pragma unroll
  for (int r = 0; r < NR; ++r)
#pragma unroll
    for (int i = 0; i < NC; ++i) { acc[r][i] = f32x4{}; PIN(acc[r][i]); }
  unsigned st = PS0, nx = PS1, fr = PS2;
  auto ldw = [&](int i) { const unsigned wp = wb[i] + st; w[i][0] = cat(rdtr(wp, 0), rdtr(wp, 5120)); w[i][1] = cat(rdtr(wp, 10240), rdtr(wp, 15360)); };
  auto ldx = [&](int r) { const unsigned xp = xb + st; x[r][0] = rd128(xp, 2048 * r); x[r][1] = rd128(xp, 2048 * r + 1024); };
  auto row = [&](int r) {
#pragma unroll
    for (int i = 0; i < NC; ++i) mfma2(acc[r][i], w[i][0], x[r][0], w[i][1], x[r][1]);
  };
  const bool me = NC == 2 && NR == 6 && rg == 0;
  auto tile_d = [&](int r, int i, unsigned u, int n) {
    od_piece(c, d, u, fr, n, [&](auto S, unsigned m, unsigned v, rsrc_t rs, unsigned so) { mfma2d<decltype(S)::value != 0>(acc[r][i], w[i][0], x[r][0], w[i][1], x[r][1], m, v, rs, so); });
  };
  if constexpr (NC == 2) { VMW(13); BAR(); }
  if constexpr (NC == 3) {
#pragma unroll
    for (int r = 0; r < NR; ++r) ldx(r);
#pragma unroll
    for (int i = 0; i < 3; ++i) ldw(i);
#pragma unroll
    for (int n = 0; n < 7; ++n) od_piece(c, d, od_blk(2), fr, n < 5 ? n : n + 5);
    st = PS1; nx = PS2; fr = PS0;
  }
#pragma nounroll
  for (int s = NC == 3 ? 1 : 0; s < 16; ++s) {
    unsigned u = od_blk(s + 2), xp = xb + st;
    if constexpr (NC == 3) {
      asm volatile("" : "+s"(u), "+v"(xp));
      SBAR();
      BAR();
      SBAR();
#pragma unroll
      for (int r = 0; r < NR; ++r) {
        mfma2(acc[r][0], w[0][0], x[r][0], w[0][1], x[r][1]);
        SBAR(); if (r) x[r - 1][0] = rd128(xp, 2048 * (r - 1)); SBAR();
        if (NR == 6 && r == 0) tile_d(0, 1, u, 11); else mfma2(acc[r][1], w[1][0], x[r][0], w[1][1], x[r][1]);
        SBAR(); if (r) x[r - 1][1] = rd128(xp, 2048 * (r - 1) + 1024); SBAR();
        tile_d(r, 2, u, r < 5 ? r : r + 5);
        SBAR();
      }
      ldx(NR - 1);
#pragma unroll
      for (int i = 0; i < 3; ++i) ldw(i);
      SBAR();
      VMW(7);
    } else {
      ldw(0); ldx(0); ldw(1);
#pragma unroll
      for (int r = 1; r < NR; ++r) ldx(r);
      SBAR();
      if constexpr (NR == 6) {
        if (me && s == 2) hs_signal(h, 1);
        if (me && s == 5) hs_wait(h, 1, hs_peeked(c.ldsb + Q2OFF + 1024));
        if (me && s == 4) hs_peek(h, c.gid, c.ldsb + Q2OFF + 1024);
      }
      LGKM0();
      SBAR();
#pragma unroll
      for (int r = 0; r < NR; ++r) {
        if (r < 5) tile_d(r, 0, u, 5 + r); else mfma2(acc[r][0], w[0][0], x[r][0], w[0][1], x[r][1]);
        if (r == NR - 1) VMW(5);
        mfma2(acc[r][1], w[1][0], x[r][0], w[1][1], x[r][1]);
        SBAR();
      }
      asm volatile("s_barrier" ::: "memory");
      SBAR();
    }
    const unsigned t = st; st = nx; nx = fr; fr = t;
  }
  if constexpr (NC == 3) {
    BAR(); SBAR();
#pragma unroll
    for (int r = 0; r < NR; ++r) row(r);
  }
  MFMA_DONE();
  constexpr int NT = NR * NC, NU = (NT + 1) / 2;
  const int wv = NC == 3 ? rg : 4 + rg;
  const unsigned ln = lane_id(), pv0 = 16 * ln + 11264 * wv;
  const rsrc_t rp = mkrs(c.part, 16 * PARTB);
  f32x4 own[3][2];
  auto scatter = [&](auto K) {
    constexpr int k = decltype(K)::value;
#pragma unroll
    for (int m = 0; m < NU; ++m) {
      const f32x4 u = acc[(2 * m) / NC][(2 * m) % NC], v = 2 * m + 1 < NT ? acc[(2 * m + 1) / NC][(2 * m + 1) % NC] : f32x4{};
      if ((m & 3) == k) { own[m >> 2][0] = u; own[m >> 2][1] = v; }
      else __builtin_amdgcn_raw_buffer_store_b128(u32x4{cvtpk(u[0], u[1]), cvtpk(u[2], u[3]), cvtpk(v[0], v[1]), cvtpk(v[2], v[3])}, rp, pv0 + (4 * k + c.nt) * PARTB + 1024 * m, 0, 16);
    }
#pragma unroll
    for (int e = 0; e < 3; ++e) if (4 * e + k >= NU) { own[e][0] = f32x4{}; own[e][1] = f32x4{}; }
  };
  if (c.kq == 0) scatter(ic<0>{}); else if (c.kq == 1) scatter(ic<1>{}); else if (c.kq == 2) scatter(ic<2>{}); else scatter(ic<3>{});
  VMW(0);
  BAR();
  if (me) { hs_signal(h, 2); hs_wait(h, 2, hs_load_now(h, h.in)); }
  BAR();
  u32x4 pp[3][3];
#pragma unroll
  for (int e = 0; e < 3; ++e)
#pragma unroll
    for (int q = 1; q < 4; ++q) {
      const int m = 4 * e + c.kq;
      pp[e][q - 1] = __builtin_amdgcn_raw_buffer_load_b128(rp, m < NU ? pv0 + (4 * ((c.kq + q) & 3) + c.nt) * PARTB + 1024 * m : 0x80000000u, 0, 16);
    }
  const rsrc_t rout = mkrs(c.out, (SQ - 1) * 4 * DM + 2 * DM);
#pragma unroll
  for (int e = 0; e < 3; ++e) {
    unsigned pk[2][2], off[2];
#pragma unroll
    for (int h = 0; h < 2; ++h) {
      const int n = 2 * (4 * e + c.kq) + h;
      f32x4 v = own[e][h];
#pragma unroll
      for (int q = 0; q < 3; ++q) {
        v[0] += __uint_as_float(pp[e][q][2 * h] << 16); v[1] += __uint_as_float(pp[e][q][2 * h] & 0xffff0000u);
        v[2] += __uint_as_float(pp[e][q][2 * h + 1] << 16); v[3] += __uint_as_float(pp[e][q][2 * h + 1] & 0xffff0000u);
      }
      pk[h][0] = cvtpk(v[0], v[1]); pk[h][1] = cvtpk(v[2], v[3]);
      const int rt = r0 + n / NC, ct = c0 + n % NC;
      const int col = 160 * c.nt + (ct < 8 ? ((c.nt & 1) ? 32 : 0) + 16 * ct : ((c.nt & 1) ? 0 : 128) + 16 * (ct - 8));
      off[h] = n < NT ? 2 * (16 * rt * (2 * DM) + col) : 0x80000000u;
    }
    auto s0 = __builtin_amdgcn_permlane16_swap(pk[0][0], pk[1][0], false, false); auto s1 = __builtin_amdgcn_permlane16_swap(pk[0][1], pk[1][1], false, false);
    __builtin_amdgcn_raw_buffer_store_b128(u32x4{s0[0], s1[0], s0[1], s1[1]}, rout, ((ln & 16) ? off[1] : off[0]) + (ln & 15) * (4 * DM) + 16 * (ln >> 5), 0, 0);
  }
}

struct AS {
  f32x4 o[3][8], S[2][3], negm, l[3]; bf16x8 q[3][4], F[2][4], ones; u32x4 P[2][3];
  unsigned kp[2], vp[2], kn[2];
};
template <int N, int I> DI void a_frag(AS& a) {
  constexpr int sub = (N >> 2) & 1, b = N & 3;
  if constexpr ((b & 1) == 0) {
    constexpr int kt = 2 * sub + (b >> 1);
    a.F[N & 1][I] = rd128(N == 8 ? a.kn[I & 1] : a.kp[I & 1], 8192 * (I >> 1) + 2048 * kt);
  } else {
    constexpr int cc = 4 * (b >> 1) + I, imm = 8192 * sub + 512 * (cc >> 1);
    a.F[N & 1][I] = cat(rdtr(a.vp[cc & 1], imm), rdtr(a.vp[cc & 1], imm + 4096));
  }
}
#define LGKM(n) __builtin_amdgcn_s_waitcnt(0xC07F | ((n) << 8))
template <int BLK, int K> DI void sm_exp(AS& a) { constexpr int b = BLK & 3, sb = (b == 1 || b == 2) ? 0 : 1, e = ((b & 1) ? 0 : 6) + K;
  a.S[sb][e >> 2][e & 3] = ex2(a.S[sb][e >> 2][e & 3]); PIN(a.S[sb][e >> 2]); }
template <int BLK, int K> DI void sm_cvt(AS& a) { constexpr int b = BLK & 3, sub = (BLK >> 2) & 1, sb = (b == 1 || b == 2) ? 0 : 1, e = ((b & 1) ? 0 : 6) + 2 * K, pb = b == 0 ? sub ^ 1 : sub, rt = e >> 2;
  a.P[pb][rt][2 * sb + ((e & 3) >> 1)] = cvtpk(a.S[sb][rt][e & 3], a.S[sb][rt][(e & 3) + 1]); PIN(a.P[pb][rt]); }
template <int N, bool MM, bool SM, bool CY, int NX, bool Z, int G> DI void a_gap(AS& a) {
  constexpr int sub = N >> 2, b = N & 3; constexpr bool QK = (b & 1) == 0;
  if constexpr (MM) {
    if constexpr (QK) {
      constexpr int rt = G >> 2, ks = G & 3, sb = b >> 1;
      if constexpr (ks == 0) { if constexpr (Z) a.S[sb][rt] = MFMA16(a.F[N & 1][0], a.q[rt][0], f32x4{}); else a.S[sb][rt] = MFMA16(a.F[N & 1][0], a.q[rt][0], a.negm); }
      else a.S[sb][rt] = MFMA16(a.F[N & 1][ks], a.q[rt][ks], a.S[sb][rt]);
    } else if constexpr (G < 12) {
      constexpr int i = G / 3, rt = G % 3, cc = 4 * (b >> 1) + i;
      if constexpr (Z) a.o[rt][cc] = MFMA16(a.F[N & 1][i], __builtin_bit_cast(bf16x8, a.P[sub ^ 1][rt]), f32x4{});
      else a.o[rt][cc] = MFMA16(a.F[N & 1][i], __builtin_bit_cast(bf16x8, a.P[sub ^ 1][rt]), a.o[rt][cc]);
    } else {
      constexpr int rt = b == 1 ? G - 12 : 2;
      if constexpr (Z) a.l[rt] = MFMA16(a.ones, __builtin_bit_cast(bf16x8, a.P[sub ^ 1][rt]), f32x4{});
      else a.l[rt] = MFMA16(a.ones, __builtin_bit_cast(bf16x8, a.P[sub ^ 1][rt]), a.l[rt]);
    }
    SBAR();
  }
  constexpr int LAST = QK ? 11 : b == 1 ? 13 : 12;
  if constexpr (G == LAST) { if constexpr (QK) LGKM(4); else LGKM(0); }
  if constexpr (G == 5 && !QK) { if constexpr (NX >= 0) LGKM(3); else LGKM(0); }
  SBAR();
  if constexpr (NX >= 0 && (G & 1) == 0 && G < 8) a_frag<NX, G / 2>(a);
  SBAR();
  if constexpr ((G & 1) && G < 12) { if constexpr (SM) sm_exp<N, G / 2>(a); }
  else if constexpr (QK) {
    if constexpr (SM && G >= 8) sm_cvt<N, G / 2 - 4>(a);
  } else {
    if constexpr (G == 0 && CY) sm_cvt<N - 1, 2>(a);
    if constexpr (SM && G >= 8 && G <= 12 && (G & 1) == 0) sm_cvt<N, G / 2 - 4>(a);
  }
  SBAR();
}
template <int N, bool MM, bool SM, bool CY, int NX, bool Z, int... Gs> DI void a_block_(AS& a, iseq<Gs...>) { (a_gap<N, MM, SM, CY, NX, Z, Gs>(a), ...); }
template <int N, bool MM, bool SM, bool CY, int NX, bool Z = false> DI void a_block(AS& a) { a_block_<N, MM, SM, CY, NX, Z>(a, mkseq<(N & 1) == 0 ? 12 : (N & 3) == 1 ? 14 : 13>{}); }
template <bool FIRST> DI void a_period(AS& a) {
  if constexpr (FIRST) {
    a_block<0, true, false, false, -1, true>(a);
    float mx = 3.0e38f;
#pragma unroll
    for (int rt = 0; rt < 3; ++rt) mx = fminf(mx, red_max(fmaxf(fmaxf(a.S[0][rt][0], a.S[0][rt][1]), fmaxf(a.S[0][rt][2], a.S[0][rt][3]))));
    a.negm = f32x4{-mx, -mx, -mx, -mx}; PIN(a.negm);
#pragma unroll
    for (int rt = 0; rt < 3; ++rt) a.S[0][rt] -= mx;
    SBAR();
    a_block<1, false, true, false, 2>(a); a_block<2, true, true, true, -1>(a); a_block<3, false, true, true, 4>(a);
  } else {
    a_block<0, true, true, true, 1>(a); a_block<1, true, true, true, 2>(a); a_block<2, true, true, true, 3>(a); a_block<3, true, true, true, 4>(a);
  }
  a_block<4, true, true, true, 5>(a); a_block<5, true, true, true, 6, FIRST>(a); a_block<6, true, true, true, 7>(a);
  if constexpr (FIRST) VMW(0);
  BAR();
  a_block<7, true, true, true, 8, FIRST>(a);
}

DI void path_a(int wi) {
  const Ctx c = make_ctx();
  {
    const rsrc_t rx = mkrs(c.x, SQ * DM * 2);
    const unsigned lane = lane_id(), vox = 1280u * (lane >> 3) + 16u * ((lane & 7) ^ ((4 * (wi & 1) + (lane >> 4)) & 7));
    auto xp = [&](int s, unsigned stage, int n) { const int pc = n < 6 ? wi + 4 * n : 24 + (wi & 1); dma_df(c.ldsb + XOFF + stage + 1024 * pc, vox, rx, 128u * s + 10240u * pc); };
#pragma unroll
    for (int n = 0; n < 7; ++n) xp(0, 0, n);
#pragma unroll
    for (int n = 0; n < 7; ++n) xp(1, XST, n);
    unsigned xf = XST;
    qproj<7>(c, wi,
      [&](auto K, int s, int n) { if constexpr (decltype(K)::value == 0) xp(s + 2, xf, n); },
      [&](int s) { if (s == 9) VMW(0); else VMW(7); xf = xf == 2 * XST ? 0 : xf + XST; });
  }
  BAR();
  AS a;
  unsigned kb0, vb0;
  { const unsigned lane = lane_id(), r16 = lane & 15, g = lane >> 4, q4 = (lane & 15) >> 2, p = lane & 3;
    kb0 = c.ldsb + 128 * r16 + 16 * (g ^ ((r16 >> 1) & 7));
    vb0 = c.ldsb + SLOT + 2048 * (g >> 1) + 64 * (4 * (g & 1) + q4) + 16 * ((p >> 1) ^ (2 * (g & 1))) + 8 * (p & 1); }
  unsigned sl = 0;
  a.ones = bf16x8{0x3f80, 0x3f80, 0x3f80, 0x3f80, 0x3f80, 0x3f80, 0x3f80, 0x3f80}; PIN(a.ones);
#pragma nounroll
  for (int hd = 0; hd < 2; ++hd) {
#pragma unroll
    for (int rt = 0; rt < 3; ++rt) {
      const int t = 3 * wi + rt;
      const unsigned blk = hd ? Q2OFF + 4096 * t : (t < 8 ? 4 * SLOT + 4096 * t : t < 11 ? MISC + 4096 * (t - 8) : SLOT + 4096 * (t - 11));
      const unsigned q0 = kb0 + blk, q1 = q0 ^ 64;
      a.q[rt][0] = rd128(q0, 0); a.q[rt][1] = rd128(q1, 0); a.q[rt][2] = rd128(q0, 2048); a.q[rt][3] = rd128(q1, 2048);
      a.P[0][rt] = u32x4{}; a.P[1][rt] = u32x4{};
    }
    a.kn[0] = kb0 + sl; a.kn[1] = a.kn[0] ^ 64;
    a_frag<8, 0>(a); a_frag<8, 1>(a); a_frag<8, 2>(a); a_frag<8, 3>(a);
    if (hd == 0) BAR();
    auto setp = [&]() {
      a.kp[0] = a.kn[0]; a.kp[1] = a.kn[1]; a.vp[0] = vb0 + sl; a.vp[1] = a.vp[0] ^ 32;
      sl = sl == 4 * SLOT ? 0 : sl + 2 * SLOT;
      a.kn[0] = kb0 + sl; a.kn[1] = a.kn[0] ^ 64;
      SBAR();
    };
    setp();
    a_period<true>(a);
#pragma nounroll
    for (int t = 1; t < 16; ++t) { setp(); a_period<false>(a); }
    a.vp[0] = vb0 + sl; a.vp[1] = a.vp[0] ^ 32;
    SBAR();
    a_frag<3, 0>(a); a_frag<3, 1>(a); a_frag<3, 2>(a); a_frag<3, 3>(a);
#pragma unroll
    for (int i = 0; i < 4; ++i) a.F[0][i] = a.F[1][i];
    a_block<0, false, true, true, 1>(a); a_block<1, true, false, true, -1>(a);
#pragma unroll
    for (int i = 0; i < 4; ++i) a.F[1][i] = a.F[0][i];
    a_block<3, true, false, false, -1>(a);
    if (hd) BAR();
    const Ctx e = make_ctx();
    const rsrc_t ro = mkrs(e.oq + (size_t)(2 * e.own + hd) * OBLK, OBLK);
    const unsigned ov = 16 * lane_id() + 6144 * wi;
#pragma unroll
    for (int rt = 0; rt < 3; ++rt) {
      float l = a.l[rt][0];
      if (__builtin_expect(__any(!(l + nanprobe(a.o[rt]) < L_LIMIT)), 0)) {
        float m;
        slow16(e.k + (size_t)hd * SK * DH, e.v + (size_t)hd * SK * DH, 0, 64, a.q[rt], a.o[rt], m, l);
        l = red_add(l);
      }
      const float il = __builtin_amdgcn_rcpf(l);
#pragma unroll
      for (int kk = 0; kk < 4; ++kk) {
        const f32x4 u = a.o[rt][2 * kk] * il, v = a.o[rt][2 * kk + 1] * il;
        u32x4 w = {cvtpk(u[0], u[1]), cvtpk(u[2], u[3]), cvtpk(v[0], v[1]), cvtpk(v[2], v[3])};
        __builtin_amdgcn_raw_buffer_store_b128(w, ro, ov + 2048 * rt + 1024 * (kk & 1) + (OBLK / 2) * (kk >> 1), 0, 16);
      }
      if (rt == 0 && hd) BAR();
    }
  }
}

DI void path_b(const int j, HS& h, unsigned long long pc) {
  const int j_ = j;
  const Ctx c = make_ctx();
  { const unsigned l = lane_id(), ph = l >> 2, i = l & 3;
    h.in = l < 12 ? 256 * c.gid + 8 * l : 0x80000000u;
    h.out = l < 12 ? (ph < 2 ? 256 * ((c.gid & ~3) | i) + 8 * (4 * ph + c.own) : 256 * ((c.gid & ~12) | (i << 2)) + 8 * (8 + c.kq)) : 0x80000000u;
    h.req = u32x2{}; h.ack = u32x2{}; h.base = c.hs;
  }
  const rsrc_t rk = mkrs(c.k, 2 * SK * DH * 2), rv = mkrs(c.v, 2 * SK * DH * 2);
  unsigned vk0, vk1, vv;
  { const unsigned i = lane_id();
    vk0 = 256 * (i >> 3) + 16 * ((i & 7) ^ (i >> 4)); vk1 = (vk0 ^ 64) + 2048;
    vv = 256 * ((i >> 2) & 7) + 64 * (i >> 5) + 16 * ((i & 3) ^ (((i >> 4) & 1) << 1)); }
  auto kpc = [&](int t, unsigned slot, int n) {
    const unsigned m = c.ldsb + slot + 2048 * j, so = (unsigned)(64 * t + 16 * j) * 256u;
    if (n == 0) dma_nt<0>(m, vk0, rk, so); if (n == 1) dma_nt<128>(m + 8064, vk0, rk, so); if (n == 2) dma_nt<0>(m + 1024, vk1, rk, so); if (n == 3) dma_nt<128>(m + 9088, vk1, rk, so);
  };
  auto vpc = [&](int t, unsigned slot, int n) {
    const unsigned m = c.ldsb + slot + 4096 * j, so = (unsigned)(64 * t - 32 + 16 * j) * 256u;
    if (n == 0) dma_nt<0>(m, vv, rv, so); if (n == 1) dma_nt<128>(m + 896, vv, rv, so); if (n == 2) dma_nt<2048>(m, vv, rv, so); if (n == 3) dma_nt<2176>(m + 896, vv, rv, so);
  };
  auto kdma = [&](int t, unsigned slot) { kpc(t, slot, 0); kpc(t, slot, 1); kpc(t, slot, 2); kpc(t, slot, 3); };
  auto vdma = [&](int t, unsigned slot) { vpc(t, slot, 0); vpc(t, slot, 1); vpc(t, slot, 2); vpc(t, slot, 3); };
  {
    const rsrc_t rw = mkrs(c.wq, (DM - 1) * HD * 2 + 512);
    const unsigned i = lane_id(), vw0 = 8192 * ((i >> 2) & 7) + 64 * (i >> 5) + 16 * (i & 3), vw1 = (vw0 ^ 32) + 65536;
    auto wpc = [&](int it, unsigned slot, int n) {
      dma_df(c.ldsb + slot + 4096 * j + 1024 * n, n < 2 ? vw0 : vw1, rw, (unsigned)(64 * (it >> 1) + 16 * j) * 8192u + 256 * (it & 1) + 128 * (n & 1));
    };
    if (j == 0) hs_peek(h, c.gid, c.ldsb + XOFF + 3 * XST + 512);
    if (j == 3) touch_code(c.ldsb + XOFF + 3 * XST, pc, c.idx);
#pragma nounroll
    for (int it = 0; it < 5; ++it) { wpc(it, it * SLOT, 0); wpc(it, it * SLOT, 1); wpc(it, it * SLOT, 2); wpc(it, it * SLOT, 3); }
    unsigned f0 = SLOT, f1 = 2 * SLOT;
    auto adv = [](unsigned s_) { s_ += 2 * SLOT; return s_ >= 5 * SLOT ? s_ - 5 * SLOT : s_; };
    qproj<6>(c, j,
      [&](auto K, int s, int n) {
        constexpr int k = decltype(K)::value;
        if constexpr (k == 0) { if (n < 4) wpc(2 * s + 3, f0, n); else wpc(2 * s + 4, f1, n - 4); }
        if constexpr (k == 1) {
          if (n == 4) touch80(c.ldsb + 2048 * j, make_ctx().warm + 10240 * j);
          if (n < 4) wpc(19, 4 * SLOT, n); else kpc(0, 0, n - 4); }
        if constexpr (k == 2) { if (n == 0) { if (j >= 2) vdma(0, SLOT); } else if (n >= 4) kpc(1, 2 * SLOT, n - 4); }
        if constexpr (k == 3) { if (n == 0 && j == 0) { const u64 r = j64(hs_peeked(c.ldsb + XOFF + 3 * XST + 512)) + HS_K; h.req = u32x2{(unsigned)r, (unsigned)(r >> 32)}; hs_store(h, h.in + 128, h.req); } }
        if constexpr (k == 4) vdma(1, 3 * SLOT);
      },
      [&](int s) { if (s == 0) VMW(12); else if (s == 9) VMW(6); else if (s < 9) VMW(4); f0 = adv(f0); f1 = adv(f1); });
  }
  if (j == 0) {
    h.base = make_ctx().hs;
    const bool idle = lane_id() >= 12;
#pragma nounroll
    for (int spin = 0; spin < SPIN_MAX; ++spin) {
      asm volatile("" ::: "memory");
      const u32x2 a0 = __builtin_amdgcn_raw_buffer_load_b64(mkrs(h.base, CNT_BYTES), h.out, 0, 16);
      h.ack = hs_load_now(h, h.out + 128);
      if (__all(idle || j64(h.ack) == j64(a0) + HS_K)) break;
      __builtin_amdgcn_s_sleep(1);
    }
  }
  VMW(8);
  BAR();
  unsigned qb, vb0;
  { const unsigned lane = lane_id(), r16 = lane & 15, g = lane >> 4, q4 = (lane & 15) >> 2, p = lane & 3;
    qb = c.ldsb + 128 * r16 + 16 * (g ^ ((r16 >> 1) & 7));
    vb0 = c.ldsb + SLOT + 8192 * ((j & 1) ^ 1) + 2048 * (g >> 1) + 64 * (4 * (g & 1) + q4) + 16 * ((p >> 1) ^ (2 * (g & 1))) + 8 * (p & 1); }
  bf16x8 q[4], vf[8]; f32x4 o[8], os[8], negm = {}; float l = 0.f, ms = 0.f, ls = 0.f; u32x4 P = {};
  unsigned sl = 0, fr = 4 * SLOT;
  auto rdq = [&](unsigned blk) { const unsigned q0 = qb + blk, q1 = q0 ^ 64; q[0] = rd128(q0, 0); q[1] = rd128(q1, 0); q[2] = rd128(q0, 2048); q[3] = rd128(q1, 2048); };
  auto rdv = [&]() {
    const unsigned v0 = vb0 + sl, v1 = v0 ^ 32;
#pragma unroll
    for (int cc = 0; cc < 8; ++cc) vf[cc] = cat(rdtr((cc & 1) ? v1 : v0, 512 * (cc >> 1)), rdtr((cc & 1) ? v1 : v0, 512 * (cc >> 1) + 4096));
  };
  auto put = [&](unsigned scr) {
    const int j = opq(j_);
    const unsigned lane = lane_id(), sc = c.ldsb + scr + 16 * lane + 8192 * j;
#pragma unroll
    for (int cc = 0; cc < 8; ++cc) wr128f(sc, 1024 * cc, os[cc]);
    if (lane < 16) { *(LAS float*)(c.ldsb + scr + 32768 + 128 * j + 4 * lane) = ms; *(LAS float*)(c.ldsb + scr + 32768 + 128 * j + 64 + 4 * lane) = ls; }
  };
  auto merge = [&](int hd, unsigned scr) {
    const int j = opq(j_);
    const unsigned lane = lane_id(), sc = c.ldsb + scr + 16 * lane + 2048 * j, ml = c.ldsb + scr + 32768 + 4 * (lane & 15);
    const Ctx e = make_ctx();
    float mw[4], lw[4]; f32x4 pa[4], pb[4];
#pragma unroll
    for (int w = 0; w < 4; ++w) { mw[w] = *(const LAS float*)(ml + 128 * w); lw[w] = *(const LAS float*)(ml + 128 * w + 64); pa[w] = rd128f(sc, 8192 * w); pb[w] = rd128f(sc, 8192 * w + 1024); }
    LGKM0(); SBAR();
    const float mt = fmaxf(fmaxf(mw[0], mw[1]), fmaxf(mw[2], mw[3]));
    float lt = 0.f;
#pragma unroll
    for (int w = 0; w < 4; ++w) { mw[w] = ex2(mw[w] - mt); PIN(mw[w]); lt = ffma_s(mw[w], lw[w], lt); }
    const float ir = __builtin_amdgcn_rcpf(lt);
    f32x4 u = {}, v = {};
#pragma unroll
    for (int w = 0; w < 4; ++w) {
      float f = mw[w] * ir; PIN(f);
#pragma unroll
      for (int i = 0; i < 4; ++i) { u[i] = ffma_s(pa[w][i], f, u[i]); v[i] = ffma_s(pb[w][i], f, v[i]); }
    }
    u32x4 w4 = {cvtpk(u[0], u[1]), cvtpk(u[2], u[3]), cvtpk(v[0], v[1]), cvtpk(v[2], v[3])};
    __builtin_amdgcn_raw_buffer_store_b128(w4, mkrs(e.oq + (size_t)(2 * e.own + hd) * OBLK, OBLK), 16 * lane + 12 * 2048 + 1024 * (j & 1) + (OBLK / 2) * (j >> 1), 0, 16);
  };
#pragma unroll
  for (int cc = 0; cc < 8; ++cc) { os[cc] = f32x4{}; vf[cc] = bf16x8{}; }
  int t = 0;
  unsigned long long ts = 0, dl = 0;
  unsigned pp = PACE;
  auto odma = [&](int st) {
    const OD d = od_make(make_ctx(), opq(j));
#pragma unroll
    for (int n = 0; n < 12; ++n) od_piece(c, d, st, st ? PS1 : PS0, n);
  };
  auto next = [&](int X) {
    const unsigned long long tw = __builtin_amdgcn_s_memrealtime();
    LGKM0();
    if (t < 29) { if (X) VMW(9); else VMW(8); }
    else if (j < 2) { if (t == 29) VMW(20); else if (t == 30) VMW(16); else VMW(0); }
    else { if (t == 29) VMW(8); else VMW(12); }
    if (j == 3 && t < PACE_END) {
      const bool probe = t > 1 && t < 15;
      if (probe) VMW(2);
      dl += pp;
      const unsigned long long lo = ts + pp - (PACE - PFAST), go = (long long)(dl - lo) > 0 ? dl : lo;
      unsigned long long now = __builtin_amdgcn_s_memrealtime();
      if (probe ? (long long)(now - ts) > (long long)(pp - 10) : t > 1 && (long long)(now - tw) > 20) { dl = (long long)(dl - now) > 0 ? dl : now; if (pp < PACE + 48) pp += 4; }
      else if (pp > PACE) --pp;
      while ((long long)(now - go) < 0) { __builtin_amdgcn_s_sleep(1); now = __builtin_amdgcn_s_memrealtime(); }
      if ((long long)(now - dl) > (long long)PCREDIT) dl = now - PCREDIT;
    }
    BAR();
    ts = __builtin_amdgcn_s_memrealtime();
    fr = sl; sl = sl == 4 * SLOT ? 0 : sl + 2 * SLOT; ++t;
    if (t < 30) kdma(t + 2, fr);
    if (t < 30 || (t == 30 && j < 2)) vdma(t + 2, fr + SLOT);
  };
  rdq(q1addr(12));
  __builtin_amdgcn_s_setprio(0);
  BAR();
  kdma(2, fr); vdma(2, fr + SLOT);
  if (j >= 2) { VMW(8); BAR(); ts = __builtin_amdgcn_s_memrealtime(); dl = ts; fr = sl; sl = 2 * SLOT; t = 1; kdma(3, fr); vdma(3, fr + SLOT); }
#pragma nounroll
  for (int hd = 0; hd < 2; ++hd) {
    if (hd) rdq(Q2OFF + 12 * 4096);
#pragma unroll
    for (int cc = 0; cc < 8; ++cc) o[cc] = f32x4{};
    negm = f32x4{}; l = 0.f;
#pragma unroll
    for (int cc = 0; cc < 8; ++cc) PIN(o[cc]);
    __builtin_amdgcn_s_setprio(0);
#pragma nounroll
    for (int u = 0; u < 8; ++u) {
      __builtin_amdgcn_s_setprio(0);
      if ((j & 1) == 0) rdv();
      {
        const unsigned k0 = qb + sl + 4096 * (j & 1), k1 = k0 ^ 64;
        f32x4 S[2]; bf16x8 kf[2][4];
#pragma unroll
        for (int h = 0; h < 2; ++h) { kf[h][0] = rd128(k0, 2048 * h); kf[h][1] = rd128(k1, 2048 * h); kf[h][2] = rd128(k0, 2048 * h + 8192); kf[h][3] = rd128(k1, 2048 * h + 8192); }
        LGKM0(); SBAR();
        __builtin_amdgcn_s_setprio(3);
#pragma unroll
        for (int h = 0; h < 2; ++h) { S[h] = MFMA16(kf[h][0], q[0], negm); S[h] = MFMA16(kf[h][1], q[1], S[h]); S[h] = MFMA16(kf[h][2], q[2], S[h]); S[h] = MFMA16(kf[h][3], q[3], S[h]); }
        SBAR();
        __builtin_amdgcn_s_setprio(0);
        SBAR();
        if (u == 0) {
          __builtin_amdgcn_s_setprio(3);
          const float mx = red_max(fmaxf(fmaxf(fmaxf(S[0][0], S[0][1]), fmaxf(S[0][2], S[0][3])), fmaxf(fmaxf(S[1][0], S[1][1]), fmaxf(S[1][2], S[1][3]))));
          negm = f32x4{-mx, -mx, -mx, -mx}; S[0] -= mx; S[1] -= mx;
        }
#pragma unroll
        for (int i = 0; i < 8; ++i) { S[i >> 2][i & 3] = ex2(S[i >> 2][i & 3]); l += S[i >> 2][i & 3]; PIN(l); }
        P = u32x4{cvtpk(S[0][0], S[0][1]), cvtpk(S[0][2], S[0][3]), cvtpk(S[1][0], S[1][1]), cvtpk(S[1][2], S[1][3])};
      }
      next(0);
      if (t == 32) odma(1);
      __builtin_amdgcn_s_setprio(0);
      if (j & 1) rdv();
      LGKM0(); SBAR();
      __builtin_amdgcn_s_setprio(3);
#pragma unroll
      for (int cc = 0; cc < 8; ++cc) o[cc] = MFMA16(vf[cc], __builtin_bit_cast(bf16x8, P), o[cc]);
      SBAR();
      __builtin_amdgcn_s_setprio(0);
      SBAR();
      if (u == 7) break;
      int X = 0;
      if (hd && u < 7) {
        __builtin_amdgcn_s_setprio(3);
        const int v = u + (j >> 1);
        if (v == 1) put(Q2OFF);
        if (v == 2) { merge(0, Q2OFF); X = 1; }
        if (j == 0) {
          if (u == 3) { h.base = make_ctx().hs; hs_signal(h, 0); X = 1; }
          if (u == 5) { h.base = make_ctx().hs; hs_wait(h, 0, hs_peeked(c.ldsb + Q2OFF + 1024)); }
          if (u == 4) { const Ctx e = make_ctx(); h.base = e.hs; hs_peek(h, e.gid, c.ldsb + Q2OFF + 1024); X = 1; }
        }
        if (j == 2 && u == 4) { touch_code(c.ldsb + Q2OFF, pc, make_ctx().idx); X = 1; }
        if (u == 6) odma(0);
        __builtin_amdgcn_s_setprio(0);
      }
      next(X);
    }
    __builtin_amdgcn_s_setprio(3);
    ls = red_add(l); ms = -negm[0];
    MFMA_DONE();
    if (__builtin_expect(__any(!(ls + nanprobe_s(o) < L_LIMIT)), 0)) {
      const Ctx e = make_ctx();
      slow16(e.k + (size_t)hd * SK * DH, e.v + (size_t)hd * SK * DH, 32 * opq(j), 16, q, o, ms, ls);
      ls = red_add(ls);
    }
#pragma unroll
    for (int cc = 0; cc < 8; ++cc) os[cc] = o[cc];
    if (t < 32) { next(0); if (t == 32) odma(1); }
  }
  put(SCR2);
  BAR();
  merge(1, SCR2);
  __builtin_amdgcn_s_setprio(0);
}

extern "C" __global__ __launch_bounds__(512, 2) void kk_fused(Args) {
  unsigned long long pc;
  asm volatile("s_getpc_b64 %0" : "=s"(pc));
  pc &= ~255ull;
  const int wid = __builtin_amdgcn_readfirstlane(threadIdx.x >> 6);
  HS h = {};
  if (wid < 4) path_a(wid); else path_b(wid - 4, h, pc);
  if (wid < 4) { if (wid & 1) oproj<3, 6>(wid, h); else oproj<3, 7>(wid, h); }
  else { if (wid & 1) oproj<2, 7>(wid - 4, h); else oproj<2, 6>(wid - 4, h); }
}

extern "C" __global__ void kk_code_end() {}
}

extern "C" void kernel_launch(void* const* d_in, const int* in_sizes, int n_in, void* d_out, int out_size, void* d_ws, size_t ws_size, hipStream_t stream) {
  using namespace kk;
  Args a;
  a.x0 = (const u16*)d_in[0]; a.k0 = (const u16*)d_in[1]; a.v0 = (const u16*)d_in[2]; a.wq0 = (const u16*)d_in[3]; a.wo0 = (const u16*)d_in[4];
  a.x1 = (const u16*)d_in[5]; a.k1 = (const u16*)d_in[6]; a.v1 = (const u16*)d_in[7]; a.wq1 = (const u16*)d_in[8]; a.wo1 = (const u16*)d_in[9];
  a.ws = (char*)d_ws; a.out = (u16*)d_out;
  (void)hipFuncSetAttribute((const void*)kk_fused, hipFuncAttributeMaxDynamicSharedMemorySize, (int)LDS_TOTAL);
  kk_fused<<<256, 512, LDS_TOTAL, stream>>>(a);
}
```
